```python
import torch

def _softmax(x, axis=-1):
    return torch.softmax(x, dim=axis)
def _gelu(x):
    return torch.nn.functional.gelu(x, approximate="tanh")
def _silu(x):
    return torch.nn.functional.silu(x)
def _sigmoid(x):
    return torch.sigmoid(x)
def _layernorm(x, axis=-1, eps=1e-5):
    m = x.mean(dim=axis, keepdim=True)
    v = x.var(dim=axis, keepdim=True, unbiased=False)
    return (x - m) / torch.sqrt(v + eps)
def _scatter_add(tgt, idx, src, axis=0):
    return tgt.clone().index_add_(axis, idx, src)
def _segment_sum(src, seg, n):
    out = torch.zeros((n,) + src.shape[1:], dtype=src.dtype)
    return out.index_add_(0, seg, src)
def _pad(x, pw):
    flat = tuple(v for pair in reversed(pw) for v in pair)
    return torch.nn.functional.pad(x, flat)

kernel_name = 'sequential_preLN__sliding_local_dilated__swiglu_ffn__prompt'

def setup_inputs():

    _w = (lambda a: (lambda t: t.to(torch.bfloat16) if t.is_floating_point() else t)(torch.as_tensor(a)))
    def _A_setup():
        B, S, D = 1, 2048, 448
        return {
            "x": _w((lambda *s: torch.randn(*s, dtype=torch.bfloat16) * 0.05)(B, S, D)),
            "wqkv": _w((lambda *s: torch.randn(*s, dtype=torch.bfloat16) * 0.05)(D, 3 * D)),
            "wo": _w((lambda *s: torch.randn(*s, dtype=torch.bfloat16) * 0.05)(D, D)),
        }
    _A_in = _A_setup()

    def _B_setup():
        B, S, D, F = 1, 2048, 448, 1792
        return {
            "x": _w((lambda *s: torch.randn(*s, dtype=torch.bfloat16) * 0.05)(B, S, D)),
            "w_gate": _w((lambda *s: torch.randn(*s, dtype=torch.bfloat16) * 0.05)(D, F)),
            "w_up": _w((lambda *s: torch.randn(*s, dtype=torch.bfloat16) * 0.05)(D, F)),
            "w_down": _w((lambda *s: torch.randn(*s, dtype=torch.bfloat16) * 0.05)(F, D)),
        }
    _B_in = _B_setup()

    return {
        "A_x": _A_in["x"],
        "A_wqkv": _A_in["wqkv"],
        "A_wo": _A_in["wo"],
        "B_x": _B_in["x"],
        "B_w_gate": _B_in["w_gate"],
        "B_w_up": _B_in["w_up"],
        "B_w_down": _B_in["w_down"],
    }

def reference(A_x, A_wqkv, A_wo, B_x, B_w_gate, B_w_up, B_w_down):
    def _A_ref(x, wqkv, wo):
        B, S, D = x.shape
        H = 16; Dh = D // H
        W, dil = 8, 2
        qkv = (lambda x, *d: x.permute(*d))((x @ wqkv).reshape(B, S, 3, H, Dh), 2, 0, 3, 1, 4)
        q, k, v = qkv[0], qkv[1], qkv[2]
        scale = Dh ** -0.5
        pos = torch.arange(S)
        off = torch.arange(W) * dil
        idx = pos[:, None] - off[None, :]
        mask = idx >= 0
        idx_c = torch.where(mask, idx, 0).reshape(-1)
        kg = k[:, :, idx_c, :].reshape(B, H, S, W, Dh)
        vg = v[:, :, idx_c, :].reshape(B, H, S, W, Dh)
        logits = torch.einsum("bhsd,bhswd->bhsw", q, kg) * scale
        logits = torch.where(mask[None, None, :, :], logits, -1e9)
        attn = _softmax(logits, axis=-1)
        o = torch.einsum("bhsw,bhswd->bhsd", attn, vg)
        return (lambda x, *d: x.permute(*d))(o, 0, 2, 1, 3).reshape(B, S, D) @ wo

    def _B_ref(x, w_gate, w_up, w_down):
        g = _silu(x @ w_gate)
        u = x @ w_up
        return (g * u) @ w_down

    x = A_x
    h = x + _A_ref(_layernorm(x, axis=-1), A_wqkv, A_wo)
    h2 = h + _B_ref(_layernorm(h, axis=-1), B_w_gate, B_w_up, B_w_down)
    return h2
```

```cpp
#include <hip/hip_runtime.h>
#include <hip/hip_bf16.h>
#include <stdint.h>

typedef unsigned short u16;
typedef __attribute__((ext_vector_type(8))) __bf16 bf16x8;
typedef __attribute__((ext_vector_type(4))) short s16x4;
typedef __attribute__((ext_vector_type(4))) float f32x4;
typedef __attribute__((ext_vector_type(4))) unsigned int u32x4;
typedef __attribute__((ext_vector_type(2))) unsigned int u32x2;
typedef __attribute__((ext_vector_type(2))) __bf16 bf16x2;
#define LDS_AS __attribute__((address_space(3)))

constexpr int S_ = 2048, D_ = 448, F_ = 1792, QKV_ = 1344;

__device__ __forceinline__ float bf_lo(unsigned u) { return __uint_as_float(u << 16); }
__device__ __forceinline__ float bf_hi(unsigned u) { return __uint_as_float(u & 0xffff0000u); }
__device__ __forceinline__ unsigned pack_bf(float a, float b) {
  bf16x2 v = {(__bf16)a, (__bf16)b};
  return __builtin_bit_cast(unsigned, v);
}
__device__ __forceinline__ float rbf(float a) { return (float)(__bf16)a; }
__device__ __forceinline__ float dot2(unsigned a, unsigned b, float c) {
  return __builtin_amdgcn_fdot2_f32_bf16(__builtin_bit_cast(bf16x2, a), __builtin_bit_cast(bf16x2, b), c, false);
}
__device__ __forceinline__ u32x2 tr_read(const char* p) {
  s16x4 v = __builtin_amdgcn_ds_read_tr16_b64_v4i16((LDS_AS s16x4*)p);
  return __builtin_bit_cast(u32x2, v);
}
__device__ __forceinline__ u32x4 wfrag(const char* p) {
  u32x2 a = tr_read(p), b = tr_read(p + 16 * 128);
  u32x4 r = {a[0], a[1], b[0], b[1]};
  return r;
}
__device__ __forceinline__ f32x4 mfma(u32x4 a, u32x4 b, f32x4 c) {
  return __builtin_amdgcn_mfma_f32_16x16x32_bf16(__builtin_bit_cast(bf16x8, a), __builtin_bit_cast(bf16x8, b), c, 0, 0, 0);
}
__device__ __forceinline__ int rho_r(int k) { return (k & ~31) | ((k & 4) << 2) | ((k & 24) >> 1) | (k & 3); }
__device__ __forceinline__ int strip_off(int r, int c) { return r * 128 + ((c ^ (((r >> 1) & 3) << 1)) << 4); }
__device__ __forceinline__ int tr_lane_off(int l, int nb) {
  int g = l >> 4, q = (l & 15) >> 2, p = l & 3;
  int swz = 2 * (g & 1) + (q >> 1);
  return (4 * g + q) * 128 + ((nb ^ swz) << 5) + p * 8;
}
__device__ __forceinline__ void ln_params(float sum, float sq, float& mb, float& rstd) {
  float mean = sum * (1.0f / D_);
  float var = fmaxf(sq * (1.0f / D_) - mean * mean, 0.0f);
  mb = rbf(mean);
  float den = rbf(sqrtf(rbf(rbf(var) + 1e-5f)));
  rstd = 1.0f / den;
}

constexpr int P1_X = 56 * 80 * 16;
constexpr int P1_WS = 224 * 128;
constexpr int P1_W = 3 * P1_WS;
constexpr int P1_SMEM = P1_X + P1_W + 8 * 16 * 8;
constexpr int P1_SP = 672;

__global__ __launch_bounds__(256, 1) void k_qkv_attn(const u16* __restrict__ x, const u16* __restrict__ wqkv,
                                                     u16* __restrict__ o_frag) {
  __shared__ __attribute__((aligned(16))) char smem[P1_SMEM];
  char* sX = smem;
  char* sW = smem + P1_X;
  float* sStat = (float*)(smem + P1_X + P1_W);

  const int tid = threadIdx.x, l = tid & 63, g = l >> 4, tl = l & 15;
  const int w = __builtin_amdgcn_readfirstlane(tid >> 6);
  const int bid = blockIdx.x, xcd = bid & 7, jj = bid >> 3;
  const int mt = xcd * 4 + (jj >> 3), hp = jj & 7;
  const int row0 = mt * 64 - 16;

  u32x4 xr[14], xe[4];
  {
    int row = max(row0 + w * 16 + tl, 0);
    const u32x4* p = (const u32x4*)(x + (size_t)row * D_ + g * 8);
#pragma unroll
    for (int s = 0; s < 14; ++s) xr[s] = p[s * 4];
    int row4 = row0 + 64 + tl;
    const u32x4* p4 = (const u32x4*)(x + (size_t)row4 * D_ + g * 8);
#pragma unroll
    for (int i = 0; i < 4; ++i) {
      int s = min(w + 4 * i, 13);
      xe[i] = p4[s * 4];
    }
  }
  u32x4 wr0[21], wr1[21];
  const int wc = tid & 7, wk = tid >> 3;
  {
    const u16* src = wqkv + (size_t)wk * QKV_ + hp * 56 + min(wc, 6) * 8;
#pragma unroll
    for (int m = 0; m < 3; ++m)
#pragma unroll
      for (int ii = 0; ii < 7; ++ii) wr0[m * 7 + ii] = *(const u32x4*)(src + (size_t)(32 * ii) * QKV_ + m * D_);
#pragma unroll
    for (int m = 0; m < 3; ++m)
#pragma unroll
      for (int ii = 0; ii < 7; ++ii) wr1[m * 7 + ii] = *(const u32x4*)(src + (size_t)(224 + 32 * ii) * QKV_ + m * D_);
  }
  const int wdst = strip_off(rho_r(wk), wc);

  {
    float sum = 0.f, sq = 0.f, sum4 = 0.f, sq4 = 0.f;
    const unsigned ones = 0x3F803F80u;
#pragma unroll
    for (int s = 0; s < 14; ++s) {
      *(u32x4*)(sX + ((4 * s + g) * 80 + w * 16 + tl) * 16) = xr[s];
#pragma unroll
      for (int e = 0; e < 4; ++e) { sum = dot2(xr[s][e], ones, sum); sq = dot2(xr[s][e], xr[s][e], sq); }
    }
#pragma unroll
    for (int i = 0; i < 4; ++i) {
      int s = w + 4 * i;
      if (s < 14) {
        *(u32x4*)(sX + ((4 * s + g) * 80 + 64 + tl) * 16) = xe[i];
#pragma unroll
        for (int e = 0; e < 4; ++e) { sum4 = dot2(xe[i][e], ones, sum4); sq4 = dot2(xe[i][e], xe[i][e], sq4); }
      }
    }
    sum += __shfl_xor(sum, 16); sq += __shfl_xor(sq, 16); sum4 += __shfl_xor(sum4, 16); sq4 += __shfl_xor(sq4, 16);
    sum += __shfl_xor(sum, 32); sq += __shfl_xor(sq, 32); sum4 += __shfl_xor(sum4, 32); sq4 += __shfl_xor(sq4, 32);
    if (l < 16) {
      sStat[(w * 16 + tl) * 2] = sum; sStat[(w * 16 + tl) * 2 + 1] = sq;
      sStat[((4 + w) * 16 + tl) * 2] = sum4; sStat[((4 + w) * 16 + tl) * 2 + 1] = sq4;
    }
  }
#pragma unroll
  for (int m = 0; m < 3; ++m)
#pragma unroll
    for (int ii = 0; ii < 7; ++ii) *(u32x4*)(sW + m * P1_WS + ii * 32 * 128 + wdst) = wr0[m * 7 + ii];
  __syncthreads();

  f32x4 acc[3][5];
#pragma unroll
  for (int m = 0; m < 3; ++m)
#pragma unroll
    for (int t = 0; t < 5; ++t) acc[m][t] = (f32x4){0.f, 0.f, 0.f, 0.f};
  float cs[3] = {0.f, 0.f, 0.f};
  const int troff = tr_lane_off(l, w);
  const int xoff = (g * 80 + tl) * 16;

#pragma unroll
  for (int half = 0; half < 2; ++half) {
#pragma unroll
    for (int s = 0; s < 7; ++s) {
      u32x4 xf[5], wf[3];
#pragma unroll
      for (int t = 0; t < 5; ++t) xf[t] = *(const u32x4*)(sX + xoff + ((half * 7 + s) * 4 * 80 + t * 16) * 16);
#pragma unroll
      for (int m = 0; m < 3; ++m) wf[m] = wfrag(sW + m * P1_WS + s * 32 * 128 + troff);
#pragma unroll
      for (int m = 0; m < 3; ++m) {
#pragma unroll
        for (int t = 0; t < 5; ++t)
          if (!(m == 0 && t == 0)) acc[m][t] = mfma(wf[m], xf[t], acc[m][t]);
#pragma unroll
        for (int e = 0; e < 4; ++e) cs[m] = dot2(wf[m][e], 0x3F803F80u, cs[m]);
      }
    }
    __syncthreads();
    if (half == 0) {
#pragma unroll
      for (int m = 0; m < 3; ++m)
#pragma unroll
        for (int ii = 0; ii < 7; ++ii) *(u32x4*)(sW + m * P1_WS + ii * 32 * 128 + wdst) = wr1[m * 7 + ii];
      __syncthreads();
    }
  }

  char* sS = sW;
  {
    float csf[3][4];
#pragma unroll
    for (int m = 0; m < 3; ++m) {
      float c = cs[m];
      c += __shfl_xor(c, 16); c += __shfl_xor(c, 32);
#pragma unroll
      for (int r = 0; r < 4; ++r) csf[m][r] = __shfl(c, 4 * g + r);
    }
    const int quad = 4 * w + g;
#pragma unroll
    for (int t = 0; t < 5; ++t) {
      float sum, sq;
      if (t < 4) { sum = sStat[(t * 16 + tl) * 2]; sq = sStat[(t * 16 + tl) * 2 + 1]; }
      else {
        sum = 0.f; sq = 0.f;
#pragma unroll
        for (int i = 0; i < 4; ++i) { sum += sStat[((4 + i) * 16 + tl) * 2]; sq += sStat[((4 + i) * 16 + tl) * 2 + 1]; }
      }
      float mb, rstd;
      ln_params(sum, sq, mb, rstd);
#pragma unroll
      for (int m = 0; m < 3; ++m) {
        if (m == 0 && t == 0) continue;
        float v0 = (acc[m][t][0] - mb * csf[m][0]) * rstd;
        float v1 = (acc[m][t][1] - mb * csf[m][1]) * rstd;
        float v2 = (acc[m][t][2] - mb * csf[m][2]) * rstd;
        float v3 = (acc[m][t][3] - mb * csf[m][3]) * rstd;
        u32x2 pk = {pack_bf(v0, v1), pack_bf(v2, v3)};
        if (quad < 14) *(u32x2*)(sS + (m * 14 + quad) * P1_SP + (t * 16 + tl) * 8) = pk;
      }
    }
  }
  __syncthreads();

  {
    const int hh = g >> 1, part = g & 1;
    const int t = w * 16 + tl;
    const int tok = mt * 64 + t;
    const int q0 = hh * 7 + part * 4;
    const int nq = part ? 3 : 4;
    u32x2 qv[4];
#pragma unroll
    for (int i = 0; i < 4; ++i) {
      int qi = q0 + min(i, nq - 1);
      qv[i] = *(const u32x2*)(sS + (0 * 14 + qi) * P1_SP + (16 + t) * 8);
      if (i >= nq) qv[i] = (u32x2){0u, 0u};
    }
    float lg[8];
#pragma unroll
    for (int ww = 0; ww < 8; ++ww) {
      float sacc = 0.f;
#pragma unroll
      for (int i = 0; i < 4; ++i) {
        int qi = q0 + min(i, nq - 1);
        u32x2 kv = *(const u32x2*)(sS + (1 * 14 + qi) * P1_SP + (16 + t - 2 * ww) * 8);
        sacc = dot2(qv[i][0], kv[0], sacc);
        sacc = dot2(qv[i][1], kv[1], sacc);
      }
      lg[ww] = sacc;
    }
    const float scale = 0.1889822365046136f;
    float mx = -3.0e38f;
#pragma unroll
    for (int ww = 0; ww < 8; ++ww) {
      float sacc = lg[ww] + __shfl_xor(lg[ww], 16);
      float v = rbf(rbf(sacc) * scale);
      v = (tok - 2 * ww >= 0) ? v : -1e9f;
      lg[ww] = v;
      mx = fmaxf(mx, v);
    }
    float den = 0.f;
#pragma unroll
    for (int ww = 0; ww < 8; ++ww) { lg[ww] = __expf(lg[ww] - mx); den += lg[ww]; }
    float inv = 1.0f / den;
#pragma unroll
    for (int ww = 0; ww < 8; ++ww) lg[ww] = rbf(lg[ww] * inv);
    float oacc[4][4];
#pragma unroll
    for (int i = 0; i < 4; ++i)
#pragma unroll
      for (int e = 0; e < 4; ++e) oacc[i][e] = 0.f;
#pragma unroll
    for (int ww = 0; ww < 8; ++ww) {
#pragma unroll
      for (int i = 0; i < 4; ++i) {
        int qi = q0 + min(i, nq - 1);
        u32x2 vv = *(const u32x2*)(sS + (2 * 14 + qi) * P1_SP + (16 + t - 2 * ww) * 8);
        oacc[i][0] = fmaf(lg[ww], bf_lo(vv[0]), oacc[i][0]);
        oacc[i][1] = fmaf(lg[ww], bf_hi(vv[0]), oacc[i][1]);
        oacc[i][2] = fmaf(lg[ww], bf_lo(vv[1]), oacc[i][2]);
        oacc[i][3] = fmaf(lg[ww], bf_hi(vv[1]), oacc[i][3]);
      }
    }
    const int tbg = tok >> 4;
#pragma unroll
    for (int i = 0; i < 4; ++i) {
      if (i < nq) {
        int f = hp * 56 + (q0 + i) * 4;
        int s = f >> 5, ii = (f >> 4) & 1, gg = (f >> 2) & 3;
        u32x2 pk = {pack_bf(oacc[i][0], oacc[i][1]), pack_bf(oacc[i][2], oacc[i][3])};
        *(u32x2*)((char*)o_frag + ((size_t)((tbg * 14 + s) * 64 + gg * 16 + tl)) * 16 + ii * 8) = pk;
      }
    }
  }
}

__global__ __launch_bounds__(256, 1) void k_wo(const u16* __restrict__ o_frag, const u16* __restrict__ wo,
                                               const u16* __restrict__ x, u16* __restrict__ h) {
  __shared__ __attribute__((aligned(16))) char sW[448 * 128];
  const int tid = threadIdx.x, l = tid & 63, g = l >> 4, tl = l & 15;
  const int w = __builtin_amdgcn_readfirstlane(tid >> 6);
  const int bid = blockIdx.x, xcd = bid & 7, jj = bid >> 3;
  const int mt = xcd * 4 + jj / 7, nt = jj % 7;
  const int wt = w >> 1, wf = w & 1;

  u32x4 wr[14];
  const int wc = tid & 7, wk = tid >> 3;
  {
    const u16* src = wo + (size_t)wk * D_ + nt * 64 + wc * 8;
#pragma unroll
    for (int ii = 0; ii < 14; ++ii) wr[ii] = *(const u32x4*)(src + (size_t)(32 * ii) * D_);
  }
  u32x4 xf[2][14];
#pragma unroll
  for (int t = 0; t < 2; ++t) {
    const u32x4* p = (const u32x4*)o_frag + (size_t)((mt * 4 + wt * 2 + t) * 14) * 64 + l;
#pragma unroll
    for (int s = 0; s < 14; ++s) xf[t][s] = p[s * 64];
  }
  u32x2 res[2][2];
#pragma unroll
  for (int t = 0; t < 2; ++t)
#pragma unroll
    for (int f = 0; f < 2; ++f)
      res[t][f] = *(const u32x2*)(x + (size_t)(mt * 64 + (wt * 2 + t) * 16 + tl) * D_ + nt * 64 + (wf * 2 + f) * 16 + 4 * g);

  const int wdst = strip_off(wk, wc);
#pragma unroll
  for (int ii = 0; ii < 14; ++ii) *(u32x4*)(sW + ii * 32 * 128 + wdst) = wr[ii];
  __syncthreads();

  f32x4 acc[2][2];
#pragma unroll
  for (int t = 0; t < 2; ++t)
#pragma unroll
    for (int f = 0; f < 2; ++f) acc[t][f] = (f32x4){0.f, 0.f, 0.f, 0.f};
  const int tro0 = tr_lane_off(l, wf * 2), tro1 = tr_lane_off(l, wf * 2 + 1);
#pragma unroll
  for (int s = 0; s < 14; ++s) {
    u32x4 w0 = wfrag(sW + s * 32 * 128 + tro0), w1 = wfrag(sW + s * 32 * 128 + tro1);
#pragma unroll
    for (int t = 0; t < 2; ++t) {
      acc[t][0] = mfma(w0, xf[t][s], acc[t][0]);
      acc[t][1] = mfma(w1, xf[t][s], acc[t][1]);
    }
  }
#pragma unroll
  for (int t = 0; t < 2; ++t)
#pragma unroll
    for (int f = 0; f < 2; ++f) {
      float v0 = bf_lo(res[t][f][0]) + rbf(acc[t][f][0]);
      float v1 = bf_hi(res[t][f][0]) + rbf(acc[t][f][1]);
      float v2 = bf_lo(res[t][f][1]) + rbf(acc[t][f][2]);
      float v3 = bf_hi(res[t][f][1]) + rbf(acc[t][f][3]);
      u32x2 pk = {pack_bf(v0, v1), pack_bf(v2, v3)};
      *(u32x2*)(h + (size_t)(mt * 64 + (wt * 2 + t) * 16 + tl) * D_ + nt * 64 + (wf * 2 + f) * 16 + 4 * g) = pk;
    }
}

constexpr int P3_WS = 448 * 128;
__global__ __launch_bounds__(512, 2) void k_gateup(const u16* __restrict__ h, const u16* __restrict__ wg,
                                                   const u16* __restrict__ wu, u16* __restrict__ a_frag) {
  __shared__ __attribute__((aligned(16))) char smem[2 * P3_WS + 8 * 16 * 4];
  char* sW = smem;
  float* sCs = (float*)(smem + 2 * P3_WS);
  const int tid = threadIdx.x, l = tid & 63, g = l >> 4, tl = l & 15;
  const int w = __builtin_amdgcn_readfirstlane(tid >> 6);
  const int bid = blockIdx.x, xcd = bid & 7, jj = bid >> 3;
  const int mt = (xcd & 1) * 4 + jj / 7, nt = (xcd >> 1) * 7 + jj % 7;

  u32x4 xf[2][14];
#pragma unroll
  for (int t = 0; t < 2; ++t) {
    const u32x4* p = (const u32x4*)(h + (size_t)(mt * 256 + (w * 2 + t) * 16 + tl) * D_ + g * 8);
#pragma unroll
    for (int s = 0; s < 14; ++s) xf[t][s] = p[s * 4];
  }
  u32x4 wr[14];
  const int wc = tid & 7, wk = tid >> 3;
  {
    const u16* sg = wg + (size_t)wk * F_ + nt * 64 + wc * 8;
    const u16* su = wu + (size_t)wk * F_ + nt * 64 + wc * 8;
#pragma unroll
    for (int ii = 0; ii < 7; ++ii) {
      wr[ii] = *(const u32x4*)(sg + (size_t)(64 * ii) * F_);
      wr[7 + ii] = *(const u32x4*)(su + (size_t)(64 * ii) * F_);
    }
  }
  const int wdst = strip_off(rho_r(wk), wc);
#pragma unroll
  for (int ii = 0; ii < 7; ++ii) {
    *(u32x4*)(sW + ii * 64 * 128 + wdst) = wr[ii];
    *(u32x4*)(sW + P3_WS + ii * 64 * 128 + wdst) = wr[7 + ii];
  }
  float mb[2], rstd[2];
#pragma unroll
  for (int t = 0; t < 2; ++t) {
    float sum = 0.f, sq = 0.f;
#pragma unroll
    for (int s = 0; s < 14; ++s)
#pragma unroll
      for (int e = 0; e < 4; ++e) { sum = dot2(xf[t][s][e], 0x3F803F80u, sum); sq = dot2(xf[t][s][e], xf[t][s][e], sq); }
    sum += __shfl_xor(sum, 16); sq += __shfl_xor(sq, 16);
    sum += __shfl_xor(sum, 32); sq += __shfl_xor(sq, 32);
    ln_params(sum, sq, mb[t], rstd[t]);
  }
  __syncthreads();
  {
    const char* base = sW + (w >> 2) * P3_WS + tr_lane_off(l, w & 3);
    float c = 0.f;
#pragma unroll
    for (int s = 0; s < 14; ++s) {
      u32x4 wf = wfrag(base + s * 32 * 128);
#pragma unroll
      for (int e = 0; e < 4; ++e) c = dot2(wf[e], 0x3F803F80u, c);
    }
    c += __shfl_xor(c, 16); c += __shfl_xor(c, 32);
    if (l < 16) sCs[w * 16 + tl] = c;
  }

  f32x4 acc[2][8];
#pragma unroll
  for (int t = 0; t < 2; ++t)
#pragma unroll
    for (int f = 0; f < 8; ++f) acc[t][f] = (f32x4){0.f, 0.f, 0.f, 0.f};
  int tro[4];
#pragma unroll
  for (int f = 0; f < 4; ++f) tro[f] = tr_lane_off(l, f);
#pragma unroll
  for (int s = 0; s < 14; ++s) {
#pragma unroll
    for (int f = 0; f < 8; ++f) {
      u32x4 wf = wfrag(sW + (f >> 2) * P3_WS + s * 32 * 128 + tro[f & 3]);
      acc[0][f] = mfma(wf, xf[0][s], acc[0][f]);
      acc[1][f] = mfma(wf, xf[1][s], acc[1][f]);
    }
  }
  __syncthreads();
#pragma unroll
  for (int fp = 0; fp < 2; ++fp) {
    f32x4 cg[2], cu[2];
#pragma unroll
    for (int i = 0; i < 2; ++i) {
      cg[i] = *(const f32x4*)(sCs + (fp * 2 + i) * 16 + 4 * g);
      cu[i] = *(const f32x4*)(sCs + (4 + fp * 2 + i) * 16 + 4 * g);
    }
#pragma unroll
    for (int t = 0; t < 2; ++t) {
      u32x4 out;
#pragma unroll
      for (int i = 0; i < 2; ++i) {
        float av[4];
#pragma unroll
        for (int r = 0; r < 4; ++r) {
          float gv = rbf((acc[t][fp * 2 + i][r] - mb[t] * cg[i][r]) * rstd[t]);
          float uv = rbf((acc[t][4 + fp * 2 + i][r] - mb[t] * cu[i][r]) * rstd[t]);
          float sl = rbf(gv / (1.0f + __expf(-gv)));
          av[r] = sl * uv;
        }
        out[i * 2] = pack_bf(av[0], av[1]);
        out[i * 2 + 1] = pack_bf(av[2], av[3]);
      }
      const int tbg = mt * 16 + w * 2 + t;
      *((u32x4*)a_frag + (size_t)(tbg * 56 + nt * 2 + fp) * 64 + l) = out;
    }
  }
}

constexpr int P4_SS = 8;
constexpr int P4_BUF = P4_SS * 32 * 128;
__global__ __launch_bounds__(256, 1) void k_down(const u16* __restrict__ a_frag, const u16* __restrict__ wd,
                                                 const u16* __restrict__ h, u16* __restrict__ out) {
  __shared__ __attribute__((aligned(16))) char sW[2 * P4_BUF];
  const int tid = threadIdx.x, l = tid & 63, g = l >> 4, tl = l & 15;
  const int w = __builtin_amdgcn_readfirstlane(tid >> 6);
  const int bid = blockIdx.x, xcd = bid & 7, jj = bid >> 3;
  const int mt = xcd * 4 + jj / 7, nt = jj % 7;
  const int wt = w >> 1, wf = w & 1;
  const int wc = tid & 7, wk = tid >> 3;
  const u16* wsrc = wd + (size_t)wk * D_ + nt * 64 + wc * 8;
  const u32x4* xp0 = (const u32x4*)a_frag + (size_t)((mt * 4 + wt * 2) * 56) * 64 + l;
  const u32x4* xp1 = xp0 + 56 * 64;
  const int wdst = strip_off(wk, wc);
  const int tro0 = tr_lane_off(l, wf * 2), tro1 = tr_lane_off(l, wf * 2 + 1);

  u32x4 wr[P4_SS];
  u32x4 xa[2][P4_SS], xb[2][P4_SS];
#pragma unroll
  for (int ii = 0; ii < P4_SS; ++ii) wr[ii] = *(const u32x4*)(wsrc + (size_t)(32 * ii) * D_);
#pragma unroll
  for (int s = 0; s < P4_SS; ++s) { xa[0][s] = xp0[s * 64]; xa[1][s] = xp1[s * 64]; }
  u32x2 res[2][2];
#pragma unroll
  for (int t = 0; t < 2; ++t)
#pragma unroll
    for (int f = 0; f < 2; ++f)
      res[t][f] = *(const u32x2*)(h + (size_t)(mt * 64 + (wt * 2 + t) * 16 + tl) * D_ + nt * 64 + (wf * 2 + f) * 16 + 4 * g);

  f32x4 acc[2][2];
#pragma unroll
  for (int t = 0; t < 2; ++t)
#pragma unroll
    for (int f = 0; f < 2; ++f) acc[t][f] = (f32x4){0.f, 0.f, 0.f, 0.f};

#pragma unroll
  for (int ss = 0; ss < 7; ++ss) {
    char* buf = sW + (ss & 1) * P4_BUF;
#pragma unroll
    for (int ii = 0; ii < P4_SS; ++ii) *(u32x4*)(buf + ii * 32 * 128 + wdst) = wr[ii];
    if (ss < 6) {
#pragma unroll
      for (int ii = 0; ii < P4_SS; ++ii) wr[ii] = *(const u32x4*)(wsrc + (size_t)(32 * ((ss + 1) * P4_SS + ii)) * D_);
#pragma unroll
      for (int s = 0; s < P4_SS; ++s) {
        if (ss & 1) { xa[0][s] = xp0[((ss + 1) * P4_SS + s) * 64]; xa[1][s] = xp1[((ss + 1) * P4_SS + s) * 64]; }
        else        { xb[0][s] = xp0[((ss + 1) * P4_SS + s) * 64]; xb[1][s] = xp1[((ss + 1) * P4_SS + s) * 64]; }
      }
    }
    __syncthreads();
#pragma unroll
    for (int s = 0; s < P4_SS; ++s) {
      u32x4 w0 = wfrag(buf + s * 32 * 128 + tro0), w1 = wfrag(buf + s * 32 * 128 + tro1);
#pragma unroll
      for (int t = 0; t < 2; ++t) {
        u32x4 xv = (ss & 1) ? xb[t][s] : xa[t][s];
        acc[t][0] = mfma(w0, xv, acc[t][0]);
        acc[t][1] = mfma(w1, xv, acc[t][1]);
      }
    }
  }
#pragma unroll
  for (int t = 0; t < 2; ++t)
#pragma unroll
    for (int f = 0; f < 2; ++f) {
      float v0 = bf_lo(res[t][f][0]) + rbf(acc[t][f][0]);
      float v1 = bf_hi(res[t][f][0]) + rbf(acc[t][f][1]);
      float v2 = bf_lo(res[t][f][1]) + rbf(acc[t][f][2]);
      float v3 = bf_hi(res[t][f][1]) + rbf(acc[t][f][3]);
      u32x2 pk = {pack_bf(v0, v1), pack_bf(v2, v3)};
      *(u32x2*)(out + (size_t)(mt * 64 + (wt * 2 + t) * 16 + tl) * D_ + nt * 64 + (wf * 2 + f) * 16 + 4 * g) = pk;
    }
}

extern "C" void kernel_launch(void* const* d_in, const int* in_sizes, int n_in, void* d_out, int out_size,
                              void* d_ws, size_t ws_size, hipStream_t stream) {
  const u16* x = (const u16*)d_in[0];
  const u16* wqkv = (const u16*)d_in[1];
  const u16* wo = (const u16*)d_in[2];
  const u16* wg = (const u16*)d_in[4];
  const u16* wu = (const u16*)d_in[5];
  const u16* wd = (const u16*)d_in[6];
  u16* out = (u16*)d_out;
  char* ws = (char*)d_ws;
  u16* o_frag = (u16*)ws;
  u16* h = (u16*)(ws + (2u << 20));
  u16* a_frag = (u16*)(ws + (4u << 20));
  k_qkv_attn<<<256, 256, 0, stream>>>(x, wqkv, o_frag);
  k_wo<<<224, 256, 0, stream>>>(o_frag, wo, x, h);
  k_gateup<<<224, 512, 0, stream>>>(h, wg, wu, a_frag);
  k_down<<<224, 256, 0, stream>>>(a_frag, wd, h, out);
}
```

```cpp
#include <hip/hip_runtime.h>
#include <hip/hip_bf16.h>
#include <stdint.h>

typedef unsigned short u16;
typedef __attribute__((ext_vector_type(8))) __bf16 bf16x8;
typedef __attribute__((ext_vector_type(4))) short s16x4;
typedef __attribute__((ext_vector_type(4))) float f32x4;
typedef __attribute__((ext_vector_type(4))) unsigned int u32x4;
typedef __attribute__((ext_vector_type(2))) unsigned int u32x2;
typedef __attribute__((ext_vector_type(2))) __bf16 bf16x2;
#define LDS_AS __attribute__((address_space(3)))

#define STAMP(K,I)

constexpr int S_ = 2048, D_ = 448, F_ = 1792, QKV_ = 1344;

__device__ __forceinline__ float bf_lo(unsigned u) { return __uint_as_float(u << 16); }
__device__ __forceinline__ float bf_hi(unsigned u) { return __uint_as_float(u & 0xffff0000u); }
__device__ __forceinline__ unsigned pack_bf(float a, float b) {
  bf16x2 v = {(__bf16)a, (__bf16)b};
  return __builtin_bit_cast(unsigned, v);
}
__device__ __forceinline__ float rbf(float a) { return (float)(__bf16)a; }
__device__ __forceinline__ float dot2(unsigned a, unsigned b, float c) {
  return __builtin_amdgcn_fdot2_f32_bf16(__builtin_bit_cast(bf16x2, a), __builtin_bit_cast(bf16x2, b), c, false);
}
__device__ __forceinline__ u32x2 tr_read(const char* p) {
  s16x4 v = __builtin_amdgcn_ds_read_tr16_b64_v4i16((LDS_AS s16x4*)p);
  return __builtin_bit_cast(u32x2, v);
}
__device__ __forceinline__ u32x4 wfrag(const char* p) {
  u32x2 a = tr_read(p), b = tr_read(p + 16 * 128);
  u32x4 r = {a[0], a[1], b[0], b[1]};
  return r;
}
__device__ __forceinline__ f32x4 mfma(u32x4 a, u32x4 b, f32x4 c) {
  return __builtin_amdgcn_mfma_f32_16x16x32_bf16(__builtin_bit_cast(bf16x8, a), __builtin_bit_cast(bf16x8, b), c, 0, 0, 0);
}
__device__ __forceinline__ int rho_r(int k) { return (k & ~31) | ((k & 4) << 2) | ((k & 24) >> 1) | (k & 3); }
__device__ __forceinline__ int strip_off(int r, int c) { return r * 128 + ((c ^ (((r >> 1) & 3) << 1)) << 4); }
__device__ __forceinline__ int tr_lane_off(int l, int nb) {
  int g = l >> 4, q = (l & 15) >> 2, p = l & 3;
  int swz = 2 * (g & 1) + (q >> 1);
  return (4 * g + q) * 128 + ((nb ^ swz) << 5) + p * 8;
}
__device__ __forceinline__ void ln_params(float sum, float sq, float& mb, float& rstd) {
  float mean = sum * (1.0f / D_);
  float var = fmaxf(sq * (1.0f / D_) - mean * mean, 0.0f);
  mb = rbf(mean);
  float den = rbf(sqrtf(rbf(rbf(var) + 1e-5f)));
  rstd = 1.0f / den;
}
__device__ __forceinline__ u32x4 add_bf8(u32x4 a, u32x4 b) {
  u32x4 r;
#pragma unroll
  for (int e = 0; e < 4; ++e) r[e] = pack_bf(bf_lo(a[e]) + bf_lo(b[e]), bf_hi(a[e]) + bf_hi(b[e]));
  return r;
}

constexpr int P1_XP = 81;
constexpr int P1_X = 56 * P1_XP * 16;
constexpr int P1_WG = 64 * 128;
constexpr int P1_SLOT = 3 * P1_WG;
constexpr int P1_SP = 672;
constexpr int P1_SMEM = P1_X + 2 * P1_SLOT + 8 * 16 * 8;

__global__ __launch_bounds__(256, 1) void k_qkv_attn(const u16* __restrict__ x, const u16* __restrict__ wqkv,
                                                     u16* __restrict__ o_frag) {
  __shared__ __attribute__((aligned(16))) char smem[P1_SMEM];
  char* sX = smem;
  char* sW = smem + P1_X;
  float* sStat = (float*)(smem + P1_X + 2 * P1_SLOT);

  const int tid = threadIdx.x, l = tid & 63, g = l >> 4, tl = l & 15;
  const int w = __builtin_amdgcn_readfirstlane(tid >> 6);
  const int bid = blockIdx.x, xcd = bid & 7, jj = bid >> 3;
  const int mt = xcd * 4 + (jj >> 3), hp = jj & 7;
  const int row0 = mt * 64 - 16;
  STAMP(0, 0);

  u32x4 xr[18];
#pragma unroll
  for (int i = 0; i < 18; ++i) {
    int idx = min(tid + 256 * i, 80 * 56 - 1);
    int r = idx / 56, c = idx - r * 56;
    int row = max(row0 + r, 0);
    xr[i] = *(const u32x4*)(x + (size_t)row * D_ + c * 8);
  }
  const int wc = tid & 7, wk = tid >> 3;
  const u16* wsrc = wqkv + (size_t)wk * QKV_ + hp * 56 + min(wc, 6) * 8;
  u32x4 wr[2][6];
#pragma unroll
  for (int gi = 0; gi < 2; ++gi)
#pragma unroll
    for (int m = 0; m < 3; ++m)
#pragma unroll
      for (int hf = 0; hf < 2; ++hf) wr[gi][m * 2 + hf] = *(const u32x4*)(wsrc + (size_t)(64 * gi + 32 * hf) * QKV_ + m * D_);
  const int wdst = strip_off(rho_r(wk), wc);

#pragma unroll
  for (int i = 0; i < 18; ++i) {
    int idx = tid + 256 * i;
    int r = idx / 56, c = idx - r * 56;
    if (idx < 80 * 56) *(u32x4*)(sX + (c * P1_XP + r) * 16) = xr[i];
  }
#pragma unroll
  for (int m = 0; m < 3; ++m)
#pragma unroll
    for (int hf = 0; hf < 2; ++hf) *(u32x4*)(sW + m * P1_WG + hf * 32 * 128 + wdst) = wr[0][m * 2 + hf];
  __syncthreads();
  STAMP(0, 1);

  const int xoff = (g * P1_XP + tl) * 16;
  {
    float sum = 0.f, sq = 0.f, sum4 = 0.f, sq4 = 0.f;
    const unsigned ones = 0x3F803F80u;
#pragma unroll
    for (int s = 0; s < 14; ++s) {
      u32x4 v = *(const u32x4*)(sX + xoff + (s * 4 * P1_XP + w * 16) * 16);
#pragma unroll
      for (int e = 0; e < 4; ++e) { sum = dot2(v[e], ones, sum); sq = dot2(v[e], v[e], sq); }
    }
#pragma unroll
    for (int i = 0; i < 4; ++i) {
      int s = w + 4 * i;
      if (s < 14) {
        u32x4 v = *(const u32x4*)(sX + xoff + (s * 4 * P1_XP + 64) * 16);
#pragma unroll
        for (int e = 0; e < 4; ++e) { sum4 = dot2(v[e], ones, sum4); sq4 = dot2(v[e], v[e], sq4); }
      }
    }
    sum += __shfl_xor(sum, 16); sq += __shfl_xor(sq, 16); sum4 += __shfl_xor(sum4, 16); sq4 += __shfl_xor(sq4, 16);
    sum += __shfl_xor(sum, 32); sq += __shfl_xor(sq, 32); sum4 += __shfl_xor(sum4, 32); sq4 += __shfl_xor(sq4, 32);
    if (l < 16) {
      sStat[(w * 16 + tl) * 2] = sum; sStat[(w * 16 + tl) * 2 + 1] = sq;
      sStat[((4 + w) * 16 + tl) * 2] = sum4; sStat[((4 + w) * 16 + tl) * 2 + 1] = sq4;
    }
  }

  f32x4 acc[3][5];
#pragma unroll
  for (int m = 0; m < 3; ++m)
#pragma unroll
    for (int t = 0; t < 5; ++t) acc[m][t] = (f32x4){0.f, 0.f, 0.f, 0.f};
  float cs[3] = {0.f, 0.f, 0.f};
  const int troff = tr_lane_off(l, w);

#pragma unroll
  for (int ii = 0; ii < 7; ++ii) {
    u32x4 wn[6];
    if (ii + 2 < 7) {
#pragma unroll
      for (int m = 0; m < 3; ++m)
#pragma unroll
        for (int hf = 0; hf < 2; ++hf) wn[m * 2 + hf] = *(const u32x4*)(wsrc + (size_t)(64 * (ii + 2) + 32 * hf) * QKV_ + m * D_);
    }
    const char* slot = sW + (ii & 1) * P1_SLOT;
#pragma unroll
    for (int s = 0; s < 2; ++s) {
      u32x4 xf[5], wf[3];
#pragma unroll
      for (int t = 0; t < 5; ++t) xf[t] = *(const u32x4*)(sX + xoff + ((ii * 2 + s) * 4 * P1_XP + t * 16) * 16);
#pragma unroll
      for (int m = 0; m < 3; ++m) wf[m] = wfrag(slot + m * P1_WG + s * 32 * 128 + troff);
#pragma unroll
      for (int m = 0; m < 3; ++m) {
#pragma unroll
        for (int t = 0; t < 5; ++t)
          if (!(m == 0 && t == 0)) acc[m][t] = mfma(wf[m], xf[t], acc[m][t]);
#pragma unroll
        for (int e = 0; e < 4; ++e) cs[m] = dot2(wf[m][e], 0x3F803F80u, cs[m]);
      }
    }
    if (ii + 1 < 7) {
      char* nslot = sW + ((ii + 1) & 1) * P1_SLOT;
#pragma unroll
      for (int m = 0; m < 3; ++m)
#pragma unroll
        for (int hf = 0; hf < 2; ++hf) *(u32x4*)(nslot + m * P1_WG + hf * 32 * 128 + wdst) = wr[(ii + 1) & 1][m * 2 + hf];
      if (ii + 2 < 7) {
#pragma unroll
        for (int e = 0; e < 6; ++e) wr[ii & 1][e] = wn[e];
      }
      __syncthreads();
    }
  }
  STAMP(0, 2);

  __syncthreads();
  char* sS = sX;
  {
    float csf[3][4];
#pragma unroll
    for (int m = 0; m < 3; ++m) {
      float c = cs[m];
      c += __shfl_xor(c, 16); c += __shfl_xor(c, 32);
#pragma unroll
      for (int r = 0; r < 4; ++r) csf[m][r] = __shfl(c, 4 * g + r);
    }
    const int quad = 4 * w + g;
#pragma unroll
    for (int t = 0; t < 5; ++t) {
      float sum, sq;
      if (t < 4) { sum = sStat[(t * 16 + tl) * 2]; sq = sStat[(t * 16 + tl) * 2 + 1]; }
      else {
        sum = 0.f; sq = 0.f;
#pragma unroll
        for (int i = 0; i < 4; ++i) { sum += sStat[((4 + i) * 16 + tl) * 2]; sq += sStat[((4 + i) * 16 + tl) * 2 + 1]; }
      }
      float mb, rstd;
      ln_params(sum, sq, mb, rstd);
#pragma unroll
      for (int m = 0; m < 3; ++m) {
        if (m == 0 && t == 0) continue;
        float v0 = (acc[m][t][0] - mb * csf[m][0]) * rstd;
        float v1 = (acc[m][t][1] - mb * csf[m][1]) * rstd;
        float v2 = (acc[m][t][2] - mb * csf[m][2]) * rstd;
        float v3 = (acc[m][t][3] - mb * csf[m][3]) * rstd;
        u32x2 pk = {pack_bf(v0, v1), pack_bf(v2, v3)};
        if (quad < 14) *(u32x2*)(sS + (m * 14 + quad) * P1_SP + (t * 16 + tl) * 8) = pk;
      }
    }
  }
  __syncthreads();
  STAMP(0, 3);

  {
    const int hh = g >> 1, part = g & 1;
    const int t = w * 16 + tl;
    const int tok = mt * 64 + t;
    const int q0 = hh * 7 + part * 4;
    const int nq = part ? 3 : 4;
    u32x2 qv[4];
#pragma unroll
    for (int i = 0; i < 4; ++i) {
      int qi = q0 + min(i, nq - 1);
      qv[i] = *(const u32x2*)(sS + (0 * 14 + qi) * P1_SP + (16 + t) * 8);
      if (i >= nq) qv[i] = (u32x2){0u, 0u};
    }
    float lg[8];
#pragma unroll
    for (int ww = 0; ww < 8; ++ww) {
      float sacc = 0.f;
#pragma unroll
      for (int i = 0; i < 4; ++i) {
        int qi = q0 + min(i, nq - 1);
        u32x2 kv = *(const u32x2*)(sS + (1 * 14 + qi) * P1_SP + (16 + t - 2 * ww) * 8);
        sacc = dot2(qv[i][0], kv[0], sacc);
        sacc = dot2(qv[i][1], kv[1], sacc);
      }
      lg[ww] = sacc;
    }
    const float scale = 0.1889822365046136f;
    float mx = -3.0e38f;
#pragma unroll
    for (int ww = 0; ww < 8; ++ww) {
      float sacc = lg[ww] + __shfl_xor(lg[ww], 16);
      float v = rbf(rbf(sacc) * scale);
      v = (tok - 2 * ww >= 0) ? v : -1e9f;
      lg[ww] = v;
      mx = fmaxf(mx, v);
    }
    float den = 0.f;
#pragma unroll
    for (int ww = 0; ww < 8; ++ww) { lg[ww] = __expf(lg[ww] - mx); den += lg[ww]; }
    float inv = 1.0f / den;
#pragma unroll
    for (int ww = 0; ww < 8; ++ww) lg[ww] = rbf(lg[ww] * inv);
    float oacc[4][4];
#pragma unroll
    for (int i = 0; i < 4; ++i)
#pragma unroll
      for (int e = 0; e < 4; ++e) oacc[i][e] = 0.f;
#pragma unroll
    for (int ww = 0; ww < 8; ++ww) {
#pragma unroll
      for (int i = 0; i < 4; ++i) {
        int qi = q0 + min(i, nq - 1);
        u32x2 vv = *(const u32x2*)(sS + (2 * 14 + qi) * P1_SP + (16 + t - 2 * ww) * 8);
        oacc[i][0] = fmaf(lg[ww], bf_lo(vv[0]), oacc[i][0]);
        oacc[i][1] = fmaf(lg[ww], bf_hi(vv[0]), oacc[i][1]);
        oacc[i][2] = fmaf(lg[ww], bf_lo(vv[1]), oacc[i][2]);
        oacc[i][3] = fmaf(lg[ww], bf_hi(vv[1]), oacc[i][3]);
      }
    }
    const int tbg = tok >> 4;
#pragma unroll
    for (int i = 0; i < 4; ++i) {
      if (i < nq) {
        int f = hp * 56 + (q0 + i) * 4;
        int s = f >> 5, ii = (f >> 4) & 1, gg = (f >> 2) & 3;
        u32x2 pk = {pack_bf(oacc[i][0], oacc[i][1]), pack_bf(oacc[i][2], oacc[i][3])};
        *(u32x2*)((char*)o_frag + ((size_t)((tbg * 14 + s) * 64 + gg * 16 + tl)) * 16 + ii * 8) = pk;
      }
    }
  }
  STAMP(0, 4);
}

constexpr int EP = 144;
__global__ __launch_bounds__(256, 1) void k_wo(const u16* __restrict__ o_frag, const u16* __restrict__ wo,
                                               const u16* __restrict__ x, u16* __restrict__ h,
                                               u16* __restrict__ h_frag) {
  __shared__ __attribute__((aligned(16))) char smem[448 * 128 + 64 * EP];
  char* sW = smem;
  char* sT = smem + 448 * 128;
  const int tid = threadIdx.x, l = tid & 63, g = l >> 4, tl = l & 15;
  const int w = __builtin_amdgcn_readfirstlane(tid >> 6);
  const int bid = blockIdx.x, xcd = bid & 7, jj = bid >> 3;
  const int mt = xcd * 4 + jj / 7, nt = jj % 7;
  STAMP(1, 0);

  u32x4 wr[14];
  const int wc = tid & 7, wk = tid >> 3;
  {
    const u16* src = wo + (size_t)wk * D_ + nt * 64 + wc * 8;
#pragma unroll
    for (int ii = 0; ii < 14; ++ii) wr[ii] = *(const u32x4*)(src + (size_t)(32 * ii) * D_);
  }
  u32x4 xf[14];
  {
    const u32x4* p = (const u32x4*)o_frag + (size_t)((mt * 4 + w) * 14) * 64 + l;
#pragma unroll
    for (int s = 0; s < 14; ++s) xf[s] = p[s * 64];
  }
  u32x4 res[2];
#pragma unroll
  for (int i = 0; i < 2; ++i)
    res[i] = *(const u32x4*)(x + (size_t)(mt * 64 + w * 16 + i * 8 + (l >> 3)) * D_ + nt * 64 + (l & 7) * 8);

  const int wdst = strip_off(wk, wc);
#pragma unroll
  for (int ii = 0; ii < 14; ++ii) *(u32x4*)(sW + ii * 32 * 128 + wdst) = wr[ii];
  __syncthreads();
  STAMP(1, 1);

  f32x4 acc[4];
#pragma unroll
  for (int f = 0; f < 4; ++f) acc[f] = (f32x4){0.f, 0.f, 0.f, 0.f};
  int tro[4];
#pragma unroll
  for (int f = 0; f < 4; ++f) tro[f] = tr_lane_off(l, f);
#pragma unroll
  for (int s = 0; s < 14; ++s) {
#pragma unroll
    for (int f = 0; f < 4; ++f) acc[f] = mfma(wfrag(sW + s * 32 * 128 + tro[f]), xf[s], acc[f]);
  }
  STAMP(1, 2);
  char* tw = sT + w * 16 * EP;
#pragma unroll
  for (int f = 0; f < 4; ++f) {
    u32x2 pk = {pack_bf(acc[f][0], acc[f][1]), pack_bf(acc[f][2], acc[f][3])};
    *(u32x2*)(tw + tl * EP + f * 32 + g * 8) = pk;
  }
  __builtin_amdgcn_wave_barrier();
  asm volatile("" ::: "memory");
#pragma unroll
  for (int i = 0; i < 2; ++i) {
    char* p = tw + (i * 8 + (l >> 3)) * EP + (l & 7) * 16;
    u32x4 hv = add_bf8(res[i], *(const u32x4*)p);
    *(u32x4*)(h + (size_t)(mt * 64 + w * 16 + i * 8 + (l >> 3)) * D_ + nt * 64 + (l & 7) * 8) = hv;
    *(u32x4*)p = hv;
  }
  __builtin_amdgcn_wave_barrier();
  asm volatile("" ::: "memory");
#pragma unroll
  for (int i = 0; i < 2; ++i) {
    u32x4 v = *(const u32x4*)(tw + tl * EP + (i * 4 + g) * 16);
    *((u32x4*)h_frag + (size_t)((mt * 4 + w) * 14 + nt * 2 + i) * 64 + l) = v;
  }
  STAMP(1, 3);
}

constexpr int P3_WS = 448 * 128;
__global__ __launch_bounds__(512, 2) void k_gateup(const u16* __restrict__ h_frag, const u16* __restrict__ wg,
                                                   const u16* __restrict__ wu, u16* __restrict__ a_frag) {
  __shared__ __attribute__((aligned(16))) char smem[2 * P3_WS + 8 * 16 * 4];
  char* sW = smem;
  float* sCs = (float*)(smem + 2 * P3_WS);
  const int tid = threadIdx.x, l = tid & 63, g = l >> 4, tl = l & 15;
  const int w = __builtin_amdgcn_readfirstlane(tid >> 6);
  const int bid = blockIdx.x, xcd = bid & 7, jj = bid >> 3;
  const int mt = (xcd & 1) * 4 + jj / 7, nt = (xcd >> 1) * 7 + jj % 7;
  STAMP(2, 0);

  const int wc = tid & 7, wk = tid >> 3;
  const u16* sg = wg + (size_t)wk * F_ + nt * 64 + wc * 8;
  const u16* su = wu + (size_t)wk * F_ + nt * 64 + wc * 8;
  const u32x4* xp = (const u32x4*)h_frag + (size_t)((mt * 16 + w * 2) * 14) * 64 + l;
  const int wdst = strip_off(rho_r(wk), wc);

  u32x4 wr[7][2];
  u32x4 xf[2][14];
#pragma unroll
  for (int gi = 0; gi < 2; ++gi) {
    wr[gi][0] = *(const u32x4*)(sg + (size_t)(64 * gi) * F_);
    wr[gi][1] = *(const u32x4*)(su + (size_t)(64 * gi) * F_);
#pragma unroll
    for (int s = 0; s < 2; ++s) { xf[0][gi * 2 + s] = xp[(gi * 2 + s) * 64]; xf[1][gi * 2 + s] = xp[(14 + gi * 2 + s) * 64]; }
  }

  f32x4 acc[2][8];
#pragma unroll
  for (int t = 0; t < 2; ++t)
#pragma unroll
    for (int f = 0; f < 8; ++f) acc[t][f] = (f32x4){0.f, 0.f, 0.f, 0.f};
  int tro[4];
#pragma unroll
  for (int f = 0; f < 4; ++f) tro[f] = tr_lane_off(l, f);
  float sum[2] = {0.f, 0.f}, sq[2] = {0.f, 0.f};
  const char* csbase = sW + (w >> 2) * P3_WS + tr_lane_off(l, w & 3);
  float csum = 0.f;

#pragma unroll
  for (int ii = 0; ii < 7; ++ii) {
    if (ii + 2 < 7) {
      const int gi = ii + 2;
      wr[gi][0] = *(const u32x4*)(sg + (size_t)(64 * gi) * F_);
      wr[gi][1] = *(const u32x4*)(su + (size_t)(64 * gi) * F_);
#pragma unroll
      for (int s = 0; s < 2; ++s) { xf[0][gi * 2 + s] = xp[(gi * 2 + s) * 64]; xf[1][gi * 2 + s] = xp[(14 + gi * 2 + s) * 64]; }
    }
    *(u32x4*)(sW + ii * 64 * 128 + wdst) = wr[ii][0];
    *(u32x4*)(sW + P3_WS + ii * 64 * 128 + wdst) = wr[ii][1];
    __syncthreads();
#pragma unroll
    for (int s2 = 0; s2 < 2; ++s2) {
      const int s = ii * 2 + s2;
#pragma unroll
      for (int f = 0; f < 8; ++f) {
        u32x4 wf = wfrag(sW + (f >> 2) * P3_WS + s * 32 * 128 + tro[f & 3]);
        acc[0][f] = mfma(wf, xf[0][s], acc[0][f]);
        acc[1][f] = mfma(wf, xf[1][s], acc[1][f]);
      }
      u32x4 cw = wfrag(csbase + s * 32 * 128);
#pragma unroll
      for (int e = 0; e < 4; ++e) {
        csum = dot2(cw[e], 0x3F803F80u, csum);
#pragma unroll
        for (int t = 0; t < 2; ++t) {
          sum[t] = dot2(xf[t][s][e], 0x3F803F80u, sum[t]);
          sq[t] = dot2(xf[t][s][e], xf[t][s][e], sq[t]);
        }
      }
    }
  }
  STAMP(2, 1);
  csum += __shfl_xor(csum, 16); csum += __shfl_xor(csum, 32);
  if (l < 16) sCs[w * 16 + tl] = csum;
  float mb[2], rstd[2];
#pragma unroll
  for (int t = 0; t < 2; ++t) {
    float a = sum[t], b = sq[t];
    a += __shfl_xor(a, 16); b += __shfl_xor(b, 16);
    a += __shfl_xor(a, 32); b += __shfl_xor(b, 32);
    ln_params(a, b, mb[t], rstd[t]);
  }
  __syncthreads();
  STAMP(2, 2);
#pragma unroll
  for (int fp = 0; fp < 2; ++fp) {
    f32x4 cg[2], cu[2];
#pragma unroll
    for (int i = 0; i < 2; ++i) {
      cg[i] = *(const f32x4*)(sCs + (fp * 2 + i) * 16 + 4 * g);
      cu[i] = *(const f32x4*)(sCs + (4 + fp * 2 + i) * 16 + 4 * g);
    }
#pragma unroll
    for (int t = 0; t < 2; ++t) {
      u32x4 out;
#pragma unroll
      for (int i = 0; i < 2; ++i) {
        float av[4];
#pragma unroll
        for (int r = 0; r < 4; ++r) {
          float gv = rbf((acc[t][fp * 2 + i][r] - mb[t] * cg[i][r]) * rstd[t]);
          float uv = rbf((acc[t][4 + fp * 2 + i][r] - mb[t] * cu[i][r]) * rstd[t]);
          float sl = rbf(gv * __builtin_amdgcn_rcpf(1.0f + __expf(-gv)));
          av[r] = sl * uv;
        }
        out[i * 2] = pack_bf(av[0], av[1]);
        out[i * 2 + 1] = pack_bf(av[2], av[3]);
      }
      const int tbg = mt * 16 + w * 2 + t;
      *((u32x4*)a_frag + (size_t)(tbg * 56 + nt * 2 + fp) * 64 + l) = out;
    }
  }
  STAMP(2, 3);
}

constexpr int P4_SS = 8;
constexpr int P4_BUF = P4_SS * 32 * 128;
__global__ __launch_bounds__(256, 1) void k_down(const u16* __restrict__ a_frag, const u16* __restrict__ wd,
                                                 const u16* __restrict__ h, u16* __restrict__ out) {
  __shared__ __attribute__((aligned(16))) char smem[2 * P4_BUF + 64 * EP];
  char* sW = smem;
  char* sT = smem + 2 * P4_BUF;
  const int tid = threadIdx.x, l = tid & 63, g = l >> 4, tl = l & 15;
  const int w = __builtin_amdgcn_readfirstlane(tid >> 6);
  const int bid = blockIdx.x, xcd = bid & 7, jj = bid >> 3;
  const int mt = xcd * 4 + jj / 7, nt = jj % 7;
  const int wc = tid & 7, wk = tid >> 3;
  const u16* wsrc = wd + (size_t)wk * D_ + nt * 64 + wc * 8;
  const u32x4* xp = (const u32x4*)a_frag + (size_t)((mt * 4 + w) * 56) * 64 + l;
  const int wdst = strip_off(wk, wc);
  int tro[4];
#pragma unroll
  for (int f = 0; f < 4; ++f) tro[f] = tr_lane_off(l, f);
  STAMP(3, 0);

  u32x4 wr[2][P4_SS];
  u32x4 xr[3][P4_SS];
#pragma unroll
  for (int p = 0; p < 2; ++p) {
#pragma unroll
    for (int ii = 0; ii < P4_SS; ++ii) wr[p][ii] = *(const u32x4*)(wsrc + (size_t)(32 * (p * P4_SS + ii)) * D_);
#pragma unroll
    for (int s = 0; s < P4_SS; ++s) xr[p][s] = xp[(p * P4_SS + s) * 64];
  }
  u32x4 res[2];
#pragma unroll
  for (int i = 0; i < 2; ++i)
    res[i] = *(const u32x4*)(h + (size_t)(mt * 64 + w * 16 + i * 8 + (l >> 3)) * D_ + nt * 64 + (l & 7) * 8);

  f32x4 acc[4];
#pragma unroll
  for (int f = 0; f < 4; ++f) acc[f] = (f32x4){0.f, 0.f, 0.f, 0.f};

#pragma unroll
  for (int ss = 0; ss < 7; ++ss) {
    char* buf = sW + (ss & 1) * P4_BUF;
#pragma unroll
    for (int ii = 0; ii < P4_SS; ++ii) *(u32x4*)(buf + ii * 32 * 128 + wdst) = wr[ss & 1][ii];
    if (ss + 2 < 7) {
#pragma unroll
      for (int ii = 0; ii < P4_SS; ++ii) wr[ss & 1][ii] = *(const u32x4*)(wsrc + (size_t)(32 * ((ss + 2) * P4_SS + ii)) * D_);
#pragma unroll
      for (int s = 0; s < P4_SS; ++s) xr[(ss + 2) % 3][s] = xp[((ss + 2) * P4_SS + s) * 64];
    }
    __syncthreads();
#pragma unroll
    for (int s = 0; s < P4_SS; ++s) {
#pragma unroll
      for (int f = 0; f < 4; ++f) acc[f] = mfma(wfrag(buf + s * 32 * 128 + tro[f]), xr[ss % 3][s], acc[f]);
    }
  }
  STAMP(3, 1);
  char* tw = sT + w * 16 * EP;
#pragma unroll
  for (int f = 0; f < 4; ++f) {
    u32x2 pk = {pack_bf(acc[f][0], acc[f][1]), pack_bf(acc[f][2], acc[f][3])};
    *(u32x2*)(tw + tl * EP + f * 32 + g * 8) = pk;
  }
  __builtin_amdgcn_wave_barrier();
  asm volatile("" ::: "memory");
#pragma unroll
  for (int i = 0; i < 2; ++i) {
    const char* p = tw + (i * 8 + (l >> 3)) * EP + (l & 7) * 16;
    u32x4 hv = add_bf8(res[i], *(const u32x4*)p);
    *(u32x4*)(out + (size_t)(mt * 64 + w * 16 + i * 8 + (l >> 3)) * D_ + nt * 64 + (l & 7) * 8) = hv;
  }
  STAMP(3, 2);
}

extern "C" void kernel_launch(void* const* d_in, const int* in_sizes, int n_in, void* d_out, int out_size,
                              void* d_ws, size_t ws_size, hipStream_t stream) {
  const u16* x = (const u16*)d_in[0];
  const u16* wqkv = (const u16*)d_in[1];
  const u16* wo = (const u16*)d_in[2];
  const u16* wg = (const u16*)d_in[4];
  const u16* wu = (const u16*)d_in[5];
  const u16* wd = (const u16*)d_in[6];
  u16* out = (u16*)d_out;
  char* ws = (char*)d_ws;
  u16* o_frag = (u16*)ws;
  u16* h = (u16*)(ws + (2u << 20));
  u16* h_frag = (u16*)(ws + (4u << 20));
  u16* a_frag = (u16*)(ws + (6u << 20));
  k_qkv_attn<<<256, 256, 0, stream>>>(x, wqkv, o_frag);
  k_wo<<<224, 256, 0, stream>>>(o_frag, wo, x, h, h_frag);
  k_gateup<<<224, 512, 0, stream>>>(h_frag, wg, wu, a_frag);
  k_down<<<224, 256, 0, stream>>>(a_frag, wd, h, out);
}
```

```cpp
#include <hip/hip_runtime.h>
#include <hip/hip_bf16.h>
#include <stdint.h>

typedef unsigned short u16;
typedef __attribute__((ext_vector_type(8))) __bf16 bf16x8;
typedef __attribute__((ext_vector_type(4))) short s16x4;
typedef __attribute__((ext_vector_type(4))) float f32x4;
typedef __attribute__((ext_vector_type(4))) unsigned int u32x4;
typedef __attribute__((ext_vector_type(2))) unsigned int u32x2;
typedef __attribute__((ext_vector_type(2))) __bf16 bf16x2;
#define LDS_AS __attribute__((address_space(3)))
#define SB() __builtin_amdgcn_sched_barrier(0)

#define STAMP(K,I)

constexpr int S_ = 2048, D_ = 448, F_ = 1792, QKV_ = 1344;
constexpr unsigned ONES = 0x3F803F80u;

__device__ __forceinline__ float bf_lo(unsigned u) { return __uint_as_float(u << 16); }
__device__ __forceinline__ float bf_hi(unsigned u) { return __uint_as_float(u & 0xffff0000u); }
__device__ __forceinline__ unsigned pack_bf(float a, float b) {
  bf16x2 v = {(__bf16)a, (__bf16)b};
  return __builtin_bit_cast(unsigned, v);
}
__device__ __forceinline__ float rbf(float a) { return (float)(__bf16)a; }
__device__ __forceinline__ float dot2(unsigned a, unsigned b, float c) {
  return __builtin_amdgcn_fdot2_f32_bf16(__builtin_bit_cast(bf16x2, a), __builtin_bit_cast(bf16x2, b), c, false);
}
__device__ __forceinline__ u32x2 tr_read(const char* p) {
  s16x4 v = __builtin_amdgcn_ds_read_tr16_b64_v4i16((LDS_AS s16x4*)p);
  return __builtin_bit_cast(u32x2, v);
}
__device__ __forceinline__ u32x4 wfrag(const char* p) {
  u32x2 a = tr_read(p), b = tr_read(p + 16 * 128);
  u32x4 r = {a[0], a[1], b[0], b[1]};
  return r;
}
__device__ __forceinline__ f32x4 mfma(u32x4 a, u32x4 b, f32x4 c) {
  return __builtin_amdgcn_mfma_f32_16x16x32_bf16(__builtin_bit_cast(bf16x8, a), __builtin_bit_cast(bf16x8, b), c, 0, 0, 0);
}
__device__ __forceinline__ int rho_r(int k) { return (k & ~31) | ((k & 4) << 2) | ((k & 24) >> 1) | (k & 3); }
__device__ __forceinline__ int strip_off(int r, int c) { return r * 128 + ((c ^ (((r >> 1) & 3) << 1)) << 4); }
__device__ __forceinline__ int tr_lane_off(int l, int nb) {
  int g = l >> 4, q = (l & 15) >> 2, p = l & 3;
  int swz = 2 * (g & 1) + (q >> 1);
  return (4 * g + q) * 128 + ((nb ^ swz) << 5) + p * 8;
}
__device__ __forceinline__ void ln_params(float sum, float sq, float& mb, float& rstd) {
  float mean = sum * (1.0f / D_);
  float var = fmaxf(sq * (1.0f / D_) - mean * mean, 0.0f);
  mb = rbf(mean);
  float den = rbf(sqrtf(rbf(rbf(var) + 1e-5f)));
  rstd = 1.0f / den;
}
__device__ __forceinline__ u32x4 add_bf8(u32x4 a, u32x4 b) {
  u32x4 r;
#pragma unroll
  for (int e = 0; e < 4; ++e) r[e] = pack_bf(bf_lo(a[e]) + bf_lo(b[e]), bf_hi(a[e]) + bf_hi(b[e]));
  return r;
}

constexpr int P1_XP = 81;
constexpr int P1_X = 56 * P1_XP * 16;
constexpr int P1_WG = 64 * 128;
constexpr int P1_SLOT = 3 * P1_WG;
constexpr int P1_SP = 672;
constexpr int P1_SMEM = P1_X + 3 * P1_SLOT + 4 * 16 * 8;

__global__ __launch_bounds__(256, 1) void k_qkv_attn(const u16* __restrict__ x, const u16* __restrict__ wqkv,
                                                     u16* __restrict__ o_frag) {
  __shared__ __attribute__((aligned(16))) char smem[P1_SMEM];
  char* sX = smem;
  char* sW = smem + P1_X;
  float* sStat = (float*)(smem + P1_X + 3 * P1_SLOT);

  const int tid = threadIdx.x, l = tid & 63, g = l >> 4, tl = l & 15;
  const int w = __builtin_amdgcn_readfirstlane(tid >> 6);
  const int bid = blockIdx.x, xcd = bid & 7, jj = bid >> 3;
  const int mt = xcd * 4 + (jj >> 3), hp = jj & 7;
  const int row0 = mt * 64 - 16;
  STAMP(0, 0);

  const int wc = tid & 7, wk = tid >> 3;
  const u16* wsrc = wqkv + (size_t)wk * QKV_ + hp * 56 + min(wc, 6) * 8;
  const int wdst = strip_off(rho_r(wk), wc);
  const u16* xsrc[3];
  int xdst[3];
#pragma unroll
  for (int j = 0; j < 3; ++j) {
    int r = min(wk + 32 * j, 79);
    xsrc[j] = x + (size_t)max(row0 + r, 0) * D_ + wc * 8;
    xdst[j] = (wc * P1_XP + r) * 16;
  }
  const bool x2ok = tid < 128;

  u32x4 wr[3][6], xr[3][3];
#define P1_GLOAD_W(B, GI, E) wr[B][E] = *(const u32x4*)(wsrc + (size_t)(64 * (GI) + 32 * ((E) & 1)) * QKV_ + ((E) >> 1) * D_)
#define P1_GLOAD_X(B, GI, J) xr[B][J] = *(const u32x4*)(xsrc[J] + 64 * (GI))
#define P1_STORE(B, GI) do { \
    char* ns_ = sW + ((GI) % 3) * P1_SLOT + wdst; \
    _Pragma("unroll") for (int e = 0; e < 6; ++e) *(u32x4*)(ns_ + (e >> 1) * P1_WG + (e & 1) * 32 * 128) = wr[B][e]; \
    *(u32x4*)(sX + (GI) * 8 * P1_XP * 16 + xdst[0]) = xr[B][0]; \
    *(u32x4*)(sX + (GI) * 8 * P1_XP * 16 + xdst[1]) = xr[B][1]; \
    if (x2ok) *(u32x4*)(sX + (GI) * 8 * P1_XP * 16 + xdst[2]) = xr[B][2]; \
  } while (0)
#pragma unroll
  for (int gi = 0; gi < 3; ++gi) {
#pragma unroll
    for (int j = 0; j < 3; ++j) P1_GLOAD_X(gi, gi, j);
#pragma unroll
    for (int e = 0; e < 6; ++e) P1_GLOAD_W(gi, gi, e);
  }
  P1_STORE(0, 0);
  __syncthreads();
  STAMP(0, 1);

  const int xoff = (g * P1_XP + tl) * 16;
  f32x4 acc[3][5];
#pragma unroll
  for (int m = 0; m < 3; ++m)
#pragma unroll
    for (int t = 0; t < 5; ++t) acc[m][t] = (f32x4){0.f, 0.f, 0.f, 0.f};
  float cs[3] = {0.f, 0.f, 0.f};
  float sum = 0.f, sq = 0.f, sum4 = 0.f, sq4 = 0.f;
  const int troff = tr_lane_off(l, w);

  u32x4 xf[2][6], wf[2][3];
#define P1_LOAD(B, S) do { \
    const char* slot_ = sW + (((S) >> 1) % 3) * P1_SLOT + ((S) & 1) * 32 * 128 + troff; \
    _Pragma("unroll") for (int m = 0; m < 3; ++m) wf[B][m] = wfrag(slot_ + m * P1_WG); \
    _Pragma("unroll") for (int t = 0; t < 5; ++t) xf[B][t] = *(const u32x4*)(sX + xoff + ((S) * 4 * P1_XP + t * 16) * 16); \
    xf[B][5] = *(const u32x4*)(sX + xoff + ((S) * 4 * P1_XP + w * 16) * 16); \
  } while (0)
  P1_LOAD(0, 0);
#pragma unroll
  for (int ii = 0; ii < 7; ++ii) {
    if (ii + 1 < 7) {
      P1_STORE((ii + 1) % 3, ii + 1);
      __syncthreads();
    }
#pragma unroll
    for (int s2 = 0; s2 < 2; ++s2) {
      const int s = ii * 2 + s2;
      if (s + 1 < 14) P1_LOAD((s + 1) & 1, s + 1);
      SB();
#pragma unroll
      for (int m = 0; m < 3; ++m) {
        if (ii + 3 < 7) {
          P1_GLOAD_W(ii % 3, ii + 3, m * 2 + s2);
          if (m + 3 * s2 < 3) P1_GLOAD_X(ii % 3, ii + 3, m);
        }
#pragma unroll
        for (int t = 0; t < 5; ++t)
          if (!(m == 0 && t == 0)) acc[m][t] = mfma(wf[s & 1][m], xf[s & 1][t], acc[m][t]);
#pragma unroll
        for (int e = 0; e < 4; ++e) cs[m] = dot2(wf[s & 1][m][e], ONES, cs[m]);
        if (m == 0) {
#pragma unroll
          for (int e = 0; e < 4; ++e) { sum = dot2(xf[s & 1][5][e], ONES, sum); sq = dot2(xf[s & 1][5][e], xf[s & 1][5][e], sq); }
        }
        if (m == 1) {
#pragma unroll
          for (int e = 0; e < 4; ++e) { sum4 = dot2(xf[s & 1][4][e], ONES, sum4); sq4 = dot2(xf[s & 1][4][e], xf[s & 1][4][e], sq4); }
        }
        SB();
      }
    }
  }
  STAMP(0, 2);
  sum += __shfl_xor(sum, 16); sq += __shfl_xor(sq, 16); sum4 += __shfl_xor(sum4, 16); sq4 += __shfl_xor(sq4, 16);
  sum += __shfl_xor(sum, 32); sq += __shfl_xor(sq, 32); sum4 += __shfl_xor(sum4, 32); sq4 += __shfl_xor(sq4, 32);
  if (l < 16) { sStat[(w * 16 + tl) * 2] = sum; sStat[(w * 16 + tl) * 2 + 1] = sq; }

  __syncthreads();
  char* sS = sX;
  {
    float csf[3][4];
#pragma unroll
    for (int m = 0; m < 3; ++m) {
      float c = cs[m];
      c += __shfl_xor(c, 16); c += __shfl_xor(c, 32);
#pragma unroll
      for (int r = 0; r < 4; ++r) csf[m][r] = __shfl(c, 4 * g + r);
    }
    const int quad = 4 * w + g;
#pragma unroll
    for (int t = 0; t < 5; ++t) {
      float sm, sv;
      if (t < 4) { sm = sStat[(t * 16 + tl) * 2]; sv = sStat[(t * 16 + tl) * 2 + 1]; }
      else { sm = sum4; sv = sq4; }
      float mb, rstd;
      ln_params(sm, sv, mb, rstd);
#pragma unroll
      for (int m = 0; m < 3; ++m) {
        if (m == 0 && t == 0) continue;
        float v0 = (acc[m][t][0] - mb * csf[m][0]) * rstd;
        float v1 = (acc[m][t][1] - mb * csf[m][1]) * rstd;
        float v2 = (acc[m][t][2] - mb * csf[m][2]) * rstd;
        float v3 = (acc[m][t][3] - mb * csf[m][3]) * rstd;
        u32x2 pk = {pack_bf(v0, v1), pack_bf(v2, v3)};
        if (quad < 14) *(u32x2*)(sS + (m * 14 + quad) * P1_SP + (t * 16 + tl) * 8) = pk;
      }
    }
  }
  __syncthreads();
  STAMP(0, 3);

  {
    const int hh = g >> 1, part = g & 1;
    const int t = w * 16 + tl;
    const int tok = mt * 64 + t;
    const int q0 = hh * 7 + part * 4;
    const int nq = part ? 3 : 4;
    u32x2 qv[4];
#pragma unroll
    for (int i = 0; i < 4; ++i) {
      int qi = q0 + min(i, nq - 1);
      qv[i] = *(const u32x2*)(sS + (0 * 14 + qi) * P1_SP + (16 + t) * 8);
      if (i >= nq) qv[i] = (u32x2){0u, 0u};
    }
    float lg[8];
#pragma unroll
    for (int ww = 0; ww < 8; ++ww) {
      float sacc = 0.f;
#pragma unroll
      for (int i = 0; i < 4; ++i) {
        int qi = q0 + min(i, nq - 1);
        u32x2 kv = *(const u32x2*)(sS + (1 * 14 + qi) * P1_SP + (16 + t - 2 * ww) * 8);
        sacc = dot2(qv[i][0], kv[0], sacc);
        sacc = dot2(qv[i][1], kv[1], sacc);
      }
      lg[ww] = sacc;
    }
    const float scale = 0.1889822365046136f;
    float mx = -3.0e38f;
#pragma unroll
    for (int ww = 0; ww < 8; ++ww) {
      float sacc = lg[ww] + __shfl_xor(lg[ww], 16);
      float v = rbf(rbf(sacc) * scale);
      v = (tok - 2 * ww >= 0) ? v : -1e9f;
      lg[ww] = v;
      mx = fmaxf(mx, v);
    }
    float den = 0.f;
#pragma unroll
    for (int ww = 0; ww < 8; ++ww) { lg[ww] = __expf(lg[ww] - mx); den += lg[ww]; }
    float inv = 1.0f / den;
#pragma unroll
    for (int ww = 0; ww < 8; ++ww) lg[ww] = rbf(lg[ww] * inv);
    float oacc[4][4];
#pragma unroll
    for (int i = 0; i < 4; ++i)
#pragma unroll
      for (int e = 0; e < 4; ++e) oacc[i][e] = 0.f;
#pragma unroll
    for (int ww = 0; ww < 8; ++ww) {
#pragma unroll
      for (int i = 0; i < 4; ++i) {
        int qi = q0 + min(i, nq - 1);
        u32x2 vv = *(const u32x2*)(sS + (2 * 14 + qi) * P1_SP + (16 + t - 2 * ww) * 8);
        oacc[i][0] = fmaf(lg[ww], bf_lo(vv[0]), oacc[i][0]);
        oacc[i][1] = fmaf(lg[ww], bf_hi(vv[0]), oacc[i][1]);
        oacc[i][2] = fmaf(lg[ww], bf_lo(vv[1]), oacc[i][2]);
        oacc[i][3] = fmaf(lg[ww], bf_hi(vv[1]), oacc[i][3]);
      }
    }
    const int tbg = tok >> 4;
#pragma unroll
    for (int i = 0; i < 4; ++i) {
      if (i < nq) {
        int f = hp * 56 + (q0 + i) * 4;
        int s = f >> 5, ii = (f >> 4) & 1, gg = (f >> 2) & 3;
        u32x2 pk = {pack_bf(oacc[i][0], oacc[i][1]), pack_bf(oacc[i][2], oacc[i][3])};
        *(u32x2*)((char*)o_frag + ((size_t)((tbg * 14 + s) * 64 + gg * 16 + tl)) * 16 + ii * 8) = pk;
      }
    }
  }
  STAMP(0, 4);
}

constexpr int EP = 144;
__global__ __launch_bounds__(512, 2) void k_wo(const u16* __restrict__ o_frag, const u16* __restrict__ wo,
                                               const u16* __restrict__ x, u16* __restrict__ h,
                                               u16* __restrict__ h_frag, float* __restrict__ stats) {
  __shared__ __attribute__((aligned(16))) char smem[448 * 128 + 64 * EP + 4 * 4 * 1024];
  char* sW = smem;
  char* sT = smem + 448 * 128;
  char* sR = smem + 448 * 128 + 64 * EP;
  const int tid = threadIdx.x, l = tid & 63, g = l >> 4, tl = l & 15;
  const int w = __builtin_amdgcn_readfirstlane(tid >> 6);
  const int tb = w & 3, kh = w >> 2;
  const int bid = blockIdx.x, xcd = bid & 7, jj = bid >> 3;
  const int mt = xcd * 4 + jj / 7, nt = jj % 7;
  STAMP(1, 0);

  u32x4 wr[7];
  const int wc = tid & 7, wk = tid >> 3;
  {
    const u16* src = wo + (size_t)wk * D_ + nt * 64 + wc * 8;
#pragma unroll
    for (int ii = 0; ii < 7; ++ii) wr[ii] = *(const u32x4*)(src + (size_t)(64 * ii) * D_);
  }
  u32x4 xf[7];
  {
    const u32x4* p = (const u32x4*)o_frag + (size_t)((mt * 4 + tb) * 14 + kh * 7) * 64 + l;
#pragma unroll
    for (int s = 0; s < 7; ++s) xf[s] = p[s * 64];
  }
  u32x4 res[2];
  if (kh == 0) {
#pragma unroll
    for (int i = 0; i < 2; ++i)
      res[i] = *(const u32x4*)(x + (size_t)(mt * 64 + tb * 16 + i * 8 + (l >> 3)) * D_ + nt * 64 + (l & 7) * 8);
  }

  const int wdst = strip_off(wk, wc);
#pragma unroll
  for (int ii = 0; ii < 7; ++ii) *(u32x4*)(sW + ii * 64 * 128 + wdst) = wr[ii];
  __syncthreads();
  STAMP(1, 1);

  f32x4 acc[4];
#pragma unroll
  for (int f = 0; f < 4; ++f) acc[f] = (f32x4){0.f, 0.f, 0.f, 0.f};
  int tro[4];
#pragma unroll
  for (int f = 0; f < 4; ++f) tro[f] = tr_lane_off(l, f) + kh * 7 * 32 * 128;
  u32x4 wf[2][4];
#pragma unroll
  for (int f = 0; f < 4; ++f) wf[0][f] = wfrag(sW + tro[f]);
#pragma unroll
  for (int s = 0; s < 7; ++s) {
    if (s + 1 < 7) {
#pragma unroll
      for (int f = 0; f < 4; ++f) wf[(s + 1) & 1][f] = wfrag(sW + (s + 1) * 32 * 128 + tro[f]);
    }
    SB();
#pragma unroll
    for (int f = 0; f < 4; ++f) acc[f] = mfma(wf[s & 1][f], xf[s], acc[f]);
    SB();
  }
  STAMP(1, 2);
  if (kh == 1) {
#pragma unroll
    for (int f = 0; f < 4; ++f) *(f32x4*)(sR + (tb * 4 + f) * 1024 + l * 16) = acc[f];
  }
  __syncthreads();
  if (kh == 0) {
#pragma unroll
    for (int f = 0; f < 4; ++f) acc[f] += *(const f32x4*)(sR + (tb * 4 + f) * 1024 + l * 16);
    char* tw = sT + tb * 16 * EP;
#pragma unroll
    for (int f = 0; f < 4; ++f) {
      u32x2 pk = {pack_bf(acc[f][0], acc[f][1]), pack_bf(acc[f][2], acc[f][3])};
      *(u32x2*)(tw + tl * EP + f * 32 + g * 8) = pk;
    }
    __builtin_amdgcn_wave_barrier();
    asm volatile("" ::: "memory");
#pragma unroll
    for (int i = 0; i < 2; ++i) {
      char* p = tw + (i * 8 + (l >> 3)) * EP + (l & 7) * 16;
      u32x4 hv = add_bf8(res[i], *(const u32x4*)p);
      *(u32x4*)(h + (size_t)(mt * 64 + tb * 16 + i * 8 + (l >> 3)) * D_ + nt * 64 + (l & 7) * 8) = hv;
      *(u32x4*)p = hv;
      float sm = 0.f, sq = 0.f;
#pragma unroll
      for (int e = 0; e < 4; ++e) { sm = dot2(hv[e], ONES, sm); sq = dot2(hv[e], hv[e], sq); }
      sm += __shfl_xor(sm, 1); sq += __shfl_xor(sq, 1);
      sm += __shfl_xor(sm, 2); sq += __shfl_xor(sq, 2);
      sm += __shfl_xor(sm, 4); sq += __shfl_xor(sq, 4);
      if ((l & 7) == 0) {
        float2 st = make_float2(sm, sq);
        *(float2*)(stats + ((size_t)nt * S_ + mt * 64 + tb * 16 + i * 8 + (l >> 3)) * 2) = st;
      }
    }
    __builtin_amdgcn_wave_barrier();
    asm volatile("" ::: "memory");
#pragma unroll
    for (int i = 0; i < 2; ++i) {
      u32x4 v = *(const u32x4*)(tw + tl * EP + (i * 4 + g) * 16);
      *((u32x4*)h_frag + (size_t)((mt * 4 + tb) * 14 + nt * 2 + i) * 64 + l) = v;
    }
  }
  STAMP(1, 3);
}

constexpr int P3_WS = 448 * 128;
__global__ __launch_bounds__(512, 2) void k_gateup(const u16* __restrict__ h_frag, const u16* __restrict__ wg,
                                                   const u16* __restrict__ wu, const float* __restrict__ stats,
                                                   u16* __restrict__ a_frag) {
  __shared__ __attribute__((aligned(16))) char smem[2 * P3_WS + 8 * 16 * 4];
  char* sW = smem;
  float* sCs = (float*)(smem + 2 * P3_WS);
  const int tid = threadIdx.x, l = tid & 63, g = l >> 4, tl = l & 15;
  const int w = __builtin_amdgcn_readfirstlane(tid >> 6);
  const int bid = blockIdx.x, xcd = bid & 7, jj = bid >> 3;
  const int mt = (xcd & 1) * 4 + jj / 7, nt = (xcd >> 1) * 7 + jj % 7;
  STAMP(2, 0);

  const int wc = tid & 7, wk = tid >> 3;
  const u16* sg = wg + (size_t)wk * F_ + nt * 64 + wc * 8;
  const u16* su = wu + (size_t)wk * F_ + nt * 64 + wc * 8;
  const u32x4* xp = (const u32x4*)h_frag + (size_t)((mt * 16 + w * 2) * 14) * 64 + l;
  const int wdst = strip_off(rho_r(wk), wc);

  u32x4 wr[7][2];
  u32x4 xf[2][14];
#pragma unroll
  for (int gi = 0; gi < 2; ++gi) {
    wr[gi][0] = *(const u32x4*)(sg + (size_t)(64 * gi) * F_);
    wr[gi][1] = *(const u32x4*)(su + (size_t)(64 * gi) * F_);
#pragma unroll
    for (int s = 0; s < 2; ++s) { xf[0][gi * 2 + s] = xp[(gi * 2 + s) * 64]; xf[1][gi * 2 + s] = xp[(14 + gi * 2 + s) * 64]; }
  }

  f32x4 acc[2][8];
#pragma unroll
  for (int t = 0; t < 2; ++t)
#pragma unroll
    for (int f = 0; f < 8; ++f) acc[t][f] = (f32x4){0.f, 0.f, 0.f, 0.f};
  int tro[4];
#pragma unroll
  for (int f = 0; f < 4; ++f) tro[f] = tr_lane_off(l, f);
  const char* csbase = sW + (w >> 2) * P3_WS + tr_lane_off(l, w & 3);
  float csum = 0.f;

  *(u32x4*)(sW + wdst) = wr[0][0];
  *(u32x4*)(sW + P3_WS + wdst) = wr[0][1];
  __syncthreads();
  STAMP(2, 4);
  u32x4 wf[2][9];
#define P3_LOAD(B, S) do { \
    _Pragma("unroll") for (int f = 0; f < 8; ++f) wf[B][f] = wfrag(sW + (f >> 2) * P3_WS + (S) * 32 * 128 + tro[f & 3]); \
    wf[B][8] = wfrag(csbase + (S) * 32 * 128); \
  } while (0)
  P3_LOAD(0, 0);
  float2 st[2][2];
#pragma unroll
  for (int ii = 0; ii < 7; ++ii) {
    if (ii + 1 < 7) {
      *(u32x4*)(sW + (ii + 1) * 64 * 128 + wdst) = wr[ii + 1][0];
      *(u32x4*)(sW + P3_WS + (ii + 1) * 64 * 128 + wdst) = wr[ii + 1][1];
      __syncthreads();
    }
#pragma unroll
    for (int s2 = 0; s2 < 2; ++s2) {
      const int s = ii * 2 + s2;
      if (s + 1 < 14) P3_LOAD((s + 1) & 1, s + 1);
      SB();
#pragma unroll
      for (int q = 0; q < 4; ++q) {
        if (ii + 2 < 7) {
          const int gi = ii + 2;
          if (q == 0) wr[gi][s2] = *(const u32x4*)((s2 ? su : sg) + (size_t)(64 * gi) * F_);
          if (q == 1) xf[0][gi * 2 + s2] = xp[(gi * 2 + s2) * 64];
          if (q == 2) xf[1][gi * 2 + s2] = xp[(14 + gi * 2 + s2) * 64];
        } else if (ii == 5) {
          if (q < 2) st[s2][q] = *(const float2*)(stats + ((size_t)min(g + 4 * q, 6) * S_ + mt * 256 + (w * 2 + s2) * 16 + tl) * 2);
        }
#pragma unroll
        for (int f = q * 2; f < q * 2 + 2; ++f) {
          acc[0][f] = mfma(wf[s & 1][f], xf[0][s], acc[0][f]);
          acc[1][f] = mfma(wf[s & 1][f], xf[1][s], acc[1][f]);
        }
        csum = dot2(wf[s & 1][8][q], ONES, csum);
        SB();
      }
    }
    STAMP(2, 5 + ii);
  }
  STAMP(2, 1);
  csum += __shfl_xor(csum, 16); csum += __shfl_xor(csum, 32);
  if (l < 16) sCs[w * 16 + tl] = csum;
  float mb[2], rstd[2];
#pragma unroll
  for (int t = 0; t < 2; ++t) {
    float a = st[t][0].x + (g < 3 ? st[t][1].x : 0.f), b = st[t][0].y + (g < 3 ? st[t][1].y : 0.f);
    a += __shfl_xor(a, 16); b += __shfl_xor(b, 16);
    a += __shfl_xor(a, 32); b += __shfl_xor(b, 32);
    ln_params(a, b, mb[t], rstd[t]);
  }
  __syncthreads();
  STAMP(2, 2);
#pragma unroll
  for (int fp = 0; fp < 2; ++fp) {
    f32x4 cg[2], cu[2];
#pragma unroll
    for (int i = 0; i < 2; ++i) {
      cg[i] = *(const f32x4*)(sCs + (fp * 2 + i) * 16 + 4 * g);
      cu[i] = *(const f32x4*)(sCs + (4 + fp * 2 + i) * 16 + 4 * g);
    }
#pragma unroll
    for (int t = 0; t < 2; ++t) {
      u32x4 out;
#pragma unroll
      for (int i = 0; i < 2; ++i) {
        float av[4];
#pragma unroll
        for (int r = 0; r < 4; ++r) {
          float gv = rbf((acc[t][fp * 2 + i][r] - mb[t] * cg[i][r]) * rstd[t]);
          float uv = rbf((acc[t][4 + fp * 2 + i][r] - mb[t] * cu[i][r]) * rstd[t]);
          float sl = rbf(gv * __builtin_amdgcn_rcpf(1.0f + __expf(-gv)));
          av[r] = sl * uv;
        }
        out[i * 2] = pack_bf(av[0], av[1]);
        out[i * 2 + 1] = pack_bf(av[2], av[3]);
      }
      const int tbg = mt * 16 + w * 2 + t;
      *((u32x4*)a_frag + (size_t)(tbg * 56 + nt * 2 + fp) * 64 + l) = out;
    }
  }
  STAMP(2, 3);
}

constexpr int P4_SLOT = 128 * 128;
constexpr int P4_D = 4;
__global__ __launch_bounds__(512, 2) void k_down(const u16* __restrict__ a_frag, const u16* __restrict__ wd,
                                                 const u16* __restrict__ h, u16* __restrict__ out) {
  __shared__ __attribute__((aligned(16))) char smem[3 * P4_SLOT + 64 * EP + 4 * 4 * 1024];
  char* sW = smem;
  char* sT = smem + 3 * P4_SLOT;
  char* sR = smem + 3 * P4_SLOT + 64 * EP;
  const int tid = threadIdx.x, l = tid & 63, g = l >> 4, tl = l & 15;
  const int w = __builtin_amdgcn_readfirstlane(tid >> 6);
  const int tb = w & 3, kh = w >> 2;
  const int bid = blockIdx.x, xcd = bid & 7, jj = bid >> 3;
  const int mt = xcd * 4 + jj / 7, nt = jj % 7;
  const int wc = tid & 7, wk = tid >> 3;
  const u16* wsrc = wd + (size_t)wk * D_ + nt * 64 + wc * 8;
  const u32x4* xp = (const u32x4*)a_frag + (size_t)((mt * 4 + tb) * 56 + kh * 2) * 64 + l;
  const int wdst = strip_off(wk, wc);
  int tro[4];
#pragma unroll
  for (int f = 0; f < 4; ++f) tro[f] = tr_lane_off(l, f) + kh * 2 * 32 * 128;
  STAMP(3, 0);

  u32x4 wr[P4_D][2];
  u32x4 xr[P4_D][2];
#define P4_GW(IT, J) wr[(IT) % P4_D][J] = *(const u32x4*)(wsrc + (size_t)(128 * (IT) + 64 * (J)) * D_)
#define P4_GX(IT, J) xr[(IT) % P4_D][J] = xp[((IT) * 4 + (J)) * 64]
#pragma unroll
  for (int p = 0; p < P4_D - 1; ++p) {
    P4_GW(p, 0); P4_GW(p, 1); P4_GX(p, 0); P4_GX(p, 1);
  }
  f32x4 acc[4];
#pragma unroll
  for (int f = 0; f < 4; ++f) acc[f] = (f32x4){0.f, 0.f, 0.f, 0.f};

  *(u32x4*)(sW + wdst) = wr[0][0];
  *(u32x4*)(sW + 64 * 128 + wdst) = wr[0][1];
  __syncthreads();
  STAMP(3, 3);
  u32x4 wf[2][4];
#define P4_LOAD(KK) do { \
    const char* b_ = sW + (((KK) >> 1) % 3) * P4_SLOT + ((KK) & 1) * 32 * 128; \
    _Pragma("unroll") for (int f = 0; f < 4; ++f) wf[(KK) & 1][f] = wfrag(b_ + tro[f]); \
  } while (0)
  P4_LOAD(0);
  u32x4 res[2];
#pragma unroll
  for (int it = 0; it < 14; ++it) {
    if (it + 1 < 14) {
      char* buf = sW + ((it + 1) % 3) * P4_SLOT + wdst;
      *(u32x4*)(buf) = wr[(it + 1) % P4_D][0];
      *(u32x4*)(buf + 64 * 128) = wr[(it + 1) % P4_D][1];
      __syncthreads();
    }
#pragma unroll
    for (int j = 0; j < 2; ++j) {
      const int kk = it * 2 + j;
      if (kk + 1 < 28) P4_LOAD(kk + 1);
      SB();
      if (it + P4_D - 1 < 14) P4_GW(it + P4_D - 1, j);
      acc[0] = mfma(wf[kk & 1][0], xr[it % P4_D][j], acc[0]);
      acc[1] = mfma(wf[kk & 1][1], xr[it % P4_D][j], acc[1]);
      SB();
      if (it + P4_D - 1 < 14) P4_GX(it + P4_D - 1, j);
      else if (it == 12 && kh == 0)
        res[j] = *(const u32x4*)(h + (size_t)(mt * 64 + tb * 16 + j * 8 + (l >> 3)) * D_ + nt * 64 + (l & 7) * 8);
      acc[2] = mfma(wf[kk & 1][2], xr[it % P4_D][j], acc[2]);
      acc[3] = mfma(wf[kk & 1][3], xr[it % P4_D][j], acc[3]);
      SB();
    }
  }
  STAMP(3, 1);
  if (kh == 1) {
#pragma unroll
    for (int f = 0; f < 4; ++f) *(f32x4*)(sR + (tb * 4 + f) * 1024 + l * 16) = acc[f];
  }
  __syncthreads();
  if (kh == 0) {
#pragma unroll
    for (int f = 0; f < 4; ++f) acc[f] += *(const f32x4*)(sR + (tb * 4 + f) * 1024 + l * 16);
    char* tw = sT + tb * 16 * EP;
#pragma unroll
    for (int f = 0; f < 4; ++f) {
      u32x2 pk = {pack_bf(acc[f][0], acc[f][1]), pack_bf(acc[f][2], acc[f][3])};
      *(u32x2*)(tw + tl * EP + f * 32 + g * 8) = pk;
    }
    __builtin_amdgcn_wave_barrier();
    asm volatile("" ::: "memory");
#pragma unroll
    for (int i = 0; i < 2; ++i) {
      const char* p = tw + (i * 8 + (l >> 3)) * EP + (l & 7) * 16;
      u32x4 hv = add_bf8(res[i], *(const u32x4*)p);
      *(u32x4*)(out + (size_t)(mt * 64 + tb * 16 + i * 8 + (l >> 3)) * D_ + nt * 64 + (l & 7) * 8) = hv;
    }
  }
  STAMP(3, 2);
}

extern "C" void kernel_launch(void* const* d_in, const int* in_sizes, int n_in, void* d_out, int out_size,
                              void* d_ws, size_t ws_size, hipStream_t stream) {
  const u16* x = (const u16*)d_in[0];
  const u16* wqkv = (const u16*)d_in[1];
  const u16* wo = (const u16*)d_in[2];
  const u16* wg = (const u16*)d_in[4];
  const u16* wu = (const u16*)d_in[5];
  const u16* wd = (const u16*)d_in[6];
  u16* out = (u16*)d_out;
  char* ws = (char*)d_ws;
  u16* o_frag = (u16*)ws;
  u16* h = (u16*)(ws + (2u << 20));
  u16* h_frag = (u16*)(ws + (4u << 20));
  u16* a_frag = (u16*)(ws + (6u << 20));
  float* stats = (float*)(ws + (14u << 20));
  k_qkv_attn<<<256, 256, 0, stream>>>(x, wqkv, o_frag);
  k_wo<<<224, 512, 0, stream>>>(o_frag, wo, x, h, h_frag, stats);
  k_gateup<<<224, 512, 0, stream>>>(h_frag, wg, wu, stats, a_frag);
  k_down<<<224, 512, 0, stream>>>(a_frag, wd, h, out);
}
```

```cpp
#include <hip/hip_runtime.h>
#include <hip/hip_bf16.h>
#include <stdint.h>

typedef unsigned short u16;
typedef __attribute__((ext_vector_type(8))) __bf16 bf16x8;
typedef __attribute__((ext_vector_type(4))) short s16x4;
typedef __attribute__((ext_vector_type(4))) float f32x4;
typedef __attribute__((ext_vector_type(4))) unsigned int u32x4;
typedef __attribute__((ext_vector_type(2))) unsigned int u32x2;
typedef __attribute__((ext_vector_type(2))) __bf16 bf16x2;
#define LDS_AS __attribute__((address_space(3)))
#define SB() __builtin_amdgcn_sched_barrier(0)

#define STAMP(K,I)

constexpr int S_ = 2048, D_ = 448, F_ = 1792, QKV_ = 1344;
constexpr unsigned ONES = 0x3F803F80u;

__device__ __forceinline__ float bf_lo(unsigned u) { return __uint_as_float(u << 16); }
__device__ __forceinline__ float bf_hi(unsigned u) { return __uint_as_float(u & 0xffff0000u); }
__device__ __forceinline__ unsigned pack_bf(float a, float b) {
  bf16x2 v = {(__bf16)a, (__bf16)b};
  return __builtin_bit_cast(unsigned, v);
}
__device__ __forceinline__ float rbf(float a) { return (float)(__bf16)a; }
__device__ __forceinline__ float dot2(unsigned a, unsigned b, float c) {
  return __builtin_amdgcn_fdot2_f32_bf16(__builtin_bit_cast(bf16x2, a), __builtin_bit_cast(bf16x2, b), c, false);
}
__device__ __forceinline__ u32x2 tr_read(const char* p) {
  s16x4 v = __builtin_amdgcn_ds_read_tr16_b64_v4i16((LDS_AS s16x4*)p);
  return __builtin_bit_cast(u32x2, v);
}
__device__ __forceinline__ u32x4 wfrag(const char* p) {
  u32x2 a = tr_read(p), b = tr_read(p + 16 * 128);
  u32x4 r = {a[0], a[1], b[0], b[1]};
  return r;
}
__device__ __forceinline__ f32x4 mfma(u32x4 a, u32x4 b, f32x4 c) {
  return __builtin_amdgcn_mfma_f32_16x16x32_bf16(__builtin_bit_cast(bf16x8, a), __builtin_bit_cast(bf16x8, b), c, 0, 0, 0);
}
__device__ __forceinline__ int rho_r(int k) { return (k & ~31) | ((k & 4) << 2) | ((k & 24) >> 1) | (k & 3); }
__device__ __forceinline__ int strip_off(int r, int c) { return r * 128 + ((c ^ (((r >> 1) & 3) << 1)) << 4); }
__device__ __forceinline__ int tr_lane_off(int l, int nb) {
  int g = l >> 4, q = (l & 15) >> 2, p = l & 3;
  int swz = 2 * (g & 1) + (q >> 1);
  return (4 * g + q) * 128 + ((nb ^ swz) << 5) + p * 8;
}
__device__ __forceinline__ void ln_params(float sum, float sq, float& mb, float& rstd) {
  float mean = sum * (1.0f / D_);
  float var = fmaxf(sq * (1.0f / D_) - mean * mean, 0.0f);
  mb = rbf(mean);
  float den = rbf(sqrtf(rbf(rbf(var) + 1e-5f)));
  rstd = 1.0f / den;
}
__device__ __forceinline__ u32x4 add_bf8(u32x4 a, u32x4 b) {
  u32x4 r;
#pragma unroll
  for (int e = 0; e < 4; ++e) r[e] = pack_bf(bf_lo(a[e]) + bf_lo(b[e]), bf_hi(a[e]) + bf_hi(b[e]));
  return r;
}

constexpr int P1_XP = 81;
constexpr int P1_X = 56 * P1_XP * 16;
constexpr int P1_WG = 64 * 128;
constexpr int P1_SLOT = 3 * P1_WG;
constexpr int P1_SP = 672;
constexpr int P1_SMEM = P1_X + 3 * P1_SLOT + (80 * 2 + 3 * 64) * 4;

__global__ __launch_bounds__(512, 2) void k_qkv_attn(const u16* __restrict__ x, const u16* __restrict__ wqkv,
                                                     u16* __restrict__ o_frag) {
  __shared__ __attribute__((aligned(16))) char smem[P1_SMEM];
  char* sX = smem;
  char* sW = smem + P1_X;
  float* sSum = (float*)(smem + P1_X + 3 * P1_SLOT);
  float* sSq = sSum + 80;
  float* sCs = sSq + 80;

  const int tid = threadIdx.x, l = tid & 63, g = l >> 4, tl = l & 15;
  const int w = __builtin_amdgcn_readfirstlane(tid >> 6);
  const int fb = w & 3, hf = w >> 2;
  const int bid = blockIdx.x, xcd = bid & 7, jj = bid >> 3;
  const int mt = xcd * 4 + (jj >> 3), hp = jj & 7;
  const int row0 = mt * 64 - 16;
  STAMP(0, 0);

  const int wc = tid & 7, wk = tid >> 3;
  const u16* wsrc = wqkv + (size_t)wk * QKV_ + hp * 56 + min(wc, 6) * 8;
  const int wdst = strip_off(rho_r(wk), wc);
  const u16* xsrc0 = x + (size_t)max(row0 + wk, 0) * D_ + wc * 8;
  const u16* xsrc1 = x + (size_t)(row0 + 64 + (wk & 15)) * D_ + wc * 8;
  const int xdst0 = (wc * P1_XP + wk) * 16, xdst1 = (wc * P1_XP + 64 + (wk & 15)) * 16;
  const bool x1ok = tid < 128;

  u32x4 wr[3][3], xr[3][2];
#define P1_GLOAD_W(B, GI, M) wr[B][M] = *(const u32x4*)(wsrc + (size_t)(64 * (GI)) * QKV_ + (M) * D_)
#define P1_GLOAD_X(B, GI, J) xr[B][J] = *(const u32x4*)(((J) ? xsrc1 : xsrc0) + 64 * (GI))
#define P1_STORE(B, GI) do { \
    char* ns_ = sW + ((GI) % 3) * P1_SLOT + wdst; \
    u32x4 zz_ = {0u, 0u, 0u, 0u}, o1_ = {0x3F80u, 0u, 0u, 0u}, on_ = {ONES, ONES, ONES, ONES}; \
    *(u32x4*)(ns_) = (wc == 7) ? zz_ : wr[B][0]; \
    *(u32x4*)(ns_ + P1_WG) = (wc == 7) ? o1_ : wr[B][1]; \
    *(u32x4*)(ns_ + 2 * P1_WG) = (wc == 7) ? zz_ : wr[B][2]; \
    *(u32x4*)(sX + (GI) * 8 * P1_XP * 16 + xdst0) = (wk == 0) ? on_ : xr[B][0]; \
    if (x1ok) *(u32x4*)(sX + (GI) * 8 * P1_XP * 16 + xdst1) = xr[B][1]; \
  } while (0)
#pragma unroll
  for (int gi = 0; gi < 2; ++gi) {
#pragma unroll
    for (int j = 0; j < 2; ++j) P1_GLOAD_X(gi, gi, j);
#pragma unroll
    for (int m = 0; m < 3; ++m) P1_GLOAD_W(gi, gi, m);
  }
  P1_STORE(0, 0);
  __syncthreads();
  STAMP(0, 1);

  const int xoff = (g * P1_XP + tl + hf * 48) * 16;
  const int xgoff = (g * P1_XP + tl + (hf ? fb * 16 : 32)) * 16;
  f32x4 acc[3][3];
#pragma unroll
  for (int m = 0; m < 3; ++m)
#pragma unroll
    for (int t = 0; t < 3; ++t) acc[m][t] = (f32x4){0.f, 0.f, 0.f, 0.f};
  const int troff = tr_lane_off(l, fb);

  u32x4 xf[2][3], wf[2][3];
#define P1_LOAD(B, S) do { \
    const char* slot_ = sW + (((S) >> 1) % 3) * P1_SLOT + ((S) & 1) * 32 * 128 + troff; \
    _Pragma("unroll") for (int m = 0; m < 3; ++m) wf[B][m] = wfrag(slot_ + m * P1_WG); \
    _Pragma("unroll") for (int t = 0; t < 2; ++t) xf[B][t] = *(const u32x4*)(sX + xoff + ((S) * 4 * P1_XP + t * 16) * 16); \
    xf[B][2] = *(const u32x4*)(sX + xgoff + ((S) * 4 * P1_XP) * 16); \
  } while (0)
  P1_LOAD(0, 0);
#pragma unroll
  for (int ii = 0; ii < 7; ++ii) {
    if (ii + 1 < 7) {
      P1_STORE((ii + 1) % 3, ii + 1);
      __syncthreads();
    }
#pragma unroll
    for (int s2 = 0; s2 < 2; ++s2) {
      const int s = ii * 2 + s2;
      if (s + 1 < 14) P1_LOAD((s + 1) & 1, s + 1);
      SB();
      const int gi = (ii == 0) ? 2 + s2 : ii + 3;
#pragma unroll
      for (int m = 0; m < 3; ++m) {
        if (gi < 7) {
          if (ii == 0) { P1_GLOAD_W(gi % 3, gi, m); if (m < 2) P1_GLOAD_X(gi % 3, gi, m); }
          else if (s2 == 0) { if (m < 2) P1_GLOAD_W(gi % 3, gi, m); else P1_GLOAD_X(gi % 3, gi, 0); }
          else { if (m == 0) P1_GLOAD_W(gi % 3, gi, 2); else if (m == 1) P1_GLOAD_X(gi % 3, gi, 1); }
        }
        acc[m][0] = mfma(wf[s & 1][m], xf[s & 1][0], acc[m][0]);
        acc[m][1] = mfma(wf[s & 1][m], xf[s & 1][1], acc[m][1]);
        if (hf == 0) acc[m][2] = mfma(wf[s & 1][m], xf[s & 1][2], acc[m][2]);
        else if (m == 0) acc[0][2] = mfma(xf[s & 1][2], xf[s & 1][2], acc[0][2]);
        else if (m == 1) acc[1][2] = mfma(xf[s & 1][1], xf[s & 1][1], acc[1][2]);
        SB();
      }
    }
  }
  STAMP(0, 2);
  if (fb == 3 && g == 2) {
#pragma unroll
    for (int t = 0; t < 3; ++t)
      if (hf == 0 || t < 2) sSum[(hf * 3 + t) * 16 + tl] = acc[1][t][0];
  }
  if (hf == 1 && g == (tl >> 2)) {
    float a0 = acc[0][2][0], a1 = acc[1][2][0];
#pragma unroll
    for (int r = 1; r < 4; ++r) { if ((tl & 3) == r) { a0 = acc[0][2][r]; a1 = acc[1][2][r]; } }
    sSq[fb * 16 + tl] = a0;
    if (fb == 0) sSq[64 + tl] = a1;
  }
  if (hf == 0 && tl == 0) {
#pragma unroll
    for (int m = 0; m < 3; ++m) *(f32x4*)(sCs + m * 64 + fb * 16 + 4 * g) = acc[m][0];
  }
  __syncthreads();
  char* sS = sX;
  {
    f32x4 csf[3];
#pragma unroll
    for (int m = 0; m < 3; ++m) csf[m] = *(const f32x4*)(sCs + m * 64 + fb * 16 + 4 * g);
    const int quad = 4 * fb + g;
#pragma unroll
    for (int t = 0; t < 3; ++t) {
      if (hf == 0 || t < 2) {
        const int tb = hf * 3 + t;
        float mb, rstd;
        ln_params(sSum[tb * 16 + tl], sSq[tb * 16 + tl], mb, rstd);
#pragma unroll
        for (int m = 0; m < 3; ++m) {
          float v0 = (acc[m][t][0] - mb * csf[m][0]) * rstd;
          float v1 = (acc[m][t][1] - mb * csf[m][1]) * rstd;
          float v2 = (acc[m][t][2] - mb * csf[m][2]) * rstd;
          float v3 = (acc[m][t][3] - mb * csf[m][3]) * rstd;
          u32x2 pk = {pack_bf(v0, v1), pack_bf(v2, v3)};
          if (quad < 14) *(u32x2*)(sS + (m * 14 + quad) * P1_SP + (tb * 16 + tl) * 8) = pk;
        }
      }
    }
  }
  __syncthreads();
  STAMP(0, 3);

  {
    const int hh = l >> 5, part = (l >> 3) & 3;
    const int t = w * 8 + (l & 7);
    const int tok = mt * 64 + t;
    const int q0 = hh * 7 + part * 2;
    const int q1 = hh * 7 + min(part * 2 + 1, 6);
    const bool two = part < 3;
    u32x2 qv0 = *(const u32x2*)(sS + (0 * 14 + q0) * P1_SP + (16 + t) * 8);
    u32x2 qv1 = *(const u32x2*)(sS + (0 * 14 + q1) * P1_SP + (16 + t) * 8);
    if (!two) qv1 = (u32x2){0u, 0u};
    float lg[8];
#pragma unroll
    for (int ww = 0; ww < 8; ++ww) {
      u32x2 k0 = *(const u32x2*)(sS + (1 * 14 + q0) * P1_SP + (16 + t - 2 * ww) * 8);
      u32x2 k1 = *(const u32x2*)(sS + (1 * 14 + q1) * P1_SP + (16 + t - 2 * ww) * 8);
      float sacc = 0.f;
      sacc = dot2(qv0[0], k0[0], sacc); sacc = dot2(qv0[1], k0[1], sacc);
      sacc = dot2(qv1[0], k1[0], sacc); sacc = dot2(qv1[1], k1[1], sacc);
      lg[ww] = sacc;
    }
    const float scale = 0.1889822365046136f;
    float mx = -3.0e38f;
#pragma unroll
    for (int ww = 0; ww < 8; ++ww) {
      float sacc = lg[ww] + __shfl_xor(lg[ww], 8);
      sacc += __shfl_xor(sacc, 16);
      float v = rbf(rbf(sacc) * scale);
      v = (tok - 2 * ww >= 0) ? v : -1e9f;
      lg[ww] = v;
      mx = fmaxf(mx, v);
    }
    float den = 0.f;
#pragma unroll
    for (int ww = 0; ww < 8; ++ww) { lg[ww] = __expf(lg[ww] - mx); den += lg[ww]; }
    float inv = 1.0f / den;
#pragma unroll
    for (int ww = 0; ww < 8; ++ww) lg[ww] = rbf(lg[ww] * inv);
    float oacc[2][4];
#pragma unroll
    for (int i = 0; i < 2; ++i)
#pragma unroll
      for (int e = 0; e < 4; ++e) oacc[i][e] = 0.f;
#pragma unroll
    for (int ww = 0; ww < 8; ++ww) {
#pragma unroll
      for (int i = 0; i < 2; ++i) {
        u32x2 vv = *(const u32x2*)(sS + (2 * 14 + (i ? q1 : q0)) * P1_SP + (16 + t - 2 * ww) * 8);
        oacc[i][0] = fmaf(lg[ww], bf_lo(vv[0]), oacc[i][0]);
        oacc[i][1] = fmaf(lg[ww], bf_hi(vv[0]), oacc[i][1]);
        oacc[i][2] = fmaf(lg[ww], bf_lo(vv[1]), oacc[i][2]);
        oacc[i][3] = fmaf(lg[ww], bf_hi(vv[1]), oacc[i][3]);
      }
    }
    const int tbg = tok >> 4;
#pragma unroll
    for (int i = 0; i < 2; ++i) {
      if (i == 0 || two) {
        int f = hp * 56 + (q0 + i) * 4;
        int s = f >> 5, ii = (f >> 4) & 1, gg = (f >> 2) & 3;
        u32x2 pk = {pack_bf(oacc[i][0], oacc[i][1]), pack_bf(oacc[i][2], oacc[i][3])};
        *(u32x2*)((char*)o_frag + ((size_t)((tbg * 14 + s) * 64 + gg * 16 + (tok & 15))) * 16 + ii * 8) = pk;
      }
    }
  }
  STAMP(0, 4);
}

constexpr int EP = 144;
__global__ __launch_bounds__(512, 2) void k_wo(const u16* __restrict__ o_frag, const u16* __restrict__ wo,
                                               const u16* __restrict__ x, u16* __restrict__ h,
                                               u16* __restrict__ h_frag, float* __restrict__ stats) {
  __shared__ __attribute__((aligned(16))) char smem[448 * 128 + 64 * EP + 4 * 4 * 1024];
  char* sW = smem;
  char* sT = smem + 448 * 128;
  char* sR = smem + 448 * 128 + 64 * EP;
  const int tid = threadIdx.x, l = tid & 63, g = l >> 4, tl = l & 15;
  const int w = __builtin_amdgcn_readfirstlane(tid >> 6);
  const int tb = w & 3, kh = w >> 2;
  const int bid = blockIdx.x, xcd = bid & 7, jj = bid >> 3;
  const int mt = xcd * 4 + jj / 7, nt = jj % 7;
  STAMP(1, 0);

  u32x4 wr[7];
  const int wc = tid & 7, wk = tid >> 3;
  {
    const u16* src = wo + (size_t)wk * D_ + nt * 64 + wc * 8;
#pragma unroll
    for (int ii = 0; ii < 7; ++ii) wr[ii] = *(const u32x4*)(src + (size_t)(64 * ii) * D_);
  }
  u32x4 xf[7];
  {
    const u32x4* p = (const u32x4*)o_frag + (size_t)((mt * 4 + tb) * 14 + kh * 7) * 64 + l;
#pragma unroll
    for (int s = 0; s < 7; ++s) xf[s] = p[s * 64];
  }
  u32x4 res[2];
  if (kh == 0) {
#pragma unroll
    for (int i = 0; i < 2; ++i)
      res[i] = *(const u32x4*)(x + (size_t)(mt * 64 + tb * 16 + i * 8 + (l >> 3)) * D_ + nt * 64 + (l & 7) * 8);
  }

  const int wdst = strip_off(wk, wc);
#pragma unroll
  for (int ii = 0; ii < 7; ++ii) *(u32x4*)(sW + ii * 64 * 128 + wdst) = wr[ii];
  __syncthreads();
  STAMP(1, 1);

  f32x4 acc[4];
#pragma unroll
  for (int f = 0; f < 4; ++f) acc[f] = (f32x4){0.f, 0.f, 0.f, 0.f};
  int tro[4];
#pragma unroll
  for (int f = 0; f < 4; ++f) tro[f] = tr_lane_off(l, f) + kh * 7 * 32 * 128;
  u32x4 wf[2][4];
#pragma unroll
  for (int f = 0; f < 4; ++f) wf[0][f] = wfrag(sW + tro[f]);
#pragma unroll
  for (int s = 0; s < 7; ++s) {
    if (s + 1 < 7) {
#pragma unroll
      for (int f = 0; f < 4; ++f) wf[(s + 1) & 1][f] = wfrag(sW + (s + 1) * 32 * 128 + tro[f]);
    }
    SB();
#pragma unroll
    for (int f = 0; f < 4; ++f) acc[f] = mfma(wf[s & 1][f], xf[s], acc[f]);
    SB();
  }
  STAMP(1, 2);
  if (kh == 1) {
#pragma unroll
    for (int f = 0; f < 4; ++f) *(f32x4*)(sR + (tb * 4 + f) * 1024 + l * 16) = acc[f];
  }
  __syncthreads();
  if (kh == 0) {
#pragma unroll
    for (int f = 0; f < 4; ++f) acc[f] += *(const f32x4*)(sR + (tb * 4 + f) * 1024 + l * 16);
    char* tw = sT + tb * 16 * EP;
#pragma unroll
    for (int f = 0; f < 4; ++f) {
      u32x2 pk = {pack_bf(acc[f][0], acc[f][1]), pack_bf(acc[f][2], acc[f][3])};
      *(u32x2*)(tw + tl * EP + f * 32 + g * 8) = pk;
    }
    __builtin_amdgcn_wave_barrier();
    asm volatile("" ::: "memory");
#pragma unroll
    for (int i = 0; i < 2; ++i) {
      char* p = tw + (i * 8 + (l >> 3)) * EP + (l & 7) * 16;
      u32x4 hv = add_bf8(res[i], *(const u32x4*)p);
      *(u32x4*)(h + (size_t)(mt * 64 + tb * 16 + i * 8 + (l >> 3)) * D_ + nt * 64 + (l & 7) * 8) = hv;
      *(u32x4*)p = hv;
      float sm = 0.f, sq = 0.f;
#pragma unroll
      for (int e = 0; e < 4; ++e) { sm = dot2(hv[e], ONES, sm); sq = dot2(hv[e], hv[e], sq); }
      sm += __shfl_xor(sm, 1); sq += __shfl_xor(sq, 1);
      sm += __shfl_xor(sm, 2); sq += __shfl_xor(sq, 2);
      sm += __shfl_xor(sm, 4); sq += __shfl_xor(sq, 4);
      if ((l & 7) == 0) {
        float2 st = make_float2(sm, sq);
        *(float2*)(stats + ((size_t)nt * S_ + mt * 64 + tb * 16 + i * 8 + (l >> 3)) * 2) = st;
      }
    }
    __builtin_amdgcn_wave_barrier();
    asm volatile("" ::: "memory");
#pragma unroll
    for (int i = 0; i < 2; ++i) {
      u32x4 v = *(const u32x4*)(tw + tl * EP + (i * 4 + g) * 16);
      *((u32x4*)h_frag + (size_t)((mt * 4 + tb) * 14 + nt * 2 + i) * 64 + l) = v;
    }
  }
  STAMP(1, 3);
}

constexpr int P3_WS = 448 * 128;
__global__ __launch_bounds__(512, 2) void k_gateup(const u16* __restrict__ h_frag, const u16* __restrict__ wg,
                                                   const u16* __restrict__ wu, const float* __restrict__ stats,
                                                   u16* __restrict__ a_frag) {
  __shared__ __attribute__((aligned(16))) char smem[2 * P3_WS + 8 * 16 * 4];
  char* sW = smem;
  float* sCs = (float*)(smem + 2 * P3_WS);
  const int tid = threadIdx.x, l = tid & 63, g = l >> 4, tl = l & 15;
  const int w = __builtin_amdgcn_readfirstlane(tid >> 6);
  const int bid = blockIdx.x, xcd = bid & 7, jj = bid >> 3;
  const int mt = (xcd & 1) * 4 + jj / 7, nt = (xcd >> 1) * 7 + jj % 7;
  STAMP(2, 0);

  const int wc = tid & 7, wk = tid >> 3;
  const u16* sg = wg + (size_t)wk * F_ + nt * 64 + wc * 8;
  const u16* su = wu + (size_t)wk * F_ + nt * 64 + wc * 8;
  const u32x4* xp = (const u32x4*)h_frag + (size_t)((mt * 16 + w * 2) * 14) * 64 + l;
  const int wdst = strip_off(rho_r(wk), wc);

  u32x4 wr[7][2];
  u32x4 xf[2][14];
#pragma unroll
  for (int gi = 0; gi < 2; ++gi) {
    wr[gi][0] = *(const u32x4*)(sg + (size_t)(64 * gi) * F_);
    wr[gi][1] = *(const u32x4*)(su + (size_t)(64 * gi) * F_);
#pragma unroll
    for (int s = 0; s < 2; ++s) { xf[0][gi * 2 + s] = xp[(gi * 2 + s) * 64]; xf[1][gi * 2 + s] = xp[(14 + gi * 2 + s) * 64]; }
  }

  f32x4 acc[2][8];
#pragma unroll
  for (int t = 0; t < 2; ++t)
#pragma unroll
    for (int f = 0; f < 8; ++f) acc[t][f] = (f32x4){0.f, 0.f, 0.f, 0.f};
  int tro[4];
#pragma unroll
  for (int f = 0; f < 4; ++f) tro[f] = tr_lane_off(l, f);
  const char* csbase = sW + (w >> 2) * P3_WS + tr_lane_off(l, w & 3);
  f32x4 csum = {0.f, 0.f, 0.f, 0.f};
  const u32x4 onesf = {ONES, ONES, ONES, ONES};

  *(u32x4*)(sW + wdst) = wr[0][0];
  *(u32x4*)(sW + P3_WS + wdst) = wr[0][1];
  __syncthreads();
  STAMP(2, 4);
  u32x4 wf[2][9];
#define P3_LOAD(B, S) do { \
    _Pragma("unroll") for (int f = 0; f < 8; ++f) wf[B][f] = wfrag(sW + (f >> 2) * P3_WS + (S) * 32 * 128 + tro[f & 3]); \
    wf[B][8] = wfrag(csbase + (S) * 32 * 128); \
  } while (0)
  P3_LOAD(0, 0);
  float2 st[2][2];
#pragma unroll
  for (int ii = 0; ii < 7; ++ii) {
    if (ii + 1 < 7) {
      *(u32x4*)(sW + (ii + 1) * 64 * 128 + wdst) = wr[ii + 1][0];
      *(u32x4*)(sW + P3_WS + (ii + 1) * 64 * 128 + wdst) = wr[ii + 1][1];
      __syncthreads();
    }
#pragma unroll
    for (int s2 = 0; s2 < 2; ++s2) {
      const int s = ii * 2 + s2;
      if (s + 1 < 14) P3_LOAD((s + 1) & 1, s + 1);
      SB();
#pragma unroll
      for (int q = 0; q < 4; ++q) {
        if (ii == 0) {
          const int gi = 2 + s2;
          if (q == 0) { wr[gi][0] = *(const u32x4*)(sg + (size_t)(64 * gi) * F_); wr[gi][1] = *(const u32x4*)(su + (size_t)(64 * gi) * F_); }
          if (q == 1) { xf[0][gi * 2] = xp[(gi * 2) * 64]; xf[1][gi * 2] = xp[(14 + gi * 2) * 64]; }
          if (q == 2) { xf[0][gi * 2 + 1] = xp[(gi * 2 + 1) * 64]; xf[1][gi * 2 + 1] = xp[(14 + gi * 2 + 1) * 64]; }
        } else if (ii + 3 < 7) {
          const int gi = ii + 3;
          if (q == 0) wr[gi][s2] = *(const u32x4*)((s2 ? su : sg) + (size_t)(64 * gi) * F_);
          if (q == 1) xf[0][gi * 2 + s2] = xp[(gi * 2 + s2) * 64];
          if (q == 2) xf[1][gi * 2 + s2] = xp[(14 + gi * 2 + s2) * 64];
        } else if (ii == 4) {
          if (q < 2) st[s2][q] = *(const float2*)(stats + ((size_t)min(g + 4 * q, 6) * S_ + mt * 256 + (w * 2 + s2) * 16 + tl) * 2);
        }
#pragma unroll
        for (int f = q * 2; f < q * 2 + 2; ++f) {
          acc[0][f] = mfma(wf[s & 1][f], xf[0][s], acc[0][f]);
          acc[1][f] = mfma(wf[s & 1][f], xf[1][s], acc[1][f]);
        }
        if (q == 3) csum = mfma(wf[s & 1][8], onesf, csum);
        SB();
      }
    }
    STAMP(2, 5 + ii);
  }
  STAMP(2, 1);
  if (tl == 0) *(f32x4*)(sCs + w * 16 + 4 * g) = csum;
  float mb[2], rstd[2];
#pragma unroll
  for (int t = 0; t < 2; ++t) {
    float a = st[t][0].x + (g < 3 ? st[t][1].x : 0.f), b = st[t][0].y + (g < 3 ? st[t][1].y : 0.f);
    a += __shfl_xor(a, 16); b += __shfl_xor(b, 16);
    a += __shfl_xor(a, 32); b += __shfl_xor(b, 32);
    ln_params(a, b, mb[t], rstd[t]);
  }
  __syncthreads();
  STAMP(2, 2);
#pragma unroll
  for (int fp = 0; fp < 2; ++fp) {
    f32x4 cg[2], cu[2];
#pragma unroll
    for (int i = 0; i < 2; ++i) {
      cg[i] = *(const f32x4*)(sCs + (fp * 2 + i) * 16 + 4 * g);
      cu[i] = *(const f32x4*)(sCs + (4 + fp * 2 + i) * 16 + 4 * g);
    }
#pragma unroll
    for (int t = 0; t < 2; ++t) {
      u32x4 out;
#pragma unroll
      for (int i = 0; i < 2; ++i) {
        float av[4];
#pragma unroll
        for (int r = 0; r < 4; ++r) {
          float gv = rbf((acc[t][fp * 2 + i][r] - mb[t] * cg[i][r]) * rstd[t]);
          float uv = rbf((acc[t][4 + fp * 2 + i][r] - mb[t] * cu[i][r]) * rstd[t]);
          float sl = rbf(gv * __builtin_amdgcn_rcpf(1.0f + __expf(-gv)));
          av[r] = sl * uv;
        }
        out[i * 2] = pack_bf(av[0], av[1]);
        out[i * 2 + 1] = pack_bf(av[2], av[3]);
      }
      const int tbg = mt * 16 + w * 2 + t;
      *((u32x4*)a_frag + (size_t)(tbg * 56 + nt * 2 + fp) * 64 + l) = out;
    }
  }
  STAMP(2, 3);
}

constexpr int P4_SLOT = 128 * 128;
constexpr int P4_D = 4;
__global__ __launch_bounds__(512, 2) void k_down(const u16* __restrict__ a_frag, const u16* __restrict__ wd,
                                                 const u16* __restrict__ h, u16* __restrict__ out) {
  __shared__ __attribute__((aligned(16))) char smem[3 * P4_SLOT + 64 * EP + 4 * 4 * 1024];
  char* sW = smem;
  char* sT = smem + 3 * P4_SLOT;
  char* sR = smem + 3 * P4_SLOT + 64 * EP;
  const int tid = threadIdx.x, l = tid & 63, g = l >> 4, tl = l & 15;
  const int w = __builtin_amdgcn_readfirstlane(tid >> 6);
  const int tb = w & 3, kh = w >> 2;
  const int bid = blockIdx.x, xcd = bid & 7, jj = bid >> 3;
  const int mt = xcd * 4 + jj / 7, nt = jj % 7;
  const int wc = tid & 7, wk = tid >> 3;
  const u16* wsrc = wd + (size_t)wk * D_ + nt * 64 + wc * 8;
  const u32x4* xp = (const u32x4*)a_frag + (size_t)((mt * 4 + tb) * 56 + kh * 2) * 64 + l;
  const int wdst = strip_off(wk, wc);
  int tro[4];
#pragma unroll
  for (int f = 0; f < 4; ++f) tro[f] = tr_lane_off(l, f) + kh * 2 * 32 * 128;
  STAMP(3, 0);

  u32x4 wr[P4_D][2];
  u32x4 xr[P4_D][2];
#define P4_GW(IT, J) wr[(IT) % P4_D][J] = *(const u32x4*)(wsrc + (size_t)(128 * (IT) + 64 * (J)) * D_)
#define P4_GX(IT, J) xr[(IT) % P4_D][J] = xp[((IT) * 4 + (J)) * 64]
#pragma unroll
  for (int p = 0; p < P4_D - 1; ++p) {
    P4_GW(p, 0); P4_GW(p, 1); P4_GX(p, 0); P4_GX(p, 1);
  }
  f32x4 acc[4];
#pragma unroll
  for (int f = 0; f < 4; ++f) acc[f] = (f32x4){0.f, 0.f, 0.f, 0.f};

  *(u32x4*)(sW + wdst) = wr[0][0];
  *(u32x4*)(sW + 64 * 128 + wdst) = wr[0][1];
  __syncthreads();
  STAMP(3, 3);
  u32x4 wf[2][4];
#define P4_LOAD(KK) do { \
    const char* b_ = sW + (((KK) >> 1) % 3) * P4_SLOT + ((KK) & 1) * 32 * 128; \
    _Pragma("unroll") for (int f = 0; f < 4; ++f) wf[(KK) & 1][f] = wfrag(b_ + tro[f]); \
  } while (0)
  P4_LOAD(0);
  u32x4 res[2];
#pragma unroll
  for (int it = 0; it < 14; ++it) {
    if (it + 1 < 14) {
      char* buf = sW + ((it + 1) % 3) * P4_SLOT + wdst;
      *(u32x4*)(buf) = wr[(it + 1) % P4_D][0];
      *(u32x4*)(buf + 64 * 128) = wr[(it + 1) % P4_D][1];
      __syncthreads();
    }
#pragma unroll
    for (int j = 0; j < 2; ++j) {
      const int kk = it * 2 + j;
      if (kk + 1 < 28) P4_LOAD(kk + 1);
      SB();
      if (it + P4_D - 1 < 14) P4_GW(it + P4_D - 1, j);
      acc[0] = mfma(wf[kk & 1][0], xr[it % P4_D][j], acc[0]);
      acc[1] = mfma(wf[kk & 1][1], xr[it % P4_D][j], acc[1]);
      SB();
      if (it + P4_D - 1 < 14) P4_GX(it + P4_D - 1, j);
      else if (it == 12 && kh == 0)
        res[j] = *(const u32x4*)(h + (size_t)(mt * 64 + tb * 16 + j * 8 + (l >> 3)) * D_ + nt * 64 + (l & 7) * 8);
      acc[2] = mfma(wf[kk & 1][2], xr[it % P4_D][j], acc[2]);
      acc[3] = mfma(wf[kk & 1][3], xr[it % P4_D][j], acc[3]);
      SB();
    }
  }
  STAMP(3, 1);
  if (kh == 1) {
#pragma unroll
    for (int f = 0; f < 4; ++f) *(f32x4*)(sR + (tb * 4 + f) * 1024 + l * 16) = acc[f];
  }
  __syncthreads();
  if (kh == 0) {
#pragma unroll
    for (int f = 0; f < 4; ++f) acc[f] += *(const f32x4*)(sR + (tb * 4 + f) * 1024 + l * 16);
    char* tw = sT + tb * 16 * EP;
#pragma unroll
    for (int f = 0; f < 4; ++f) {
      u32x2 pk = {pack_bf(acc[f][0], acc[f][1]), pack_bf(acc[f][2], acc[f][3])};
      *(u32x2*)(tw + tl * EP + f * 32 + g * 8) = pk;
    }
    __builtin_amdgcn_wave_barrier();
    asm volatile("" ::: "memory");
#pragma unroll
    for (int i = 0; i < 2; ++i) {
      const char* p = tw + (i * 8 + (l >> 3)) * EP + (l & 7) * 16;
      u32x4 hv = add_bf8(res[i], *(const u32x4*)p);
      *(u32x4*)(out + (size_t)(mt * 64 + tb * 16 + i * 8 + (l >> 3)) * D_ + nt * 64 + (l & 7) * 8) = hv;
    }
  }
  STAMP(3, 2);
}

extern "C" void kernel_launch(void* const* d_in, const int* in_sizes, int n_in, void* d_out, int out_size,
                              void* d_ws, size_t ws_size, hipStream_t stream) {
  const u16* x = (const u16*)d_in[0];
  const u16* wqkv = (const u16*)d_in[1];
  const u16* wo = (const u16*)d_in[2];
  const u16* wg = (const u16*)d_in[4];
  const u16* wu = (const u16*)d_in[5];
  const u16* wd = (const u16*)d_in[6];
  u16* out = (u16*)d_out;
  char* ws = (char*)d_ws;
  u16* o_frag = (u16*)ws;
  u16* h = (u16*)(ws + (2u << 20));
  u16* h_frag = (u16*)(ws + (4u << 20));
  u16* a_frag = (u16*)(ws + (6u << 20));
  float* stats = (float*)(ws + (14u << 20));
  k_qkv_attn<<<256, 512, 0, stream>>>(x, wqkv, o_frag);
  k_wo<<<224, 512, 0, stream>>>(o_frag, wo, x, h, h_frag, stats);
  k_gateup<<<224, 512, 0, stream>>>(h_frag, wg, wu, stats, a_frag);
  k_down<<<224, 512, 0, stream>>>(a_frag, wd, h, out);
}
```

```cpp
#include <hip/hip_runtime.h>
#include <hip/hip_bf16.h>
#include <stdint.h>

typedef unsigned short u16;
typedef __attribute__((ext_vector_type(8))) __bf16 bf16x8;
typedef __attribute__((ext_vector_type(4))) short s16x4;
typedef __attribute__((ext_vector_type(4))) float f32x4;
typedef __attribute__((ext_vector_type(4))) unsigned int u32x4;
typedef __attribute__((ext_vector_type(2))) unsigned int u32x2;
typedef __attribute__((ext_vector_type(2))) __bf16 bf16x2;
#define LDS_AS __attribute__((address_space(3)))
#define SB() __builtin_amdgcn_sched_barrier(0)
#define GLDS(gp, lp) __builtin_amdgcn_global_load_lds((const unsigned*)(gp), (unsigned*)(lp), 16, 0, 0)
#define WAITV(n) asm volatile("s_waitcnt vmcnt(" #n ")" ::: "memory")
#define BAR() do { asm volatile("s_waitcnt lgkmcnt(0)" ::: "memory"); __builtin_amdgcn_s_barrier(); asm volatile("" ::: "memory"); } while (0)

#define STAMP(K,I)

constexpr int S_ = 2048, D_ = 448, F_ = 1792, QKV_ = 1344;
constexpr unsigned ONES = 0x3F803F80u;

__device__ __attribute__((aligned(16))) const unsigned g_const[12] = {ONES, ONES, ONES, ONES, 0x3F80u, 0u, 0u, 0u, 0u, 0u, 0u, 0u};

__device__ __forceinline__ float bf_lo(unsigned u) { return __uint_as_float(u << 16); }
__device__ __forceinline__ float bf_hi(unsigned u) { return __uint_as_float(u & 0xffff0000u); }
__device__ __forceinline__ unsigned pack_bf(float a, float b) {
  bf16x2 v = {(__bf16)a, (__bf16)b};
  return __builtin_bit_cast(unsigned, v);
}
__device__ __forceinline__ float rbf(float a) { return (float)(__bf16)a; }
__device__ __forceinline__ float dot2(unsigned a, unsigned b, float c) {
  return __builtin_amdgcn_fdot2_f32_bf16(__builtin_bit_cast(bf16x2, a), __builtin_bit_cast(bf16x2, b), c, false);
}
__device__ __forceinline__ u32x2 tr_read(const char* p) {
  s16x4 v = __builtin_amdgcn_ds_read_tr16_b64_v4i16((LDS_AS s16x4*)p);
  return __builtin_bit_cast(u32x2, v);
}
__device__ __forceinline__ u32x4 wfrag(const char* p) {
  u32x2 a = tr_read(p), b = tr_read(p + 16 * 128);
  u32x4 r = {a[0], a[1], b[0], b[1]};
  return r;
}
__device__ __forceinline__ f32x4 mfma(u32x4 a, u32x4 b, f32x4 c) {
  return __builtin_amdgcn_mfma_f32_16x16x32_bf16(__builtin_bit_cast(bf16x8, a), __builtin_bit_cast(bf16x8, b), c, 0, 0, 0);
}
__device__ __forceinline__ int rho_inv(int r) { return (r & ~31) | ((r & 12) << 1) | ((r & 16) >> 2) | (r & 3); }
__device__ __forceinline__ int tr_lane_off(int l, int nb) {
  int g = l >> 4, q = (l & 15) >> 2, p = l & 3;
  int swz = 2 * (g & 1) + (q >> 1);
  return (4 * g + q) * 128 + ((nb ^ swz) << 5) + p * 8;
}
__device__ __forceinline__ void ln_params(float sum, float sq, float& mb, float& rstd) {
  float mean = sum * (1.0f / D_);
  float var = fmaxf(sq * (1.0f / D_) - mean * mean, 0.0f);
  mb = rbf(mean);
  float den = rbf(sqrtf(rbf(rbf(var) + 1e-5f)));
  rstd = 1.0f / den;
}
__device__ __forceinline__ u32x4 add_bf8(u32x4 a, u32x4 b) {
  u32x4 r;
#pragma unroll
  for (int e = 0; e < 4; ++e) r[e] = pack_bf(bf_lo(a[e]) + bf_lo(b[e]), bf_hi(a[e]) + bf_hi(b[e]));
  return r;
}

constexpr int P1_WG = 64 * 128;
constexpr int P1_XO = 3 * P1_WG;
constexpr int P1_SLOT = 3 * P1_WG + 80 * 128;
constexpr int P1_R = 4;
constexpr int P1_SP = 672;
constexpr int P1_SMEM = P1_R * P1_SLOT + 2048 + (80 * 2 + 3 * 64) * 4;

__global__ __launch_bounds__(768, 3) void k_qkv_attn(const u16* __restrict__ x, const u16* __restrict__ wqkv,
                                                     u16* __restrict__ o_frag) {
  __shared__ __attribute__((aligned(16))) char smem[P1_SMEM];
  float* sSum = (float*)(smem + P1_R * P1_SLOT + 2048);
  float* sSq = sSum + 80;
  float* sCs = sSq + 80;

  const int tid = threadIdx.x, l = tid & 63, g = l >> 4, tl = l & 15;
  const int w = __builtin_amdgcn_readfirstlane(tid >> 6);
  const int bid = blockIdx.x, xcd = bid & 7, jj = bid >> 3;
  const int mt = xcd * 4 + (jj >> 3), hp = jj & 7;
  const int row0 = mt * 64 - 16;
  STAMP(0, 0);

  if (w >= 8) {
    const int lw = w - 8, lr = l >> 3, lc = l & 7;
    const u16* src[9];
    int dst[9], adv[9];
#pragma unroll
    for (int j = 0; j < 9; ++j) {
      const int pi = lw + 4 * j;
      if (pi < 24) {
        const int m = pi >> 3, rho = (pi & 7) * 8 + lr;
        const int c = lc ^ (((rho >> 1) & 3) << 1);
        if (c < 7) { src[j] = wqkv + (size_t)rho_inv(rho) * QKV_ + m * D_ + hp * 56 + c * 8; adv[j] = 64 * QKV_; }
        else { src[j] = (const u16*)(g_const + (m == 1 ? 4 : 8)); adv[j] = 0; }
        dst[j] = pi * 1024;
      } else if (pi < 34) {
        const int r = (pi - 24) * 8 + lr;
        const int c = lc ^ ((r >> 1) & 7);
        if (r > 0) { src[j] = x + (size_t)max(row0 + r, 0) * D_ + c * 8; adv[j] = 64; }
        else { src[j] = (const u16*)g_const; adv[j] = 0; }
        dst[j] = pi * 1024;
      } else { src[j] = (const u16*)g_const; adv[j] = 0; dst[j] = -1; }
    }
#define P1_ISSUE(ST) do { \
      char* sl_ = smem + ((ST) % P1_R) * P1_SLOT; \
      _Pragma("unroll") for (int j = 0; j < 9; ++j) { \
        GLDS(src[j] + (size_t)(ST) * adv[j], (dst[j] >= 0) ? sl_ + dst[j] : smem + P1_R * P1_SLOT + (lw & 1) * 1024); } \
    } while (0)
    P1_ISSUE(0); P1_ISSUE(1); P1_ISSUE(2);
    WAITV(9);  BAR();
    P1_ISSUE(3);
    WAITV(9);  BAR();
    P1_ISSUE(4);
    WAITV(9);  BAR();
    P1_ISSUE(5);
    WAITV(9);  BAR();
    P1_ISSUE(6);
    WAITV(9);  BAR();
    WAITV(0);  BAR();
    BAR();
    BAR(); BAR();
    return;
  }

  const int fb = w & 3, hf = w >> 2;
  int xo[3][2];
#pragma unroll
  for (int t = 0; t < 3; ++t) {
    const int tb = (t < 2) ? hf * 3 + t : (hf ? fb : 2);
    const int r = tb * 16 + tl;
#pragma unroll
    for (int s2 = 0; s2 < 2; ++s2) xo[t][s2] = P1_XO + r * 128 + (((4 * s2 + g) ^ ((r >> 1) & 7)) << 4);
  }
  f32x4 acc[3][3];
#pragma unroll
  for (int m = 0; m < 3; ++m)
#pragma unroll
    for (int t = 0; t < 3; ++t) acc[m][t] = (f32x4){0.f, 0.f, 0.f, 0.f};
  const int troff = tr_lane_off(l, fb);

  u32x4 xf[2][3], wf[2][3];
#define P1_LOAD(B, S) do { \
    const char* slot_ = smem + (((S) >> 1) % P1_R) * P1_SLOT; \
    _Pragma("unroll") for (int m = 0; m < 3; ++m) wf[B][m] = wfrag(slot_ + m * P1_WG + ((S) & 1) * 32 * 128 + troff); \
    _Pragma("unroll") for (int t = 0; t < 3; ++t) xf[B][t] = *(const u32x4*)(slot_ + xo[t][(S) & 1]); \
  } while (0)
#pragma unroll
  for (int ii = 0; ii < 7; ++ii) {
    BAR();
    if (ii == 0) { STAMP(0, 1); P1_LOAD(0, 0); }
#pragma unroll
    for (int s2 = 0; s2 < 2; ++s2) {
      const int s = ii * 2 + s2;
      if (s + 1 < 14) P1_LOAD((s + 1) & 1, s + 1);
      SB();
#pragma unroll
      for (int m = 0; m < 3; ++m) {
        acc[m][0] = mfma(wf[s & 1][m], xf[s & 1][0], acc[m][0]);
        acc[m][1] = mfma(wf[s & 1][m], xf[s & 1][1], acc[m][1]);
        if (hf == 0) acc[m][2] = mfma(wf[s & 1][m], xf[s & 1][2], acc[m][2]);
        else if (m == 0) acc[0][2] = mfma(xf[s & 1][2], xf[s & 1][2], acc[0][2]);
        else if (m == 1) acc[1][2] = mfma(xf[s & 1][1], xf[s & 1][1], acc[1][2]);
      }
      SB();
    }
  }
  STAMP(0, 2);
  if (fb == 3 && g == 2) {
#pragma unroll
    for (int t = 0; t < 3; ++t)
      if (hf == 0 || t < 2) sSum[(hf * 3 + t) * 16 + tl] = acc[1][t][0];
  }
  if (hf == 1 && g == (tl >> 2)) {
    float a0 = acc[0][2][0], a1 = acc[1][2][0];
#pragma unroll
    for (int r = 1; r < 4; ++r) { if ((tl & 3) == r) { a0 = acc[0][2][r]; a1 = acc[1][2][r]; } }
    sSq[fb * 16 + tl] = a0;
    if (fb == 0) sSq[64 + tl] = a1;
  }
  if (hf == 0 && tl == 0) {
#pragma unroll
    for (int m = 0; m < 3; ++m) *(f32x4*)(sCs + m * 64 + fb * 16 + 4 * g) = acc[m][0];
  }
  BAR();
  char* sS = smem;
  {
    f32x4 csf[3];
#pragma unroll
    for (int m = 0; m < 3; ++m) csf[m] = *(const f32x4*)(sCs + m * 64 + fb * 16 + 4 * g);
    const int quad = 4 * fb + g;
#pragma unroll
    for (int t = 0; t < 3; ++t) {
      if (hf == 0 || t < 2) {
        const int tb = hf * 3 + t;
        float mb, rstd;
        ln_params(sSum[tb * 16 + tl], sSq[tb * 16 + tl], mb, rstd);
#pragma unroll
        for (int m = 0; m < 3; ++m) {
          float v0 = (acc[m][t][0] - mb * csf[m][0]) * rstd;
          float v1 = (acc[m][t][1] - mb * csf[m][1]) * rstd;
          float v2 = (acc[m][t][2] - mb * csf[m][2]) * rstd;
          float v3 = (acc[m][t][3] - mb * csf[m][3]) * rstd;
          u32x2 pk = {pack_bf(v0, v1), pack_bf(v2, v3)};
          if (quad < 14) *(u32x2*)(sS + (m * 14 + quad) * P1_SP + (tb * 16 + tl) * 8) = pk;
        }
      }
    }
  }
  BAR();
  STAMP(0, 3);

  {
    const int hh = l >> 5, part = (l >> 3) & 3;
    const int t = w * 8 + (l & 7);
    const int tok = mt * 64 + t;
    const int q0 = hh * 7 + part * 2;
    const int q1 = hh * 7 + min(part * 2 + 1, 6);
    const bool two = part < 3;
    u32x2 qv0 = *(const u32x2*)(sS + (0 * 14 + q0) * P1_SP + (16 + t) * 8);
    u32x2 qv1 = *(const u32x2*)(sS + (0 * 14 + q1) * P1_SP + (16 + t) * 8);
    if (!two) qv1 = (u32x2){0u, 0u};
    float lg[8];
#pragma unroll
    for (int ww = 0; ww < 8; ++ww) {
      u32x2 k0 = *(const u32x2*)(sS + (1 * 14 + q0) * P1_SP + (16 + t - 2 * ww) * 8);
      u32x2 k1 = *(const u32x2*)(sS + (1 * 14 + q1) * P1_SP + (16 + t - 2 * ww) * 8);
      float sacc = 0.f;
      sacc = dot2(qv0[0], k0[0], sacc); sacc = dot2(qv0[1], k0[1], sacc);
      sacc = dot2(qv1[0], k1[0], sacc); sacc = dot2(qv1[1], k1[1], sacc);
      lg[ww] = sacc;
    }
    const float scale = 0.1889822365046136f;
    float mx = -3.0e38f;
#pragma unroll
    for (int ww = 0; ww < 8; ++ww) {
      float sacc = lg[ww] + __shfl_xor(lg[ww], 8);
      sacc += __shfl_xor(sacc, 16);
      float v = rbf(rbf(sacc) * scale);
      v = (tok - 2 * ww >= 0) ? v : -1e9f;
      lg[ww] = v;
      mx = fmaxf(mx, v);
    }
    float den = 0.f;
#pragma unroll
    for (int ww = 0; ww < 8; ++ww) { lg[ww] = __expf(lg[ww] - mx); den += lg[ww]; }
    float inv = 1.0f / den;
#pragma unroll
    for (int ww = 0; ww < 8; ++ww) lg[ww] = rbf(lg[ww] * inv);
    float oacc[2][4];
#pragma unroll
    for (int i = 0; i < 2; ++i)
#pragma unroll
      for (int e = 0; e < 4; ++e) oacc[i][e] = 0.f;
#pragma unroll
    for (int ww = 0; ww < 8; ++ww) {
#pragma unroll
      for (int i = 0; i < 2; ++i) {
        u32x2 vv = *(const u32x2*)(sS + (2 * 14 + (i ? q1 : q0)) * P1_SP + (16 + t - 2 * ww) * 8);
        oacc[i][0] = fmaf(lg[ww], bf_lo(vv[0]), oacc[i][0]);
        oacc[i][1] = fmaf(lg[ww], bf_hi(vv[0]), oacc[i][1]);
        oacc[i][2] = fmaf(lg[ww], bf_lo(vv[1]), oacc[i][2]);
        oacc[i][3] = fmaf(lg[ww], bf_hi(vv[1]), oacc[i][3]);
      }
    }
    const int tbg = tok >> 4;
#pragma unroll
    for (int i = 0; i < 2; ++i) {
      if (i == 0 || two) {
        int f = hp * 56 + (q0 + i) * 4;
        int s = f >> 5, ii = (f >> 4) & 1, gg = (f >> 2) & 3;
        u32x2 pk = {pack_bf(oacc[i][0], oacc[i][1]), pack_bf(oacc[i][2], oacc[i][3])};
        *(u32x2*)((char*)o_frag + ((size_t)((tbg * 14 + s) * 64 + gg * 16 + (tok & 15))) * 16 + ii * 8) = pk;
      }
    }
  }
  STAMP(0, 4);
}

constexpr int EP = 144;
constexpr int G_SLOT = 16384;
template <int NST, int R, bool IS_P2>
__global__ __launch_bounds__(768, 3) void k_gemm64(const u16* __restrict__ a_frag, const u16* __restrict__ wmat,
                                                   const u16* __restrict__ res, u16* __restrict__ dst,
                                                   u16* __restrict__ h_frag, float* __restrict__ stats) {
  __shared__ __attribute__((aligned(16))) char smem[R * G_SLOT + 64 * EP];
  char* sT = smem + R * G_SLOT;
  const int tid = threadIdx.x, l = tid & 63, g = l >> 4, tl = l & 15;
  const int w = __builtin_amdgcn_readfirstlane(tid >> 6);
  const int bid = blockIdx.x, xcd = bid & 7, jj = bid >> 3;
  const int mt = xcd * 4 + jj / 7, nt = jj % 7;
  constexpr int KS = NST * 2;
  STAMP(IS_P2 ? 1 : 3, 0);

  if (w >= 8) {
    const int lw = w - 8, lr = l >> 3, lc = l & 7;
    const u16* ws[2];
#pragma unroll
    for (int j = 0; j < 2; ++j) {
      const int rho = (lw + 4 * j) * 8 + lr;
      const int c = lc ^ (((rho >> 1) & 3) << 1);
      ws[j] = wmat + (size_t)rho * D_ + nt * 64 + c * 8;
    }
    const u16* xs = a_frag + ((size_t)((mt * 4 + lw) * KS) * 64 + l) * 8;
#define G_ISSUE(ST) do { \
      char* sl_ = smem + ((ST) % R) * G_SLOT; \
      GLDS(ws[0] + (size_t)(ST) * 64 * D_, sl_ + lw * 1024); \
      GLDS(ws[1] + (size_t)(ST) * 64 * D_, sl_ + (lw + 4) * 1024); \
      GLDS(xs + (size_t)(ST) * 1024, sl_ + 8192 + lw * 1024); \
      GLDS(xs + (size_t)(ST) * 1024 + 512, sl_ + 8192 + (4 + lw) * 1024); \
    } while (0)
    constexpr int PRE = (R - 1 < NST) ? R - 1 : NST;
#pragma unroll
    for (int st = 0; st < PRE; ++st) G_ISSUE(st);
#pragma unroll
    for (int i = 0; i < NST; ++i) {
      const int issued = (i + R - 2 < NST - 1) ? i + R - 2 : NST - 1;
      const int need = (i + 1 < NST) ? i + 1 : NST - 1;
      const int allow = (issued - need) * 4;
      switch (allow) {
        case 0: WAITV(0); break; case 4: WAITV(4); break; case 8: WAITV(8); break; case 12: WAITV(12); break;
        case 16: WAITV(16); break; case 20: WAITV(20); break; case 24: WAITV(24); break; case 28: WAITV(28); break;
        default: WAITV(0); break;
      }
      BAR();
      if (i + R - 1 < NST) G_ISSUE(i + R - 1);
    }
    BAR();
    if (IS_P2) BAR();
    return;
  }

  const int tb = w & 3, fh = w >> 2;
  const int tro0 = tr_lane_off(l, fh * 2), tro1 = tr_lane_off(l, fh * 2 + 1);
  const int xoff = 8192 + tb * 1024 + l * 16;
  const size_t rowoff = (size_t)(mt * 64 + tb * 16 + fh * 8 + (l >> 3)) * D_ + nt * 64 + (l & 7) * 8;
  const u32x4 rv = *(const u32x4*)(res + rowoff);
  f32x4 acc[2] = {(f32x4){0.f, 0.f, 0.f, 0.f}, (f32x4){0.f, 0.f, 0.f, 0.f}};
  u32x4 wf[2][2], xf[2];
#define G_LOAD(K) do { \
    const char* sl_ = smem + (((K) >> 1) % R) * G_SLOT; \
    wf[(K) & 1][0] = wfrag(sl_ + ((K) & 1) * 4096 + tro0); \
    wf[(K) & 1][1] = wfrag(sl_ + ((K) & 1) * 4096 + tro1); \
    xf[(K) & 1] = *(const u32x4*)(sl_ + ((K) & 1) * 4096 + xoff); \
  } while (0)
#pragma unroll
  for (int i = 0; i < NST; ++i) {
    BAR();
    if (i == 0) { STAMP(IS_P2 ? 1 : 3, 1); G_LOAD(0); }
#pragma unroll
    for (int j = 0; j < 2; ++j) {
      const int k = i * 2 + j;
      if (k + 1 < KS) G_LOAD(k + 1);
      SB();
      acc[0] = mfma(wf[k & 1][0], xf[k & 1], acc[0]);
      acc[1] = mfma(wf[k & 1][1], xf[k & 1], acc[1]);
      SB();
    }
  }
  STAMP(IS_P2 ? 1 : 3, 2);
  char* tw = sT + tb * 16 * EP;
#pragma unroll
  for (int f = 0; f < 2; ++f) {
    u32x2 pk = {pack_bf(acc[f][0], acc[f][1]), pack_bf(acc[f][2], acc[f][3])};
    *(u32x2*)(tw + tl * EP + (fh * 2 + f) * 32 + g * 8) = pk;
  }
  BAR();
  {
    char* p = tw + (fh * 8 + (l >> 3)) * EP + (l & 7) * 16;
    u32x4 hv = add_bf8(rv, *(const u32x4*)p);
    *(u32x4*)(dst + rowoff) = hv;
    if (IS_P2) {
      *(u32x4*)p = hv;
      float sm = 0.f, sq = 0.f;
#pragma unroll
      for (int e = 0; e < 4; ++e) { sm = dot2(hv[e], ONES, sm); sq = dot2(hv[e], hv[e], sq); }
      sm += __shfl_xor(sm, 1); sq += __shfl_xor(sq, 1);
      sm += __shfl_xor(sm, 2); sq += __shfl_xor(sq, 2);
      sm += __shfl_xor(sm, 4); sq += __shfl_xor(sq, 4);
      if ((l & 7) == 0) {
        float2 st = make_float2(sm, sq);
        *(float2*)(stats + ((size_t)nt * S_ + mt * 64 + tb * 16 + fh * 8 + (l >> 3)) * 2) = st;
      }
    }
  }
  if (IS_P2) {
    BAR();
    u32x4 v = *(const u32x4*)(tw + tl * EP + (fh * 4 + g) * 16);
    *((u32x4*)h_frag + (size_t)((mt * 4 + tb) * 14 + nt * 2 + fh) * 64 + l) = v;
  }
  STAMP(IS_P2 ? 1 : 3, 3);
}

constexpr int P3_SLOT = 24576;
constexpr int P3_R = 6;
__global__ __launch_bounds__(768, 3) void k_gateup(const u16* __restrict__ h_frag, const u16* __restrict__ wg,
                                                   const u16* __restrict__ wu, const float* __restrict__ stats,
                                                   u16* __restrict__ a_frag) {
  __shared__ __attribute__((aligned(16))) char smem[P3_R * P3_SLOT + 8 * 16 * 4];
  float* sCs = (float*)(smem + P3_R * P3_SLOT);
  const int tid = threadIdx.x, l = tid & 63, g = l >> 4, tl = l & 15;
  const int w = __builtin_amdgcn_readfirstlane(tid >> 6);
  const int bid = blockIdx.x, xcd = bid & 7, jj = bid >> 3;
  const int mt = (xcd & 1) * 4 + jj / 7, nt = (xcd >> 1) * 7 + jj % 7;
  STAMP(2, 0);

  if (w >= 8) {
    const int lw = w - 8, lr = l >> 3, lc = l & 7;
    const int rho = lw * 8 + lr;
    const int c = lc ^ (((rho >> 1) & 3) << 1);
    const u16* gs = wg + (size_t)rho_inv(rho) * F_ + nt * 64 + c * 8;
    const u16* us = wu + (size_t)rho_inv(rho) * F_ + nt * 64 + c * 8;
    const u16* xs = h_frag + ((size_t)((mt * 16 + lw * 4) * 14) * 64 + l) * 8;
#define P3_ISSUE(ST) do { \
      char* sl_ = smem + ((ST) % P3_R) * P3_SLOT; \
      GLDS(gs + (size_t)(ST) * 32 * F_, sl_ + lw * 1024); \
      GLDS(us + (size_t)(ST) * 32 * F_, sl_ + 4096 + lw * 1024); \
      _Pragma("unroll") for (int j = 0; j < 4; ++j) GLDS(xs + (size_t)(j * 14 + (ST)) * 512, sl_ + 8192 + (lw * 4 + j) * 1024); \
    } while (0)
#pragma unroll
    for (int st = 0; st < P3_R - 1; ++st) P3_ISSUE(st);
#pragma unroll
    for (int i = 0; i < 14; ++i) {
      const int issued = (i + P3_R - 2 < 13) ? i + P3_R - 2 : 13;
      const int need = (i + 1 < 14) ? i + 1 : 13;
      const int allow = (issued - need) * 6;
      switch (allow) {
        case 0: WAITV(0); break; case 6: WAITV(6); break; case 12: WAITV(12); break; case 18: WAITV(18); break;
        default: WAITV(0); break;
      }
      BAR();
      if (i + P3_R - 1 < 14) P3_ISSUE(i + P3_R - 1);
    }
    BAR();
    return;
  }

  float2 st[2][2];
  f32x4 acc[2][8];
#pragma unroll
  for (int t = 0; t < 2; ++t)
#pragma unroll
    for (int f = 0; f < 8; ++f) acc[t][f] = (f32x4){0.f, 0.f, 0.f, 0.f};
  int tro[4];
#pragma unroll
  for (int f = 0; f < 4; ++f) tro[f] = tr_lane_off(l, f);
  const int csoff = (w >> 2) * 4096 + tr_lane_off(l, w & 3);
  const int xoff = 8192 + w * 2048 + l * 16;
  f32x4 csum = {0.f, 0.f, 0.f, 0.f};
  const u32x4 onesf = {ONES, ONES, ONES, ONES};

  u32x4 wf[2][4], wcs, xf[2][2];
#define P3_LOADH(HS) do { \
    int so_ = (((HS) >> 1) % P3_R) * P3_SLOT; asm volatile("" : "+s"(so_)); \
    const char* sl_ = smem + so_; \
    _Pragma("unroll") for (int f = 0; f < 4; ++f) wf[(HS) & 1][f] = wfrag(sl_ + ((HS) & 1) * 4096 + tro[f]); \
    if (((HS) & 1) == 0) { xf[((HS) >> 1) & 1][0] = *(const u32x4*)(sl_ + xoff); xf[((HS) >> 1) & 1][1] = *(const u32x4*)(sl_ + xoff + 1024); } \
  } while (0)
#pragma unroll
  for (int s = 0; s < 14; ++s) {
    BAR();
    if (s == 0) { STAMP(2, 4); P3_LOADH(0); }
#pragma unroll
    for (int hb = 0; hb < 2; ++hb) {
      const int hs = s * 2 + hb;
      if (hs + 1 < 28) P3_LOADH(hs + 1);
      if (hb == 0) { int so_ = (s % P3_R) * P3_SLOT; asm volatile("" : "+s"(so_)); wcs = wfrag(smem + so_ + csoff); }
      if (s == 11) {
#pragma unroll
        for (int i = 0; i < 2; ++i)
          st[hb][i] = *(const float2*)(stats + ((size_t)min(g + 4 * i, 6) * S_ + mt * 256 + (w * 2 + hb) * 16 + tl) * 2);
      }
      SB();
#pragma unroll
      for (int f = 0; f < 4; ++f) {
        acc[0][hb * 4 + f] = mfma(wf[hb][f], xf[s & 1][0], acc[0][hb * 4 + f]);
        acc[1][hb * 4 + f] = mfma(wf[hb][f], xf[s & 1][1], acc[1][hb * 4 + f]);
      }
      if (hb == 1) csum = mfma(wcs, onesf, csum);
      SB();
    }
  }
  STAMP(2, 1);
  if (tl == 0) *(f32x4*)(sCs + w * 16 + 4 * g) = csum;
  float mb[2], rstd[2];
#pragma unroll
  for (int t = 0; t < 2; ++t) {
    float a = st[t][0].x + (g < 3 ? st[t][1].x : 0.f), b = st[t][0].y + (g < 3 ? st[t][1].y : 0.f);
    a += __shfl_xor(a, 16); b += __shfl_xor(b, 16);
    a += __shfl_xor(a, 32); b += __shfl_xor(b, 32);
    ln_params(a, b, mb[t], rstd[t]);
  }
  BAR();
  STAMP(2, 2);
#pragma unroll
  for (int fp = 0; fp < 2; ++fp) {
    f32x4 cg[2], cu[2];
#pragma unroll
    for (int i = 0; i < 2; ++i) {
      cg[i] = *(const f32x4*)(sCs + (fp * 2 + i) * 16 + 4 * g);
      cu[i] = *(const f32x4*)(sCs + (4 + fp * 2 + i) * 16 + 4 * g);
    }
#pragma unroll
    for (int t = 0; t < 2; ++t) {
      u32x4 out;
#pragma unroll
      for (int i = 0; i < 2; ++i) {
        float av[4];
#pragma unroll
        for (int r = 0; r < 4; ++r) {
          float gv = rbf((acc[t][fp * 2 + i][r] - mb[t] * cg[i][r]) * rstd[t]);
          float uv = rbf((acc[t][4 + fp * 2 + i][r] - mb[t] * cu[i][r]) * rstd[t]);
          float sl = rbf(gv * __builtin_amdgcn_rcpf(1.0f + __expf(-gv)));
          av[r] = sl * uv;
        }
        out[i * 2] = pack_bf(av[0], av[1]);
        out[i * 2 + 1] = pack_bf(av[2], av[3]);
      }
      const int tbg = mt * 16 + w * 2 + t;
      *((u32x4*)a_frag + (size_t)(tbg * 56 + nt * 2 + fp) * 64 + l) = out;
    }
  }
  STAMP(2, 3);
}

extern "C" void kernel_launch(void* const* d_in, const int* in_sizes, int n_in, void* d_out, int out_size,
                              void* d_ws, size_t ws_size, hipStream_t stream) {
  const u16* x = (const u16*)d_in[0];
  const u16* wqkv = (const u16*)d_in[1];
  const u16* wo = (const u16*)d_in[2];
  const u16* wg = (const u16*)d_in[4];
  const u16* wu = (const u16*)d_in[5];
  const u16* wd = (const u16*)d_in[6];
  u16* out = (u16*)d_out;
  char* ws = (char*)d_ws;
  u16* o_frag = (u16*)ws;
  u16* h = (u16*)(ws + (2u << 20));
  u16* h_frag = (u16*)(ws + (4u << 20));
  u16* a_frag = (u16*)(ws + (6u << 20));
  float* stats = (float*)(ws + (14u << 20));
  k_qkv_attn<<<256, 768, 0, stream>>>(x, wqkv, o_frag);
  k_gemm64<7, 8, true><<<224, 768, 0, stream>>>(o_frag, wo, x, h, h_frag, stats);
  k_gateup<<<224, 768, 0, stream>>>(h_frag, wg, wu, stats, a_frag);
  k_gemm64<28, 9, false><<<224, 768, 0, stream>>>(a_frag, wd, h, out, nullptr, nullptr);
}
```

```cpp
#include <hip/hip_runtime.h>
#include <hip/hip_bf16.h>
#include <stdint.h>

typedef unsigned short u16;
typedef __attribute__((ext_vector_type(8))) __bf16 bf16x8;
typedef __attribute__((ext_vector_type(4))) short s16x4;
typedef __attribute__((ext_vector_type(4))) float f32x4;
typedef __attribute__((ext_vector_type(4))) unsigned int u32x4;
typedef __attribute__((ext_vector_type(2))) unsigned int u32x2;
typedef __attribute__((ext_vector_type(2))) __bf16 bf16x2;
#define LDS_AS __attribute__((address_space(3)))
#define SB() __builtin_amdgcn_sched_barrier(0)
#define GLDS(gp, lp) __builtin_amdgcn_global_load_lds((const unsigned*)(gp), (unsigned*)(lp), 16, 0, 0)
#define WAITV(n) asm volatile("s_waitcnt vmcnt(" #n ")" ::: "memory")
#define BAR() do { asm volatile("s_waitcnt lgkmcnt(0)" ::: "memory"); __builtin_amdgcn_s_barrier(); asm volatile("" ::: "memory"); } while (0)

#define STAMP(K,I)

constexpr int S_ = 2048, D_ = 448, F_ = 1792, QKV_ = 1344;
constexpr unsigned ONES = 0x3F803F80u;

__device__ __attribute__((aligned(16))) const unsigned g_const[12] = {ONES, ONES, ONES, ONES, 0x3F80u, 0u, 0u, 0u, 0u, 0u, 0u, 0u};

__device__ __forceinline__ float bf_lo(unsigned u) { return __uint_as_float(u << 16); }
__device__ __forceinline__ float bf_hi(unsigned u) { return __uint_as_float(u & 0xffff0000u); }
__device__ __forceinline__ unsigned pack_bf(float a, float b) {
  bf16x2 v = {(__bf16)a, (__bf16)b};
  return __builtin_bit_cast(unsigned, v);
}
__device__ __forceinline__ float rbf(float a) { return (float)(__bf16)a; }
__device__ __forceinline__ float dot2(unsigned a, unsigned b, float c) {
  return __builtin_amdgcn_fdot2_f32_bf16(__builtin_bit_cast(bf16x2, a), __builtin_bit_cast(bf16x2, b), c, false);
}
__device__ __forceinline__ u32x2 tr_read(const char* p) {
  s16x4 v = __builtin_amdgcn_ds_read_tr16_b64_v4i16((LDS_AS s16x4*)p);
  return __builtin_bit_cast(u32x2, v);
}
__device__ __forceinline__ u32x4 wfrag(const char* p) {
  u32x2 a = tr_read(p), b = tr_read(p + 16 * 128);
  u32x4 r = {a[0], a[1], b[0], b[1]};
  return r;
}
__device__ __forceinline__ f32x4 mfma(u32x4 a, u32x4 b, f32x4 c) {
  return __builtin_amdgcn_mfma_f32_16x16x32_bf16(__builtin_bit_cast(bf16x8, a), __builtin_bit_cast(bf16x8, b), c, 0, 0, 0);
}
__device__ __forceinline__ int rho_inv(int r) { return (r & ~31) | ((r & 12) << 1) | ((r & 16) >> 2) | (r & 3); }
__device__ __forceinline__ int tr_lane_off(int l, int nb) {
  int g = l >> 4, q = (l & 15) >> 2, p = l & 3;
  int swz = 2 * (g & 1) + (q >> 1);
  return (4 * g + q) * 128 + ((nb ^ swz) << 5) + p * 8;
}
__device__ __forceinline__ void ln_params(float sum, float sq, float& mb, float& rstd) {
  float mean = sum * (1.0f / D_);
  float var = fmaxf(sq * (1.0f / D_) - mean * mean, 0.0f);
  mb = rbf(mean);
  float den = rbf(sqrtf(rbf(rbf(var) + 1e-5f)));
  rstd = 1.0f / den;
}
__device__ __forceinline__ u32x4 add_bf8(u32x4 a, u32x4 b) {
  u32x4 r;
#pragma unroll
  for (int e = 0; e < 4; ++e) r[e] = pack_bf(bf_lo(a[e]) + bf_lo(b[e]), bf_hi(a[e]) + bf_hi(b[e]));
  return r;
}

constexpr int P1_WG = 64 * 128;
constexpr int P1_XO = 3 * P1_WG;
constexpr int P1_SLOT = 3 * P1_WG + 80 * 128;
constexpr int P1_R = 4;
constexpr int P1_SP = 672;
constexpr int P1_SMEM = P1_R * P1_SLOT + 2048 + (80 * 2 + 3 * 64) * 4;

__global__ __launch_bounds__(768, 3) void k_qkv_attn(const u16* __restrict__ x, const u16* __restrict__ wqkv,
                                                     u16* __restrict__ o_frag, const u16* __restrict__ wo,
                                                     const u16* __restrict__ wg, const u16* __restrict__ wu,
                                                     const u16* __restrict__ wd) {
  __shared__ __attribute__((aligned(16))) char smem[P1_SMEM];
  float* sSum = (float*)(smem + P1_R * P1_SLOT + 2048);
  float* sSq = sSum + 80;
  float* sCs = sSq + 80;

  const int tid = threadIdx.x, l = tid & 63, g = l >> 4, tl = l & 15;
  const int w = __builtin_amdgcn_readfirstlane(tid >> 6);
  const int bid = blockIdx.x, xcd = bid & 7, jj = bid >> 3;
  const int mt = xcd * 4 + (jj >> 3), hp = jj & 7;
  const int row0 = mt * 64 - 16;
  STAMP(0, 0);

  if (w >= 8) {
    const int lw = w - 8, lr = l >> 3, lc = l & 7;
    const u16* src[9];
    int dst[9], adv[9];
#pragma unroll
    for (int j = 0; j < 9; ++j) {
      const int pi = lw + 4 * j;
      if (pi < 24) {
        const int m = pi >> 3, rho = (pi & 7) * 8 + lr;
        const int c = lc ^ (((rho >> 1) & 3) << 1);
        if (c < 7) { src[j] = wqkv + (size_t)rho_inv(rho) * QKV_ + m * D_ + hp * 56 + c * 8; adv[j] = 64 * QKV_; }
        else { src[j] = (const u16*)(g_const + (m == 1 ? 4 : 8)); adv[j] = 0; }
        dst[j] = pi * 1024;
      } else if (pi < 34) {
        const int r = (pi - 24) * 8 + lr;
        const int c = lc ^ ((r >> 1) & 7);
        if (r > 0) { src[j] = x + (size_t)max(row0 + r, 0) * D_ + c * 8; adv[j] = 64; }
        else { src[j] = (const u16*)g_const; adv[j] = 0; }
        dst[j] = pi * 1024;
      } else { src[j] = (const u16*)g_const; adv[j] = 0; dst[j] = -1; }
    }
#define P1_ISSUE(ST) do { \
      char* sl_ = smem + ((ST) % P1_R) * P1_SLOT; \
      _Pragma("unroll") for (int j = 0; j < 9; ++j) { \
        GLDS(src[j] + (size_t)(ST) * adv[j], (dst[j] >= 0) ? sl_ + dst[j] : smem + P1_R * P1_SLOT + (lw & 1) * 1024); } \
    } while (0)
    P1_ISSUE(0); P1_ISSUE(1); P1_ISSUE(2);
    WAITV(9);  BAR();
    P1_ISSUE(3);
    WAITV(9);  BAR();
    P1_ISSUE(4);
    WAITV(9);  BAR();
    P1_ISSUE(5);
    WAITV(9);  BAR();
    P1_ISSUE(6);
    WAITV(9);  BAR();
    WAITV(0);  BAR();
    {
      char* dummy = smem + P1_R * P1_SLOT + (lw & 1) * 1024;
      const int pc = (bid * 4 + lw);
      const size_t e0 = ((size_t)pc * 64 + l) * 8;
      GLDS(wg + e0, dummy); GLDS(wu + e0, dummy); GLDS(wd + e0, dummy);
      if (pc < 544) { GLDS(wg + e0 + 1024 * 512, dummy); GLDS(wu + e0 + 1024 * 512, dummy); GLDS(wd + e0 + 1024 * 512, dummy); }
      if (pc < 392) GLDS(wo + e0, dummy);
    }
    BAR();
    BAR(); BAR();
    WAITV(0);
    return;
  }

  const int fb = w & 3, hf = w >> 2;
  int xo[3][2];
#pragma unroll
  for (int t = 0; t < 3; ++t) {
    const int tb = (t < 2) ? hf * 3 + t : (hf ? fb : 2);
    const int r = tb * 16 + tl;
#pragma unroll
    for (int s2 = 0; s2 < 2; ++s2) xo[t][s2] = P1_XO + r * 128 + (((4 * s2 + g) ^ ((r >> 1) & 7)) << 4);
  }
  f32x4 acc[3][3];
#pragma unroll
  for (int m = 0; m < 3; ++m)
#pragma unroll
    for (int t = 0; t < 3; ++t) acc[m][t] = (f32x4){0.f, 0.f, 0.f, 0.f};
  const int troff = tr_lane_off(l, fb);

  u32x4 xf[2][3], wf[2][3];
#define P1_LOAD(B, S) do { \
    const char* slot_ = smem + (((S) >> 1) % P1_R) * P1_SLOT; \
    _Pragma("unroll") for (int m = 0; m < 3; ++m) wf[B][m] = wfrag(slot_ + m * P1_WG + ((S) & 1) * 32 * 128 + troff); \
    _Pragma("unroll") for (int t = 0; t < 3; ++t) xf[B][t] = *(const u32x4*)(slot_ + xo[t][(S) & 1]); \
  } while (0)
#pragma unroll
  for (int ii = 0; ii < 7; ++ii) {
    BAR();
    if (ii == 0) { STAMP(0, 1); P1_LOAD(0, 0); }
#pragma unroll
    for (int s2 = 0; s2 < 2; ++s2) {
      const int s = ii * 2 + s2;
      if (s + 1 < 14) P1_LOAD((s + 1) & 1, s + 1);
      SB();
#pragma unroll
      for (int m = 0; m < 3; ++m) {
        acc[m][0] = mfma(wf[s & 1][m], xf[s & 1][0], acc[m][0]);
        acc[m][1] = mfma(wf[s & 1][m], xf[s & 1][1], acc[m][1]);
        if (hf == 0) acc[m][2] = mfma(wf[s & 1][m], xf[s & 1][2], acc[m][2]);
        else if (m == 0) acc[0][2] = mfma(xf[s & 1][2], xf[s & 1][2], acc[0][2]);
        else if (m == 1) acc[1][2] = mfma(xf[s & 1][1], xf[s & 1][1], acc[1][2]);
      }
      SB();
    }
  }
  STAMP(0, 2);
  if (fb == 3 && g == 2) {
#pragma unroll
    for (int t = 0; t < 3; ++t)
      if (hf == 0 || t < 2) sSum[(hf * 3 + t) * 16 + tl] = acc[1][t][0];
  }
  if (hf == 1 && g == (tl >> 2)) {
    float a0 = acc[0][2][0], a1 = acc[1][2][0];
#pragma unroll
    for (int r = 1; r < 4; ++r) { if ((tl & 3) == r) { a0 = acc[0][2][r]; a1 = acc[1][2][r]; } }
    sSq[fb * 16 + tl] = a0;
    if (fb == 0) sSq[64 + tl] = a1;
  }
  if (hf == 0 && tl == 0) {
#pragma unroll
    for (int m = 0; m < 3; ++m) *(f32x4*)(sCs + m * 64 + fb * 16 + 4 * g) = acc[m][0];
  }
  BAR();
  char* sS = smem;
  {
    f32x4 csf[3];
#pragma unroll
    for (int m = 0; m < 3; ++m) csf[m] = *(const f32x4*)(sCs + m * 64 + fb * 16 + 4 * g);
    const int quad = 4 * fb + g;
#pragma unroll
    for (int t = 0; t < 3; ++t) {
      if (hf == 0 || t < 2) {
        const int tb = hf * 3 + t;
        float mb, rstd;
        ln_params(sSum[tb * 16 + tl], sSq[tb * 16 + tl], mb, rstd);
#pragma unroll
        for (int m = 0; m < 3; ++m) {
          float v0 = (acc[m][t][0] - mb * csf[m][0]) * rstd;
          float v1 = (acc[m][t][1] - mb * csf[m][1]) * rstd;
          float v2 = (acc[m][t][2] - mb * csf[m][2]) * rstd;
          float v3 = (acc[m][t][3] - mb * csf[m][3]) * rstd;
          u32x2 pk = {pack_bf(v0, v1), pack_bf(v2, v3)};
          if (quad < 14) *(u32x2*)(sS + (m * 14 + quad) * P1_SP + (tb * 16 + tl) * 8) = pk;
        }
      }
    }
  }
  BAR();
  STAMP(0, 3);

  {
    const int hh = l >> 5, part = (l >> 3) & 3;
    const int t = w * 8 + (l & 7);
    const int tok = mt * 64 + t;
    const int q0 = hh * 7 + part * 2;
    const int q1 = hh * 7 + min(part * 2 + 1, 6);
    const bool two = part < 3;
    u32x2 qv0 = *(const u32x2*)(sS + (0 * 14 + q0) * P1_SP + (16 + t) * 8);
    u32x2 qv1 = *(const u32x2*)(sS + (0 * 14 + q1) * P1_SP + (16 + t) * 8);
    if (!two) qv1 = (u32x2){0u, 0u};
    float lg[8];
#pragma unroll
    for (int ww = 0; ww < 8; ++ww) {
      u32x2 k0 = *(const u32x2*)(sS + (1 * 14 + q0) * P1_SP + (16 + t - 2 * ww) * 8);
      u32x2 k1 = *(const u32x2*)(sS + (1 * 14 + q1) * P1_SP + (16 + t - 2 * ww) * 8);
      float sacc = 0.f;
      sacc = dot2(qv0[0], k0[0], sacc); sacc = dot2(qv0[1], k0[1], sacc);
      sacc = dot2(qv1[0], k1[0], sacc); sacc = dot2(qv1[1], k1[1], sacc);
      lg[ww] = sacc;
    }
    const float scale = 0.1889822365046136f;
    float mx = -3.0e38f;
#pragma unroll
    for (int ww = 0; ww < 8; ++ww) {
      float sacc = lg[ww] + __shfl_xor(lg[ww], 8);
      sacc += __shfl_xor(sacc, 16);
      float v = rbf(rbf(sacc) * scale);
      v = (tok - 2 * ww >= 0) ? v : -1e9f;
      lg[ww] = v;
      mx = fmaxf(mx, v);
    }
    float den = 0.f;
#pragma unroll
    for (int ww = 0; ww < 8; ++ww) { lg[ww] = __expf(lg[ww] - mx); den += lg[ww]; }
    float inv = 1.0f / den;
#pragma unroll
    for (int ww = 0; ww < 8; ++ww) lg[ww] = rbf(lg[ww] * inv);
    float oacc[2][4];
#pragma unroll
    for (int i = 0; i < 2; ++i)
#pragma unroll
      for (int e = 0; e < 4; ++e) oacc[i][e] = 0.f;
#pragma unroll
    for (int ww = 0; ww < 8; ++ww) {
#pragma unroll
      for (int i = 0; i < 2; ++i) {
        u32x2 vv = *(const u32x2*)(sS + (2 * 14 + (i ? q1 : q0)) * P1_SP + (16 + t - 2 * ww) * 8);
        oacc[i][0] = fmaf(lg[ww], bf_lo(vv[0]), oacc[i][0]);
        oacc[i][1] = fmaf(lg[ww], bf_hi(vv[0]), oacc[i][1]);
        oacc[i][2] = fmaf(lg[ww], bf_lo(vv[1]), oacc[i][2]);
        oacc[i][3] = fmaf(lg[ww], bf_hi(vv[1]), oacc[i][3]);
      }
    }
    const int tbg = tok >> 4;
#pragma unroll
    for (int i = 0; i < 2; ++i) {
      if (i == 0 || two) {
        int f = hp * 56 + (q0 + i) * 4;
        int s = f >> 5, ii = (f >> 4) & 1, gg = (f >> 2) & 3;
        u32x2 pk = {pack_bf(oacc[i][0], oacc[i][1]), pack_bf(oacc[i][2], oacc[i][3])};
        *(u32x2*)((char*)o_frag + ((size_t)((tbg * 14 + s) * 64 + gg * 16 + (tok & 15))) * 16 + ii * 8) = pk;
      }
    }
  }
  STAMP(0, 4);
}

constexpr int EP = 144;
constexpr int G_SLOT = 16384;
template <int NST, int R, bool IS_P2>
__global__ __launch_bounds__(768, 3) void k_gemm64(const u16* __restrict__ a_frag, const u16* __restrict__ wmat,
                                                   const u16* __restrict__ res, u16* __restrict__ dst,
                                                   u16* __restrict__ h_frag, float* __restrict__ stats) {
  __shared__ __attribute__((aligned(16))) char smem[R * G_SLOT + 64 * EP];
  char* sT = smem + R * G_SLOT;
  const int tid = threadIdx.x, l = tid & 63, g = l >> 4, tl = l & 15;
  const int w = __builtin_amdgcn_readfirstlane(tid >> 6);
  const int bid = blockIdx.x, xcd = bid & 7, jj = bid >> 3;
  const int mt = xcd * 4 + jj / 7, nt = jj % 7;
  constexpr int KS = NST * 2;
  STAMP(IS_P2 ? 1 : 3, 0);

  if (w >= 8) {
    const int lw = w - 8, lr = l >> 3, lc = l & 7;
    const u16* ws[2];
#pragma unroll
    for (int j = 0; j < 2; ++j) {
      const int rho = (lw + 4 * j) * 8 + lr;
      const int c = lc ^ (((rho >> 1) & 3) << 1);
      ws[j] = wmat + (size_t)rho * D_ + nt * 64 + c * 8;
    }
    const u16* xs = a_frag + ((size_t)((mt * 4 + lw) * KS) * 64 + l) * 8;
#define G_ISSUE(ST) do { \
      char* sl_ = smem + ((ST) % R) * G_SLOT; \
      GLDS(ws[0] + (size_t)(ST) * 64 * D_, sl_ + lw * 1024); \
      GLDS(ws[1] + (size_t)(ST) * 64 * D_, sl_ + (lw + 4) * 1024); \
      GLDS(xs + (size_t)(ST) * 1024, sl_ + 8192 + lw * 1024); \
      GLDS(xs + (size_t)(ST) * 1024 + 512, sl_ + 8192 + (4 + lw) * 1024); \
    } while (0)
    constexpr int PRE = (R - 1 < NST) ? R - 1 : NST;
#pragma unroll
    for (int st = 0; st < PRE; ++st) G_ISSUE(st);
#pragma unroll
    for (int i = 0; i < NST; ++i) {
      const int issued = (i + R - 2 < NST - 1) ? i + R - 2 : NST - 1;
      const int need = (i + 1 < NST) ? i + 1 : NST - 1;
      const int allow = (issued - need) * 4;
      switch (allow) {
        case 0: WAITV(0); break; case 4: WAITV(4); break; case 8: WAITV(8); break; case 12: WAITV(12); break;
        case 16: WAITV(16); break; case 20: WAITV(20); break; case 24: WAITV(24); break; case 28: WAITV(28); break;
        default: WAITV(0); break;
      }
      BAR();
      if (i + R - 1 < NST) G_ISSUE(i + R - 1);
    }
    BAR();
    if (IS_P2) BAR();
    return;
  }

  const int tb = w & 3, fh = w >> 2;
  const int tro0 = tr_lane_off(l, fh * 2), tro1 = tr_lane_off(l, fh * 2 + 1);
  const int xoff = 8192 + tb * 1024 + l * 16;
  const size_t rowoff = (size_t)(mt * 64 + tb * 16 + fh * 8 + (l >> 3)) * D_ + nt * 64 + (l & 7) * 8;
  const u32x4 rv = *(const u32x4*)(res + rowoff);
  f32x4 acc[2] = {(f32x4){0.f, 0.f, 0.f, 0.f}, (f32x4){0.f, 0.f, 0.f, 0.f}};
  u32x4 wf[2][2], xf[2];
#define G_LOAD(K) do { \
    const char* sl_ = smem + (((K) >> 1) % R) * G_SLOT; \
    wf[(K) & 1][0] = wfrag(sl_ + ((K) & 1) * 4096 + tro0); \
    wf[(K) & 1][1] = wfrag(sl_ + ((K) & 1) * 4096 + tro1); \
    xf[(K) & 1] = *(const u32x4*)(sl_ + ((K) & 1) * 4096 + xoff); \
  } while (0)
#pragma unroll
  for (int i = 0; i < NST; ++i) {
    BAR();
    if (i == 0) { STAMP(IS_P2 ? 1 : 3, 1); G_LOAD(0); }
#pragma unroll
    for (int j = 0; j < 2; ++j) {
      const int k = i * 2 + j;
      if (k + 1 < KS) G_LOAD(k + 1);
      SB();
      acc[0] = mfma(wf[k & 1][0], xf[k & 1], acc[0]);
      acc[1] = mfma(wf[k & 1][1], xf[k & 1], acc[1]);
      SB();
    }
  }
  STAMP(IS_P2 ? 1 : 3, 2);
  char* tw = sT + tb * 16 * EP;
#pragma unroll
  for (int f = 0; f < 2; ++f) {
    u32x2 pk = {pack_bf(acc[f][0], acc[f][1]), pack_bf(acc[f][2], acc[f][3])};
    *(u32x2*)(tw + tl * EP + (fh * 2 + f) * 32 + g * 8) = pk;
  }
  BAR();
  {
    char* p = tw + (fh * 8 + (l >> 3)) * EP + (l & 7) * 16;
    u32x4 hv = add_bf8(rv, *(const u32x4*)p);
    *(u32x4*)(dst + rowoff) = hv;
    if (IS_P2) {
      *(u32x4*)p = hv;
      float sm = 0.f, sq = 0.f;
#pragma unroll
      for (int e = 0; e < 4; ++e) { sm = dot2(hv[e], ONES, sm); sq = dot2(hv[e], hv[e], sq); }
      sm += __shfl_xor(sm, 1); sq += __shfl_xor(sq, 1);
      sm += __shfl_xor(sm, 2); sq += __shfl_xor(sq, 2);
      sm += __shfl_xor(sm, 4); sq += __shfl_xor(sq, 4);
      if ((l & 7) == 0) {
        float2 st = make_float2(sm, sq);
        *(float2*)(stats + ((size_t)nt * S_ + mt * 64 + tb * 16 + fh * 8 + (l >> 3)) * 2) = st;
      }
    }
  }
  if (IS_P2) {
    BAR();
    u32x4 v = *(const u32x4*)(tw + tl * EP + (fh * 4 + g) * 16);
    *((u32x4*)h_frag + (size_t)((mt * 4 + tb) * 14 + nt * 2 + fh) * 64 + l) = v;
  }
  STAMP(IS_P2 ? 1 : 3, 3);
}

constexpr int P3_SLOT = 24576;
constexpr int P3_R = 6;
__global__ __launch_bounds__(768, 3) void k_gateup(const u16* __restrict__ h_frag, const u16* __restrict__ wg,
                                                   const u16* __restrict__ wu, const float* __restrict__ stats,
                                                   u16* __restrict__ a_frag) {
  __shared__ __attribute__((aligned(16))) char smem[P3_R * P3_SLOT + 8 * 16 * 4];
  float* sCs = (float*)(smem + P3_R * P3_SLOT);
  const int tid = threadIdx.x, l = tid & 63, g = l >> 4, tl = l & 15;
  const int w = __builtin_amdgcn_readfirstlane(tid >> 6);
  const int bid = blockIdx.x, xcd = bid & 7, jj = bid >> 3;
  const int mt = (xcd & 1) * 4 + jj / 7, nt = (xcd >> 1) * 7 + jj % 7;
  STAMP(2, 0);

  if (w >= 8) {
    const int lw = w - 8, lr = l >> 3, lc = l & 7;
    const int rho = lw * 8 + lr;
    const int c = lc ^ (((rho >> 1) & 3) << 1);
    const u16* gs = wg + (size_t)rho_inv(rho) * F_ + nt * 64 + c * 8;
    const u16* us = wu + (size_t)rho_inv(rho) * F_ + nt * 64 + c * 8;
    const u16* xs = h_frag + ((size_t)((mt * 16 + lw * 4) * 14) * 64 + l) * 8;
#define P3_ISSUE(ST) do { \
      char* sl_ = smem + ((ST) % P3_R) * P3_SLOT; \
      GLDS(gs + (size_t)(ST) * 32 * F_, sl_ + lw * 1024); \
      GLDS(us + (size_t)(ST) * 32 * F_, sl_ + 4096 + lw * 1024); \
      _Pragma("unroll") for (int j = 0; j < 4; ++j) GLDS(xs + (size_t)(j * 14 + (ST)) * 512, sl_ + 8192 + (lw * 4 + j) * 1024); \
    } while (0)
#pragma unroll
    for (int st = 0; st < P3_R - 1; ++st) P3_ISSUE(st);
#pragma unroll
    for (int i = 0; i < 14; ++i) {
      const int issued = (i + P3_R - 2 < 13) ? i + P3_R - 2 : 13;
      const int need = (i + 1 < 14) ? i + 1 : 13;
      const int allow = (issued - need) * 6;
      switch (allow) {
        case 0: WAITV(0); break; case 6: WAITV(6); break; case 12: WAITV(12); break; case 18: WAITV(18); break;
        default: WAITV(0); break;
      }
      BAR();
      if (i + P3_R - 1 < 14) P3_ISSUE(i + P3_R - 1);
    }
    BAR();
    return;
  }

  float2 st[2][2];
  f32x4 acc[2][8];
#pragma unroll
  for (int t = 0; t < 2; ++t)
#pragma unroll
    for (int f = 0; f < 8; ++f) acc[t][f] = (f32x4){0.f, 0.f, 0.f, 0.f};
  int tro[4];
#pragma unroll
  for (int f = 0; f < 4; ++f) tro[f] = tr_lane_off(l, f);
  const int csoff = (w >> 2) * 4096 + tr_lane_off(l, w & 3);
  const int xoff = 8192 + w * 2048 + l * 16;
  f32x4 csum = {0.f, 0.f, 0.f, 0.f};
  const u32x4 onesf = {ONES, ONES, ONES, ONES};

  u32x4 wf[2][4], wcs, xf[2][2];
#define P3_LOADH(HS) do { \
    int so_ = (((HS) >> 1) % P3_R) * P3_SLOT; asm volatile("" : "+s"(so_)); \
    const char* sl_ = smem + so_; \
    _Pragma("unroll") for (int f = 0; f < 4; ++f) wf[(HS) & 1][f] = wfrag(sl_ + ((HS) & 1) * 4096 + tro[f]); \
    if (((HS) & 1) == 0) { xf[((HS) >> 1) & 1][0] = *(const u32x4*)(sl_ + xoff); xf[((HS) >> 1) & 1][1] = *(const u32x4*)(sl_ + xoff + 1024); } \
  } while (0)
#pragma unroll
  for (int s = 0; s < 14; ++s) {
    BAR();
    if (s == 0) { STAMP(2, 4); P3_LOADH(0); }
#pragma unroll
    for (int hb = 0; hb < 2; ++hb) {
      const int hs = s * 2 + hb;
      if (hs + 1 < 28) P3_LOADH(hs + 1);
      if (hb == 0) { int so_ = (s % P3_R) * P3_SLOT; asm volatile("" : "+s"(so_)); wcs = wfrag(smem + so_ + csoff); }
      if (s == 11) {
#pragma unroll
        for (int i = 0; i < 2; ++i)
          st[hb][i] = *(const float2*)(stats + ((size_t)min(g + 4 * i, 6) * S_ + mt * 256 + (w * 2 + hb) * 16 + tl) * 2);
      }
      SB();
#pragma unroll
      for (int f = 0; f < 4; ++f) {
        acc[0][hb * 4 + f] = mfma(wf[hb][f], xf[s & 1][0], acc[0][hb * 4 + f]);
        acc[1][hb * 4 + f] = mfma(wf[hb][f], xf[s & 1][1], acc[1][hb * 4 + f]);
      }
      if (hb == 1) csum = mfma(wcs, onesf, csum);
      SB();
    }
  }
  STAMP(2, 1);
  if (tl == 0) *(f32x4*)(sCs + w * 16 + 4 * g) = csum;
  float mb[2], rstd[2];
#pragma unroll
  for (int t = 0; t < 2; ++t) {
    float a = st[t][0].x + (g < 3 ? st[t][1].x : 0.f), b = st[t][0].y + (g < 3 ? st[t][1].y : 0.f);
    a += __shfl_xor(a, 16); b += __shfl_xor(b, 16);
    a += __shfl_xor(a, 32); b += __shfl_xor(b, 32);
    ln_params(a, b, mb[t], rstd[t]);
  }
  BAR();
  STAMP(2, 2);
#pragma unroll
  for (int fp = 0; fp < 2; ++fp) {
    f32x4 cg[2], cu[2];
#pragma unroll
    for (int i = 0; i < 2; ++i) {
      cg[i] = *(const f32x4*)(sCs + (fp * 2 + i) * 16 + 4 * g);
      cu[i] = *(const f32x4*)(sCs + (4 + fp * 2 + i) * 16 + 4 * g);
    }
#pragma unroll
    for (int t = 0; t < 2; ++t) {
      u32x4 out;
#pragma unroll
      for (int i = 0; i < 2; ++i) {
        float av[4];
#pragma unroll
        for (int r = 0; r < 4; ++r) {
          float gv = rbf((acc[t][fp * 2 + i][r] - mb[t] * cg[i][r]) * rstd[t]);
          float uv = rbf((acc[t][4 + fp * 2 + i][r] - mb[t] * cu[i][r]) * rstd[t]);
          float sl = rbf(gv * __builtin_amdgcn_rcpf(1.0f + __expf(-gv)));
          av[r] = sl * uv;
        }
        out[i * 2] = pack_bf(av[0], av[1]);
        out[i * 2 + 1] = pack_bf(av[2], av[3]);
      }
      const int tbg = mt * 16 + w * 2 + t;
      *((u32x4*)a_frag + (size_t)(tbg * 56 + nt * 2 + fp) * 64 + l) = out;
    }
  }
  STAMP(2, 3);
}

extern "C" void kernel_launch(void* const* d_in, const int* in_sizes, int n_in, void* d_out, int out_size,
                              void* d_ws, size_t ws_size, hipStream_t stream) {
  const u16* x = (const u16*)d_in[0];
  const u16* wqkv = (const u16*)d_in[1];
  const u16* wo = (const u16*)d_in[2];
  const u16* wg = (const u16*)d_in[4];
  const u16* wu = (const u16*)d_in[5];
  const u16* wd = (const u16*)d_in[6];
  u16* out = (u16*)d_out;
  char* ws = (char*)d_ws;
  u16* o_frag = (u16*)ws;
  u16* h = (u16*)(ws + (2u << 20));
  u16* h_frag = (u16*)(ws + (4u << 20));
  u16* a_frag = (u16*)(ws + (6u << 20));
  float* stats = (float*)(ws + (14u << 20));
  k_qkv_attn<<<256, 768, 0, stream>>>(x, wqkv, o_frag, wo, wg, wu, wd);
  k_gemm64<7, 8, true><<<224, 768, 0, stream>>>(o_frag, wo, x, h, h_frag, stats);
  k_gateup<<<224, 768, 0, stream>>>(h_frag, wg, wu, stats, a_frag);
  k_gemm64<28, 9, false><<<224, 768, 0, stream>>>(a_frag, wd, h, out, nullptr, nullptr);
}
```

```cpp
#include <hip/hip_runtime.h>
#include <hip/hip_bf16.h>
#include <stdint.h>

typedef unsigned short u16;
typedef __attribute__((ext_vector_type(8))) __bf16 bf16x8;
typedef __attribute__((ext_vector_type(4))) short s16x4;
typedef __attribute__((ext_vector_type(4))) float f32x4;
typedef __attribute__((ext_vector_type(4))) unsigned int u32x4;
typedef __attribute__((ext_vector_type(2))) unsigned int u32x2;
typedef __attribute__((ext_vector_type(2))) __bf16 bf16x2;
#define LDS_AS __attribute__((address_space(3)))
#define SB() __builtin_amdgcn_sched_barrier(0)
#define GLDS(gp, lp) __builtin_amdgcn_global_load_lds((const unsigned*)(gp), (unsigned*)(lp), 16, 0, 0)
#define WAITV(n) asm volatile("s_waitcnt vmcnt(" #n ")" ::: "memory")
#define RBAR() do { asm volatile("" ::: "memory"); __builtin_amdgcn_s_barrier(); asm volatile("" ::: "memory"); } while (0)
#define BAR() do { asm volatile("s_waitcnt lgkmcnt(0)" ::: "memory"); __builtin_amdgcn_s_barrier(); asm volatile("" ::: "memory"); } while (0)

#define STAMP(K,I)

constexpr int S_ = 2048, D_ = 448, F_ = 1792, QKV_ = 1344;
constexpr unsigned ONES = 0x3F803F80u;

__device__ __attribute__((aligned(16))) const unsigned g_const[12] = {ONES, ONES, ONES, ONES, 0x3F80u, 0u, 0u, 0u, 0u, 0u, 0u, 0u};

__device__ __forceinline__ float bf_lo(unsigned u) { return __uint_as_float(u << 16); }
__device__ __forceinline__ float bf_hi(unsigned u) { return __uint_as_float(u & 0xffff0000u); }
__device__ __forceinline__ unsigned pack_bf(float a, float b) {
  bf16x2 v = {(__bf16)a, (__bf16)b};
  return __builtin_bit_cast(unsigned, v);
}
__device__ __forceinline__ float rbf(float a) { return (float)(__bf16)a; }
__device__ __forceinline__ float dot2(unsigned a, unsigned b, float c) {
  return __builtin_amdgcn_fdot2_f32_bf16(__builtin_bit_cast(bf16x2, a), __builtin_bit_cast(bf16x2, b), c, false);
}
__device__ __forceinline__ u32x2 tr_read(const char* p) {
  s16x4 v = __builtin_amdgcn_ds_read_tr16_b64_v4i16((LDS_AS s16x4*)p);
  return __builtin_bit_cast(u32x2, v);
}
__device__ __forceinline__ u32x4 wfrag(const char* p) {
  u32x2 a = tr_read(p), b = tr_read(p + 16 * 128);
  u32x4 r = {a[0], a[1], b[0], b[1]};
  return r;
}
__device__ __forceinline__ f32x4 mfma(u32x4 a, u32x4 b, f32x4 c) {
  return __builtin_amdgcn_mfma_f32_16x16x32_bf16(__builtin_bit_cast(bf16x8, a), __builtin_bit_cast(bf16x8, b), c, 0, 0, 0);
}
__device__ __forceinline__ int rho_inv(int r) { return (r & ~31) | ((r & 12) << 1) | ((r & 16) >> 2) | (r & 3); }
__device__ __forceinline__ int tr_lane_off(int l, int nb) {
  int g = l >> 4, q = (l & 15) >> 2, p = l & 3;
  int swz = 2 * (g & 1) + (q >> 1);
  return (4 * g + q) * 128 + ((nb ^ swz) << 5) + p * 8;
}
__device__ __forceinline__ void ln_params(float sum, float sq, float& mb, float& rstd) {
  float mean = sum * (1.0f / D_);
  float var = fmaxf(sq * (1.0f / D_) - mean * mean, 0.0f);
  mb = rbf(mean);
  float den = rbf(sqrtf(rbf(rbf(var) + 1e-5f)));
  rstd = 1.0f / den;
}
__device__ __forceinline__ u32x4 add_bf8(u32x4 a, u32x4 b) {
  u32x4 r;
#pragma unroll
  for (int e = 0; e < 4; ++e) r[e] = pack_bf(bf_lo(a[e]) + bf_lo(b[e]), bf_hi(a[e]) + bf_hi(b[e]));
  return r;
}

constexpr int P1_WG = 64 * 128;
constexpr int P1_XO = 3 * P1_WG;
constexpr int P1_SLOT = 3 * P1_WG + 80 * 128;
constexpr int P1_R = 4;
constexpr int P1_SP = 672;
constexpr int P1_SMEM = P1_R * P1_SLOT + 2048 + (80 * 2 + 3 * 64) * 4;

__global__ __launch_bounds__(768, 3) void k_qkv_attn(const u16* __restrict__ x, const u16* __restrict__ wqkv,
                                                     u16* __restrict__ o_frag, const u16* __restrict__ wo,
                                                     const u16* __restrict__ wg, const u16* __restrict__ wu,
                                                     const u16* __restrict__ wd) {
  __shared__ __attribute__((aligned(16))) char smem[P1_SMEM];
  float* sSum = (float*)(smem + P1_R * P1_SLOT + 2048);
  float* sSq = sSum + 80;
  float* sCs = sSq + 80;

  const int tid = threadIdx.x, l = tid & 63, g = l >> 4, tl = l & 15;
  const int w = __builtin_amdgcn_readfirstlane(tid >> 6);
  const int bid = blockIdx.x, xcd = bid & 7, jj = bid >> 3;
  const int mt = xcd * 4 + (jj >> 3), hp = jj & 7;
  const int row0 = mt * 64 - 16;
  STAMP(0, 0);

  if (w >= 8) {
    const int lw = w - 8, lr = l >> 3, lc = l & 7;
    const u16* src[9];
    int dst[9], adv[9];
#pragma unroll
    for (int j = 0; j < 9; ++j) {
      const int pi = lw + 4 * j;
      if (pi < 24) {
        const int m = pi >> 3, rho = (pi & 7) * 8 + lr;
        const int c = lc ^ (((rho >> 1) & 3) << 1);
        if (c < 7) { src[j] = wqkv + (size_t)rho_inv(rho) * QKV_ + m * D_ + hp * 56 + c * 8; adv[j] = 64 * QKV_; }
        else { src[j] = (const u16*)(g_const + (m == 1 ? 4 : 8)); adv[j] = 0; }
        dst[j] = pi * 1024;
      } else if (pi < 34) {
        const int r = (pi - 24) * 8 + lr;
        const int c = lc ^ ((r >> 1) & 7);
        if (r > 0) { src[j] = x + (size_t)max(row0 + r, 0) * D_ + c * 8; adv[j] = 64; }
        else { src[j] = (const u16*)g_const; adv[j] = 0; }
        dst[j] = pi * 1024;
      } else { src[j] = (const u16*)g_const; adv[j] = 0; dst[j] = -1; }
    }
#define P1_ISSUE(ST) do { \
      char* sl_ = smem + ((ST) % P1_R) * P1_SLOT; \
      _Pragma("unroll") for (int j = 0; j < 9; ++j) { \
        GLDS(src[j] + (size_t)(ST) * adv[j], (dst[j] >= 0) ? sl_ + dst[j] : smem + P1_R * P1_SLOT + (lw & 1) * 1024); } \
    } while (0)
    P1_ISSUE(0); P1_ISSUE(1);
    WAITV(9);  BAR();
    P1_ISSUE(2);
    WAITV(9);  BAR();
    P1_ISSUE(3);
    WAITV(9);  BAR();
    P1_ISSUE(4);
    WAITV(9);  BAR();
    P1_ISSUE(5);
    WAITV(9);  BAR();
    P1_ISSUE(6);
    WAITV(9);  BAR();
    WAITV(0);  BAR();
    {
      char* dummy = smem + P1_R * P1_SLOT + (lw & 1) * 1024;
      const int pc = (bid * 4 + lw);
      const size_t e0 = ((size_t)pc * 64 + l) * 8;
      GLDS(wg + e0, dummy); GLDS(wu + e0, dummy); GLDS(wd + e0, dummy);
      if (pc < 544) { GLDS(wg + e0 + 1024 * 512, dummy); GLDS(wu + e0 + 1024 * 512, dummy); GLDS(wd + e0 + 1024 * 512, dummy); }
      if (pc < 392) GLDS(wo + e0, dummy);
    }
    BAR();
    BAR(); BAR();
    WAITV(0);
    return;
  }

  const int fb = w & 3, hf = w >> 2;
  int xo[3][2];
#pragma unroll
  for (int t = 0; t < 3; ++t) {
    const int tb = (t < 2) ? hf * 3 + t : (hf ? fb : 2);
    const int r = tb * 16 + tl;
#pragma unroll
    for (int s2 = 0; s2 < 2; ++s2) xo[t][s2] = P1_XO + r * 128 + (((4 * s2 + g) ^ ((r >> 1) & 7)) << 4);
  }
  f32x4 acc[3][3];
#pragma unroll
  for (int m = 0; m < 3; ++m)
#pragma unroll
    for (int t = 0; t < 3; ++t) acc[m][t] = (f32x4){0.f, 0.f, 0.f, 0.f};
  const int troff = tr_lane_off(l, fb);

  u32x4 xf[2][3], wf[2][3];
#define P1_LOAD(B, S) do { \
    const char* slot_ = smem + (((S) >> 1) % P1_R) * P1_SLOT; \
    _Pragma("unroll") for (int m = 0; m < 3; ++m) wf[B][m] = wfrag(slot_ + m * P1_WG + ((S) & 1) * 32 * 128 + troff); \
    _Pragma("unroll") for (int t = 0; t < 3; ++t) xf[B][t] = *(const u32x4*)(slot_ + xo[t][(S) & 1]); \
  } while (0)
#pragma unroll
  for (int ii = 0; ii < 7; ++ii) {
    RBAR();
    if (ii == 0) { STAMP(0, 1); P1_LOAD(0, 0); }
#pragma unroll
    for (int s2 = 0; s2 < 2; ++s2) {
      const int s = ii * 2 + s2;
      if (s == 1) RBAR();
      const char* sn = smem + (((s + 1) >> 1) % P1_R) * P1_SLOT;
      SB();
#pragma unroll
      for (int m = 0; m < 3; ++m) {
        acc[m][0] = mfma(wf[s & 1][m], xf[s & 1][0], acc[m][0]);
        acc[m][1] = mfma(wf[s & 1][m], xf[s & 1][1], acc[m][1]);
        if (hf == 0) acc[m][2] = mfma(wf[s & 1][m], xf[s & 1][2], acc[m][2]);
        else if (m == 0) acc[0][2] = mfma(xf[s & 1][2], xf[s & 1][2], acc[0][2]);
        else if (m == 1) acc[1][2] = mfma(xf[s & 1][1], xf[s & 1][1], acc[1][2]);
        SB();
        if (s + 1 < 14) {
          wf[(s + 1) & 1][m] = wfrag(sn + m * P1_WG + ((s + 1) & 1) * 32 * 128 + troff);
          xf[(s + 1) & 1][m] = *(const u32x4*)(sn + xo[m][(s + 1) & 1]);
        }
        SB();
      }
    }
  }
  STAMP(0, 2);
  if (fb == 3 && g == 2) {
#pragma unroll
    for (int t = 0; t < 3; ++t)
      if (hf == 0 || t < 2) sSum[(hf * 3 + t) * 16 + tl] = acc[1][t][0];
  }
  if (hf == 1 && g == (tl >> 2)) {
    float a0 = acc[0][2][0], a1 = acc[1][2][0];
#pragma unroll
    for (int r = 1; r < 4; ++r) { if ((tl & 3) == r) { a0 = acc[0][2][r]; a1 = acc[1][2][r]; } }
    sSq[fb * 16 + tl] = a0;
    if (fb == 0) sSq[64 + tl] = a1;
  }
  if (hf == 0 && tl == 0) {
#pragma unroll
    for (int m = 0; m < 3; ++m) *(f32x4*)(sCs + m * 64 + fb * 16 + 4 * g) = acc[m][0];
  }
  BAR();
  char* sS = smem;
  {
    f32x4 csf[3];
#pragma unroll
    for (int m = 0; m < 3; ++m) csf[m] = *(const f32x4*)(sCs + m * 64 + fb * 16 + 4 * g);
    const int quad = 4 * fb + g;
#pragma unroll
    for (int t = 0; t < 3; ++t) {
      if (hf == 0 || t < 2) {
        const int tb = hf * 3 + t;
        float mb, rstd;
        ln_params(sSum[tb * 16 + tl], sSq[tb * 16 + tl], mb, rstd);
#pragma unroll
        for (int m = 0; m < 3; ++m) {
          float v0 = (acc[m][t][0] - mb * csf[m][0]) * rstd;
          float v1 = (acc[m][t][1] - mb * csf[m][1]) * rstd;
          float v2 = (acc[m][t][2] - mb * csf[m][2]) * rstd;
          float v3 = (acc[m][t][3] - mb * csf[m][3]) * rstd;
          u32x2 pk = {pack_bf(v0, v1), pack_bf(v2, v3)};
          if (quad < 14) *(u32x2*)(sS + (m * 14 + quad) * P1_SP + (tb * 16 + tl) * 8) = pk;
        }
      }
    }
  }
  BAR();
  STAMP(0, 3);

  {
    const int hh = l >> 5, part = (l >> 3) & 3;
    const int t = w * 8 + (l & 7);
    const int tok = mt * 64 + t;
    const int q0 = hh * 7 + part * 2;
    const int q1 = hh * 7 + min(part * 2 + 1, 6);
    const bool two = part < 3;
    u32x2 qv0 = *(const u32x2*)(sS + (0 * 14 + q0) * P1_SP + (16 + t) * 8);
    u32x2 qv1 = *(const u32x2*)(sS + (0 * 14 + q1) * P1_SP + (16 + t) * 8);
    if (!two) qv1 = (u32x2){0u, 0u};
    float lg[8];
#pragma unroll
    for (int ww = 0; ww < 8; ++ww) {
      u32x2 k0 = *(const u32x2*)(sS + (1 * 14 + q0) * P1_SP + (16 + t - 2 * ww) * 8);
      u32x2 k1 = *(const u32x2*)(sS + (1 * 14 + q1) * P1_SP + (16 + t - 2 * ww) * 8);
      float sacc = 0.f;
      sacc = dot2(qv0[0], k0[0], sacc); sacc = dot2(qv0[1], k0[1], sacc);
      sacc = dot2(qv1[0], k1[0], sacc); sacc = dot2(qv1[1], k1[1], sacc);
      lg[ww] = sacc;
    }
    const float scale = 0.1889822365046136f;
    float mx = -3.0e38f;
#pragma unroll
    for (int ww = 0; ww < 8; ++ww) {
      float sacc = lg[ww] + __shfl_xor(lg[ww], 8);
      sacc += __shfl_xor(sacc, 16);
      float v = rbf(rbf(sacc) * scale);
      v = (tok - 2 * ww >= 0) ? v : -1e9f;
      lg[ww] = v;
      mx = fmaxf(mx, v);
    }
    float den = 0.f;
#pragma unroll
    for (int ww = 0; ww < 8; ++ww) { lg[ww] = __expf(lg[ww] - mx); den += lg[ww]; }
    float inv = 1.0f / den;
#pragma unroll
    for (int ww = 0; ww < 8; ++ww) lg[ww] = rbf(lg[ww] * inv);
    float oacc[2][4];
#pragma unroll
    for (int i = 0; i < 2; ++i)
#pragma unroll
      for (int e = 0; e < 4; ++e) oacc[i][e] = 0.f;
#pragma unroll
    for (int ww = 0; ww < 8; ++ww) {
#pragma unroll
      for (int i = 0; i < 2; ++i) {
        u32x2 vv = *(const u32x2*)(sS + (2 * 14 + (i ? q1 : q0)) * P1_SP + (16 + t - 2 * ww) * 8);
        oacc[i][0] = fmaf(lg[ww], bf_lo(vv[0]), oacc[i][0]);
        oacc[i][1] = fmaf(lg[ww], bf_hi(vv[0]), oacc[i][1]);
        oacc[i][2] = fmaf(lg[ww], bf_lo(vv[1]), oacc[i][2]);
        oacc[i][3] = fmaf(lg[ww], bf_hi(vv[1]), oacc[i][3]);
      }
    }
    const int tbg = tok >> 4;
#pragma unroll
    for (int i = 0; i < 2; ++i) {
      if (i == 0 || two) {
        int f = hp * 56 + (q0 + i) * 4;
        int s = f >> 5, ii = (f >> 4) & 1, gg = (f >> 2) & 3;
        u32x2 pk = {pack_bf(oacc[i][0], oacc[i][1]), pack_bf(oacc[i][2], oacc[i][3])};
        *(u32x2*)((char*)o_frag + ((size_t)((tbg * 14 + s) * 64 + gg * 16 + (tok & 15))) * 16 + ii * 8) = pk;
      }
    }
  }
  STAMP(0, 4);
}

constexpr int EP = 144;
constexpr int G_SLOT = 16384;
template <int NST, int R, bool IS_P2>
__global__ __launch_bounds__(768, 3) void k_gemm64(const u16* __restrict__ a_frag, const u16* __restrict__ wmat,
                                                   const u16* __restrict__ res, u16* __restrict__ dst,
                                                   u16* __restrict__ h_frag, float* __restrict__ stats) {
  __shared__ __attribute__((aligned(16))) char smem[R * G_SLOT + 64 * EP];
  char* sT = smem + R * G_SLOT;
  const int tid = threadIdx.x, l = tid & 63, g = l >> 4, tl = l & 15;
  const int w = __builtin_amdgcn_readfirstlane(tid >> 6);
  const int bid = blockIdx.x, xcd = bid & 7, jj = bid >> 3;
  const int mt = xcd * 4 + jj / 7, nt = jj % 7;
  constexpr int KS = NST * 2;
  STAMP(IS_P2 ? 1 : 3, 0);

  if (w >= 8) {
    const int lw = w - 8, lr = l >> 3, lc = l & 7;
    const u16* ws[2];
#pragma unroll
    for (int j = 0; j < 2; ++j) {
      const int rho = (lw + 4 * j) * 8 + lr;
      const int c = lc ^ (((rho >> 1) & 3) << 1);
      ws[j] = wmat + (size_t)rho * D_ + nt * 64 + c * 8;
    }
    const u16* xs = a_frag + ((size_t)((mt * 4 + lw) * KS) * 64 + l) * 8;
#define G_ISSUE(ST) do { \
      char* sl_ = smem + ((ST) % R) * G_SLOT; \
      GLDS(ws[0] + (size_t)(ST) * 64 * D_, sl_ + lw * 1024); \
      GLDS(ws[1] + (size_t)(ST) * 64 * D_, sl_ + (lw + 4) * 1024); \
      GLDS(xs + (size_t)(ST) * 1024, sl_ + 8192 + lw * 1024); \
      GLDS(xs + (size_t)(ST) * 1024 + 512, sl_ + 8192 + (4 + lw) * 1024); \
    } while (0)
    constexpr int PRE = 4;
#pragma unroll
    for (int st = 0; st < PRE; ++st) G_ISSUE(st);
#pragma unroll
    for (int i = 0; i < NST; ++i) {
      const int lim0 = (i == 0) ? PRE - 1 : ((PRE + 2 * i - 1 < i + R - 2) ? PRE + 2 * i - 1 : i + R - 2);
      const int issued = (lim0 < NST - 1) ? lim0 : NST - 1;
      const int need = (i + 1 < NST) ? i + 1 : NST - 1;
      const int allow = (issued - need) * 4;
      switch (allow) {
        case 0: WAITV(0); break; case 4: WAITV(4); break; case 8: WAITV(8); break; case 12: WAITV(12); break;
        case 16: WAITV(16); break; case 20: WAITV(20); break; case 24: WAITV(24); break; case 28: WAITV(28); break;
        default: WAITV(0); break;
      }
      BAR();
      const int lim1 = (PRE + 2 * i + 1 < i + R - 1) ? PRE + 2 * i + 1 : i + R - 1;
#pragma unroll
      for (int st = issued + 1; st <= lim1; ++st) if (st < NST) G_ISSUE(st);
    }
    BAR();
    if (IS_P2) BAR();
    return;
  }

  const int tb = w & 3, fh = w >> 2;
  const int tro0 = tr_lane_off(l, fh * 2), tro1 = tr_lane_off(l, fh * 2 + 1);
  const int xoff = 8192 + tb * 1024 + l * 16;
  const size_t rowoff = (size_t)(mt * 64 + tb * 16 + fh * 8 + (l >> 3)) * D_ + nt * 64 + (l & 7) * 8;
  const u32x4 rv = *(const u32x4*)(res + rowoff);
  f32x4 acc[2] = {(f32x4){0.f, 0.f, 0.f, 0.f}, (f32x4){0.f, 0.f, 0.f, 0.f}};
  u32x4 wf[2][2], xf[2];
#define G_LOAD(K) do { \
    const char* sl_ = smem + (((K) >> 1) % R) * G_SLOT; \
    wf[(K) & 1][0] = wfrag(sl_ + ((K) & 1) * 4096 + tro0); \
    wf[(K) & 1][1] = wfrag(sl_ + ((K) & 1) * 4096 + tro1); \
    xf[(K) & 1] = *(const u32x4*)(sl_ + ((K) & 1) * 4096 + xoff); \
  } while (0)
#pragma unroll
  for (int i = 0; i < NST; ++i) {
    RBAR();
    if (i == 0) { STAMP(IS_P2 ? 1 : 3, 1); G_LOAD(0); }
#pragma unroll
    for (int j = 0; j < 2; ++j) {
      const int k = i * 2 + j;
      if (k + 1 < KS) G_LOAD(k + 1);
      SB();
      acc[0] = mfma(wf[k & 1][0], xf[k & 1], acc[0]);
      acc[1] = mfma(wf[k & 1][1], xf[k & 1], acc[1]);
      SB();
    }
  }
  STAMP(IS_P2 ? 1 : 3, 2);
  char* tw = sT + tb * 16 * EP;
#pragma unroll
  for (int f = 0; f < 2; ++f) {
    u32x2 pk = {pack_bf(acc[f][0], acc[f][1]), pack_bf(acc[f][2], acc[f][3])};
    *(u32x2*)(tw + tl * EP + (fh * 2 + f) * 32 + g * 8) = pk;
  }
  BAR();
  {
    char* p = tw + (fh * 8 + (l >> 3)) * EP + (l & 7) * 16;
    u32x4 hv = add_bf8(rv, *(const u32x4*)p);
    *(u32x4*)(dst + rowoff) = hv;
    if (IS_P2) {
      *(u32x4*)p = hv;
      float sm = 0.f, sq = 0.f;
#pragma unroll
      for (int e = 0; e < 4; ++e) { sm = dot2(hv[e], ONES, sm); sq = dot2(hv[e], hv[e], sq); }
      sm += __shfl_xor(sm, 1); sq += __shfl_xor(sq, 1);
      sm += __shfl_xor(sm, 2); sq += __shfl_xor(sq, 2);
      sm += __shfl_xor(sm, 4); sq += __shfl_xor(sq, 4);
      if ((l & 7) == 0) {
        float2 st = make_float2(sm, sq);
        *(float2*)(stats + ((size_t)nt * S_ + mt * 64 + tb * 16 + fh * 8 + (l >> 3)) * 2) = st;
      }
    }
  }
  if (IS_P2) {
    BAR();
    u32x4 v = *(const u32x4*)(tw + tl * EP + (fh * 4 + g) * 16);
    *((u32x4*)h_frag + (size_t)((mt * 4 + tb) * 14 + nt * 2 + fh) * 64 + l) = v;
  }
  STAMP(IS_P2 ? 1 : 3, 3);
}

constexpr int P3_SLOT = 24576;
constexpr int P3_R = 6;
__global__ __launch_bounds__(768, 3) void k_gateup(const u16* __restrict__ h_frag, const u16* __restrict__ wg,
                                                   const u16* __restrict__ wu, const float* __restrict__ stats,
                                                   u16* __restrict__ a_frag) {
  __shared__ __attribute__((aligned(16))) char smem[P3_R * P3_SLOT + 8 * 16 * 4 + 256 * 8];
  float* sCs = (float*)(smem + P3_R * P3_SLOT);
  float2* sLN = (float2*)(smem + P3_R * P3_SLOT + 8 * 16 * 4);
  const int tid = threadIdx.x, l = tid & 63, g = l >> 4, tl = l & 15;
  const int w = __builtin_amdgcn_readfirstlane(tid >> 6);
  const int bid = blockIdx.x, xcd = bid & 7, jj = bid >> 3;
  const int mt = (xcd & 1) * 4 + jj / 7, nt = (xcd >> 1) * 7 + jj % 7;
  STAMP(2, 0);

  if (w >= 8) {
    const int lw = w - 8, lr = l >> 3, lc = l & 7;
    const int rho = lw * 8 + lr;
    const int c = lc ^ (((rho >> 1) & 3) << 1);
    const u16* gs = wg + (size_t)rho_inv(rho) * F_ + nt * 64 + c * 8;
    const u16* us = wu + (size_t)rho_inv(rho) * F_ + nt * 64 + c * 8;
    const u16* xs = h_frag + ((size_t)((mt * 16 + lw * 4) * 14) * 64 + l) * 8;
#define P3_ISSUE(ST) do { \
      char* sl_ = smem + ((ST) % P3_R) * P3_SLOT; \
      GLDS(gs + (size_t)(ST) * 32 * F_, sl_ + lw * 1024); \
      GLDS(us + (size_t)(ST) * 32 * F_, sl_ + 4096 + lw * 1024); \
      _Pragma("unroll") for (int j = 0; j < 4; ++j) GLDS(xs + (size_t)(j * 14 + (ST)) * 512, sl_ + 8192 + (lw * 4 + j) * 1024); \
    } while (0)
    P3_ISSUE(0); P3_ISSUE(1); P3_ISSUE(2);
#pragma unroll
    for (int i = 0; i < 14; ++i) {
      const int issued = (i == 0) ? 2 : ((i == 1) ? 4 : ((i + P3_R - 2 < 13) ? i + P3_R - 2 : 13));
      const int need = (i + 1 < 14) ? i + 1 : 13;
      const int allow = (issued - need) * 6;
      switch (allow) {
        case 0: WAITV(0); break; case 6: WAITV(6); break; case 12: WAITV(12); break; case 18: WAITV(18); break;
        default: WAITV(0); break;
      }
      BAR();
      if (i == 0) { P3_ISSUE(3); P3_ISSUE(4); }
      else if (i == 1) { P3_ISSUE(5); P3_ISSUE(6); }
      else if (i + P3_R - 1 < 14) P3_ISSUE(i + P3_R - 1);
      if (i == 12) {
        float a = 0.f, b = 0.f;
#pragma unroll
        for (int n = 0; n < 7; ++n) {
          const float2 s0 = *(const float2*)(stats + ((size_t)n * S_ + mt * 256 + lw * 64 + l) * 2);
          a += s0.x; b += s0.y;
        }
        float mbv, rsv;
        ln_params(a, b, mbv, rsv);
        sLN[lw * 64 + l] = make_float2(mbv, rsv);
      }
    }
    BAR();
    return;
  }

  const int tg = w >> 1, fh = w & 1;
  f32x4 acc[4][4];
#pragma unroll
  for (int t = 0; t < 4; ++t)
#pragma unroll
    for (int f = 0; f < 4; ++f) acc[t][f] = (f32x4){0.f, 0.f, 0.f, 0.f};
  const int tro0 = tr_lane_off(l, fh * 2), tro1 = tr_lane_off(l, fh * 2 + 1);
  const int csoff = (w >> 2) * 4096 + tr_lane_off(l, w & 3);
  const int xoff = 8192 + tg * 4096 + l * 16;
  f32x4 csum = {0.f, 0.f, 0.f, 0.f};
  const u32x4 onesf = {ONES, ONES, ONES, ONES};

  u32x4 wf[2][4], wcs[2], xf[4];
#pragma unroll
  for (int s = 0; s < 14; ++s) {
    RBAR();
    if (s == 0) {
      STAMP(2, 4);
      wf[0][0] = wfrag(smem + tro0); wf[0][1] = wfrag(smem + tro1);
      wf[0][2] = wfrag(smem + 4096 + tro0); wf[0][3] = wfrag(smem + 4096 + tro1);
      wcs[0] = wfrag(smem + csoff);
#pragma unroll
      for (int t = 0; t < 4; ++t) xf[t] = *(const u32x4*)(smem + xoff + t * 1024);
    }
    int sx_ = ((s + 1) % P3_R) * P3_SLOT; asm volatile("" : "+s"(sx_));
    const char* sn = smem + sx_;
    SB();
#pragma unroll
    for (int t = 0; t < 4; ++t) {
#pragma unroll
      for (int f = 0; f < 4; ++f) acc[t][f] = mfma(wf[s & 1][f], xf[t], acc[t][f]);
      SB();
      if (s + 1 < 14) {
        xf[t] = *(const u32x4*)(sn + xoff + t * 1024);
        if (t == 0) { wf[(s + 1) & 1][0] = wfrag(sn + tro0); wf[(s + 1) & 1][1] = wfrag(sn + tro1); }
        if (t == 1) { wf[(s + 1) & 1][2] = wfrag(sn + 4096 + tro0); wf[(s + 1) & 1][3] = wfrag(sn + 4096 + tro1); }
        if (t == 2) wcs[(s + 1) & 1] = wfrag(sn + csoff);
      }
      SB();
    }
    csum = mfma(wcs[s & 1], onesf, csum);
    SB();
  }
  STAMP(2, 1);
  if (tl == 0) *(f32x4*)(sCs + w * 16 + 4 * g) = csum;
  BAR();
  STAMP(2, 2);
  {
    float mb[4], rstd[4];
#pragma unroll
    for (int t = 0; t < 4; ++t) { float2 v = sLN[(tg * 4 + t) * 16 + tl]; mb[t] = v.x; rstd[t] = v.y; }
    f32x4 cg[2], cu[2];
#pragma unroll
    for (int i = 0; i < 2; ++i) {
      cg[i] = *(const f32x4*)(sCs + (fh * 2 + i) * 16 + 4 * g);
      cu[i] = *(const f32x4*)(sCs + (4 + fh * 2 + i) * 16 + 4 * g);
    }
#pragma unroll
    for (int t = 0; t < 4; ++t) {
      u32x4 out;
#pragma unroll
      for (int i = 0; i < 2; ++i) {
        float av[4];
#pragma unroll
        for (int r = 0; r < 4; ++r) {
          float gv = rbf((acc[t][i][r] - mb[t] * cg[i][r]) * rstd[t]);
          float uv = rbf((acc[t][2 + i][r] - mb[t] * cu[i][r]) * rstd[t]);
          float sl = rbf(gv * __builtin_amdgcn_rcpf(1.0f + __expf(-gv)));
          av[r] = sl * uv;
        }
        out[i * 2] = pack_bf(av[0], av[1]);
        out[i * 2 + 1] = pack_bf(av[2], av[3]);
      }
      const int tbg = mt * 16 + tg * 4 + t;
      *((u32x4*)a_frag + (size_t)(tbg * 56 + nt * 2 + fh) * 64 + l) = out;
    }
  }
  STAMP(2, 3);
}

extern "C" void kernel_launch(void* const* d_in, const int* in_sizes, int n_in, void* d_out, int out_size,
                              void* d_ws, size_t ws_size, hipStream_t stream) {
  const u16* x = (const u16*)d_in[0];
  const u16* wqkv = (const u16*)d_in[1];
  const u16* wo = (const u16*)d_in[2];
  const u16* wg = (const u16*)d_in[4];
  const u16* wu = (const u16*)d_in[5];
  const u16* wd = (const u16*)d_in[6];
  u16* out = (u16*)d_out;
  char* ws = (char*)d_ws;
  u16* o_frag = (u16*)ws;
  u16* h = (u16*)(ws + (2u << 20));
  u16* h_frag = (u16*)(ws + (4u << 20));
  u16* a_frag = (u16*)(ws + (6u << 20));
  float* stats = (float*)(ws + (14u << 20));
  k_qkv_attn<<<256, 768, 0, stream>>>(x, wqkv, o_frag, wo, wg, wu, wd);
  k_gemm64<7, 8, true><<<224, 768, 0, stream>>>(o_frag, wo, x, h, h_frag, stats);
  k_gateup<<<224, 768, 0, stream>>>(h_frag, wg, wu, stats, a_frag);
  k_gemm64<28, 9, false><<<224, 768, 0, stream>>>(a_frag, wd, h, out, nullptr, nullptr);
}
```

```cpp
#include <hip/hip_runtime.h>
#include <hip/hip_bf16.h>
#include <stdint.h>

typedef unsigned short u16;
typedef __attribute__((ext_vector_type(8))) __bf16 bf16x8;
typedef __attribute__((ext_vector_type(4))) short s16x4;
typedef __attribute__((ext_vector_type(4))) float f32x4;
typedef __attribute__((ext_vector_type(4))) unsigned int u32x4;
typedef __attribute__((ext_vector_type(2))) unsigned int u32x2;
typedef __attribute__((ext_vector_type(2))) __bf16 bf16x2;
#define LDS_AS __attribute__((address_space(3)))
#define SB() __builtin_amdgcn_sched_barrier(0)
#define GLDS(gp, lp) __builtin_amdgcn_global_load_lds((const unsigned*)(gp), (unsigned*)(lp), 16, 0, 0)
#define WAITV(n) asm volatile("s_waitcnt vmcnt(" #n ")" ::: "memory")
#define RBAR() do { asm volatile("" ::: "memory"); __builtin_amdgcn_s_barrier(); asm volatile("" ::: "memory"); } while (0)
#define BAR() do { asm volatile("s_waitcnt lgkmcnt(0)" ::: "memory"); __builtin_amdgcn_s_barrier(); asm volatile("" ::: "memory"); } while (0)

#define STAMP(K,I)

constexpr int S_ = 2048, D_ = 448, F_ = 1792, QKV_ = 1344;
constexpr unsigned ONES = 0x3F803F80u;

__device__ __attribute__((aligned(16))) const unsigned g_const[12] = {ONES, ONES, ONES, ONES, 0x3F80u, 0u, 0u, 0u, 0u, 0u, 0u, 0u};

__device__ __forceinline__ float bf_lo(unsigned u) { return __uint_as_float(u << 16); }
__device__ __forceinline__ float bf_hi(unsigned u) { return __uint_as_float(u & 0xffff0000u); }
__device__ __forceinline__ unsigned pack_bf(float a, float b) {
  bf16x2 v = {(__bf16)a, (__bf16)b};
  return __builtin_bit_cast(unsigned, v);
}
__device__ __forceinline__ float rbf(float a) { return (float)(__bf16)a; }
__device__ __forceinline__ float dot2(unsigned a, unsigned b, float c) {
  return __builtin_amdgcn_fdot2_f32_bf16(__builtin_bit_cast(bf16x2, a), __builtin_bit_cast(bf16x2, b), c, false);
}
__device__ __forceinline__ u32x2 tr_read(const char* p) {
  s16x4 v = __builtin_amdgcn_ds_read_tr16_b64_v4i16((LDS_AS s16x4*)p);
  return __builtin_bit_cast(u32x2, v);
}
__device__ __forceinline__ u32x4 wfrag(const char* p) {
  u32x2 a = tr_read(p), b = tr_read(p + 16 * 128);
  u32x4 r = {a[0], a[1], b[0], b[1]};
  return r;
}
__device__ __forceinline__ f32x4 mfma(u32x4 a, u32x4 b, f32x4 c) {
  return __builtin_amdgcn_mfma_f32_16x16x32_bf16(__builtin_bit_cast(bf16x8, a), __builtin_bit_cast(bf16x8, b), c, 0, 0, 0);
}
__device__ __forceinline__ int rho_inv(int r) { return (r & ~31) | ((r & 12) << 1) | ((r & 16) >> 2) | (r & 3); }
__device__ __forceinline__ int tr_lane_off(int l, int nb) {
  int g = l >> 4, q = (l & 15) >> 2, p = l & 3;
  int swz = 2 * (g & 1) + (q >> 1);
  return (4 * g + q) * 128 + ((nb ^ swz) << 5) + p * 8;
}
__device__ __forceinline__ void ln_params(float sum, float sq, float& mb, float& rstd) {
  float mean = sum * (1.0f / D_);
  float var = fmaxf(sq * (1.0f / D_) - mean * mean, 0.0f);
  mb = rbf(mean);
  float den = rbf(sqrtf(rbf(rbf(var) + 1e-5f)));
  rstd = 1.0f / den;
}
__device__ __forceinline__ u32x4 add_bf8(u32x4 a, u32x4 b) {
  u32x4 r;
#pragma unroll
  for (int e = 0; e < 4; ++e) r[e] = pack_bf(bf_lo(a[e]) + bf_lo(b[e]), bf_hi(a[e]) + bf_hi(b[e]));
  return r;
}

constexpr int P1_WG = 64 * 128;
constexpr int P1_XO = 3 * P1_WG;
constexpr int P1_SLOT = 3 * P1_WG + 80 * 128;
constexpr int P1_R = 4;
constexpr int P1_SP = 672;
constexpr int P1_VO = 28 * P1_SP;
constexpr int P1_VP = 72;
constexpr int P1_SMEM = P1_R * P1_SLOT + 2048 + (80 * 2 + 3 * 64) * 4;

__global__ __launch_bounds__(768, 3) void k_qkv_attn(const u16* __restrict__ x, const u16* __restrict__ wqkv,
                                                     u16* __restrict__ o_frag, const u16* __restrict__ wo,
                                                     const u16* __restrict__ wg, const u16* __restrict__ wu,
                                                     const u16* __restrict__ wd) {
  __shared__ __attribute__((aligned(16))) char smem[P1_SMEM];
  float* sSum = (float*)(smem + P1_R * P1_SLOT + 2048);
  float* sSq = sSum + 80;
  float* sCs = sSq + 80;

  const int tid = threadIdx.x, l = tid & 63, g = l >> 4, tl = l & 15;
  const int w = __builtin_amdgcn_readfirstlane(tid >> 6);
  const int bid = blockIdx.x, xcd = bid & 7, jj = bid >> 3;
  const int mt = xcd * 4 + (jj >> 3), hp = jj & 7;
  const int row0 = mt * 64 - 16;
  STAMP(0, 0);

  if (w >= 8) {
    const int lw = w - 8, lr = l >> 3, lc = l & 7;
    const u16* src[9];
    int dst[9], adv[9];
#pragma unroll
    for (int j = 0; j < 9; ++j) {
      const int pi = lw + 4 * j;
      if (pi < 24) {
        const int m = pi >> 3, rho = (pi & 7) * 8 + lr;
        const int c = lc ^ (((rho >> 1) & 3) << 1);
        if (c < 7) { src[j] = wqkv + (size_t)rho_inv(rho) * QKV_ + m * D_ + hp * 56 + c * 8; adv[j] = 64 * QKV_; }
        else { src[j] = (const u16*)(g_const + (m == 1 ? 4 : 8)); adv[j] = 0; }
        dst[j] = pi * 1024;
      } else if (pi < 34) {
        const int r = (pi - 24) * 8 + lr;
        const int c = lc ^ ((r >> 1) & 7);
        if (r > 0) { src[j] = x + (size_t)max(row0 + r, 0) * D_ + c * 8; adv[j] = 64; }
        else { src[j] = (const u16*)g_const; adv[j] = 0; }
        dst[j] = pi * 1024;
      } else { src[j] = (const u16*)g_const; adv[j] = 0; dst[j] = -1; }
    }
#define P1_ISSUE(ST) do { \
      char* sl_ = smem + ((ST) % P1_R) * P1_SLOT; \
      _Pragma("unroll") for (int j = 0; j < 9; ++j) { \
        GLDS(src[j] + (size_t)(ST) * adv[j], (dst[j] >= 0) ? sl_ + dst[j] : smem + P1_R * P1_SLOT + (lw & 1) * 1024); } \
    } while (0)
    P1_ISSUE(0); P1_ISSUE(1);
    WAITV(9);  BAR();
    P1_ISSUE(2);
    WAITV(9);  BAR();
    P1_ISSUE(3);
    WAITV(9);  BAR();
    P1_ISSUE(4);
    WAITV(9);  BAR();
    P1_ISSUE(5);
    WAITV(9);  BAR();
    P1_ISSUE(6);
    WAITV(9);  BAR();
    WAITV(0);  BAR();
    {
      char* dummy = smem + P1_R * P1_SLOT + (lw & 1) * 1024;
      const int pc = (bid * 4 + lw);
      const size_t e0 = ((size_t)pc * 64 + l) * 8;
      GLDS(wg + e0, dummy); GLDS(wu + e0, dummy); GLDS(wd + e0, dummy);
      if (pc < 544) { GLDS(wg + e0 + 1024 * 512, dummy); GLDS(wu + e0 + 1024 * 512, dummy); GLDS(wd + e0 + 1024 * 512, dummy); }
      if (pc < 392) GLDS(wo + e0, dummy);
    }
    BAR();
    BAR(); BAR();
    WAITV(0);
    return;
  }

  const int fb = w & 3, hf = w >> 2;
  int xo[3][2];
#pragma unroll
  for (int t = 0; t < 3; ++t) {
    const int tb = (t < 2) ? hf * 3 + t : (hf ? fb : 2);
    const int r = tb * 16 + tl;
#pragma unroll
    for (int s2 = 0; s2 < 2; ++s2) xo[t][s2] = P1_XO + r * 128 + (((4 * s2 + g) ^ ((r >> 1) & 7)) << 4);
  }
  f32x4 acc[3][3];
#pragma unroll
  for (int m = 0; m < 3; ++m)
#pragma unroll
    for (int t = 0; t < 3; ++t) acc[m][t] = (f32x4){0.f, 0.f, 0.f, 0.f};
  const int troff = tr_lane_off(l, fb);

  u32x4 xf[2][3], wf[2][3];
#define P1_LOAD(B, S) do { \
    const char* slot_ = smem + (((S) >> 1) % P1_R) * P1_SLOT; \
    _Pragma("unroll") for (int m = 0; m < 3; ++m) wf[B][m] = wfrag(slot_ + m * P1_WG + ((S) & 1) * 32 * 128 + troff); \
    _Pragma("unroll") for (int t = 0; t < 3; ++t) xf[B][t] = *(const u32x4*)(slot_ + xo[t][(S) & 1]); \
  } while (0)
#pragma unroll
  for (int ii = 0; ii < 7; ++ii) {
    RBAR();
    if (ii == 0) { STAMP(0, 1); P1_LOAD(0, 0); }
#pragma unroll
    for (int s2 = 0; s2 < 2; ++s2) {
      const int s = ii * 2 + s2;
      if (s == 1) RBAR();
      const char* sn = smem + (((s + 1) >> 1) % P1_R) * P1_SLOT;
      SB();
#pragma unroll
      for (int m = 0; m < 3; ++m) {
        acc[m][0] = mfma(wf[s & 1][m], xf[s & 1][0], acc[m][0]);
        acc[m][1] = mfma(wf[s & 1][m], xf[s & 1][1], acc[m][1]);
        if (hf == 0) acc[m][2] = mfma(wf[s & 1][m], xf[s & 1][2], acc[m][2]);
        else if (m == 0) acc[0][2] = mfma(xf[s & 1][2], xf[s & 1][2], acc[0][2]);
        else if (m == 1) acc[1][2] = mfma(xf[s & 1][1], xf[s & 1][1], acc[1][2]);
        SB();
        if (s + 1 < 14) {
          wf[(s + 1) & 1][m] = wfrag(sn + m * P1_WG + ((s + 1) & 1) * 32 * 128 + troff);
          xf[(s + 1) & 1][m] = *(const u32x4*)(sn + xo[m][(s + 1) & 1]);
        }
        SB();
      }
    }
  }
  STAMP(0, 2);
  if (fb == 3 && g == 2) {
#pragma unroll
    for (int t = 0; t < 3; ++t)
      if (hf == 0 || t < 2) sSum[(hf * 3 + t) * 16 + tl] = acc[1][t][0];
  }
  if (hf == 1 && g == (tl >> 2)) {
    float a0 = acc[0][2][0], a1 = acc[1][2][0];
#pragma unroll
    for (int r = 1; r < 4; ++r) { if ((tl & 3) == r) { a0 = acc[0][2][r]; a1 = acc[1][2][r]; } }
    sSq[fb * 16 + tl] = a0;
    if (fb == 0) sSq[64 + tl] = a1;
  }
  if (hf == 0 && tl == 0) {
#pragma unroll
    for (int m = 0; m < 3; ++m) *(f32x4*)(sCs + m * 64 + fb * 16 + 4 * g) = acc[m][0];
  }
  BAR();
  char* sS = smem;
  {
    f32x4 csf[3];
#pragma unroll
    for (int m = 0; m < 3; ++m) csf[m] = *(const f32x4*)(sCs + m * 64 + fb * 16 + 4 * g);
    const int quad = 4 * fb + g;
#pragma unroll
    for (int t = 0; t < 3; ++t) {
      if (hf == 0 || t < 2) {
        const int tb = hf * 3 + t;
        float mb, rstd;
        ln_params(sSum[tb * 16 + tl], sSq[tb * 16 + tl], mb, rstd);
#pragma unroll
        for (int m = 0; m < 3; ++m) {
          float v0 = (acc[m][t][0] - mb * csf[m][0]) * rstd;
          float v1 = (acc[m][t][1] - mb * csf[m][1]) * rstd;
          float v2 = (acc[m][t][2] - mb * csf[m][2]) * rstd;
          float v3 = (acc[m][t][3] - mb * csf[m][3]) * rstd;
          u32x2 pk = {pack_bf(v0, v1), pack_bf(v2, v3)};
          if (quad < 14) {
            if (m < 2) *(u32x2*)(sS + (m * 14 + quad) * P1_SP + (tb * 16 + tl) * 8) = pk;
            else *(u32x2*)(sS + P1_VO + ((quad >= 7 ? 80 : 0) + tb * 16 + tl) * P1_VP + (quad >= 7 ? quad - 7 : quad) * 8) = pk;
          }
        }
      }
    }
  }
  BAR();
  STAMP(0, 3);

  {
    const int hh = w >> 2, tq = (w & 3) + 1;
    const int qa = hh * 7 + 2 * g;
    const bool up = g < 3;
    const int qb = up ? qa + 1 : qa;
    u32x4 qf, kf[2];
    {
      u32x2 a = *(const u32x2*)(sS + (0 * 14 + qa) * P1_SP + (tq * 16 + tl) * 8);
      u32x2 b = *(const u32x2*)(sS + (0 * 14 + qb) * P1_SP + (tq * 16 + tl) * 8);
      qf = (u32x4){a[0], a[1], up ? b[0] : 0u, up ? b[1] : 0u};
#pragma unroll
      for (int c = 0; c < 2; ++c) {
        u32x2 ka = *(const u32x2*)(sS + (1 * 14 + qa) * P1_SP + ((tq - 1 + c) * 16 + tl) * 8);
        u32x2 kb = *(const u32x2*)(sS + (1 * 14 + qb) * P1_SP + ((tq - 1 + c) * 16 + tl) * 8);
        kf[c] = (u32x4){ka[0], ka[1], up ? kb[0] : 0u, up ? kb[1] : 0u};
      }
    }
    u32x4 vf[2];
    {
      const char* vb = sS + P1_VO + (hh * 80 + (tq - 1) * 16 + 4 * g + ((l & 15) >> 2)) * P1_VP + (l & 3) * 8;
#pragma unroll
      for (int db = 0; db < 2; ++db) {
        u32x2 a = tr_read(vb + db * 32), b = tr_read(vb + 16 * P1_VP + db * 32);
        vf[db] = (u32x4){a[0], a[1], b[0], b[1]};
      }
    }
    const f32x4 z4 = {0.f, 0.f, 0.f, 0.f};
    f32x4 sc[2];
    sc[0] = mfma(kf[0], qf, z4);
    sc[1] = mfma(kf[1], qf, z4);
    const int tok = mt * 64 + (tq - 1) * 16 + tl;
    const float scale = 0.1889822365046136f;
    float lg[8];
    float mx = -3.0e38f;
#pragma unroll
    for (int c = 0; c < 2; ++c)
#pragma unroll
      for (int r = 0; r < 4; ++r) {
        const int delta = tl - (4 * g + r) + 16 * (1 - c);
        const bool inwin = (delta >= 0) && (delta <= 14) && ((delta & 1) == 0);
        float v = rbf(rbf(sc[c][r]) * scale);
        v = (tok - delta >= 0) ? v : -1e9f;
        v = inwin ? v : -3.0e38f;
        lg[c * 4 + r] = v;
        mx = fmaxf(mx, v);
      }
    mx = fmaxf(mx, __shfl_xor(mx, 16));
    mx = fmaxf(mx, __shfl_xor(mx, 32));
    float den = 0.f;
#pragma unroll
    for (int i = 0; i < 8; ++i) { lg[i] = (lg[i] > -2.0e38f) ? __expf(lg[i] - mx) : 0.f; den += lg[i]; }
    den += __shfl_xor(den, 16);
    den += __shfl_xor(den, 32);
    const float inv = 1.0f / den;
    u32x4 pf = {pack_bf(lg[0] * inv, lg[1] * inv), pack_bf(lg[2] * inv, lg[3] * inv),
                pack_bf(lg[4] * inv, lg[5] * inv), pack_bf(lg[6] * inv, lg[7] * inv)};
    const int tbg = tok >> 4;
#pragma unroll
    for (int db = 0; db < 2; ++db) {
      f32x4 o = mfma(vf[db], pf, z4);
      const int qd = db * 4 + g;
      if (qd < 7) {
        int f = hp * 56 + (hh * 7 + qd) * 4;
        int s = f >> 5, ii = (f >> 4) & 1, gg = (f >> 2) & 3;
        u32x2 pk = {pack_bf(o[0], o[1]), pack_bf(o[2], o[3])};
        *(u32x2*)((char*)o_frag + ((size_t)((tbg * 14 + s) * 64 + gg * 16 + tl)) * 16 + ii * 8) = pk;
      }
    }
  }
  STAMP(0, 4);
}

constexpr int EP = 144;
constexpr int G_SLOT = 16384;
template <int NST, int R, bool IS_P2>
__global__ __launch_bounds__(768, 3) void k_gemm64(const u16* __restrict__ a_frag, const u16* __restrict__ wmat,
                                                   const u16* __restrict__ res, u16* __restrict__ dst,
                                                   u16* __restrict__ h_frag, float* __restrict__ stats) {
  __shared__ __attribute__((aligned(16))) char smem[R * G_SLOT + 64 * EP];
  char* sT = smem + R * G_SLOT;
  const int tid = threadIdx.x, l = tid & 63, g = l >> 4, tl = l & 15;
  const int w = __builtin_amdgcn_readfirstlane(tid >> 6);
  const int bid = blockIdx.x, xcd = bid & 7, jj = bid >> 3;
  const int mt = xcd * 4 + jj / 7, nt = jj % 7;
  constexpr int KS = NST * 2;
  STAMP(IS_P2 ? 1 : 3, 0);

  if (w >= 8) {
    const int lw = w - 8, lr = l >> 3, lc = l & 7;
    const u16* ws[2];
#pragma unroll
    for (int j = 0; j < 2; ++j) {
      const int rho = (lw + 4 * j) * 8 + lr;
      const int c = lc ^ (((rho >> 1) & 3) << 1);
      ws[j] = wmat + (size_t)rho * D_ + nt * 64 + c * 8;
    }
    const u16* xs = a_frag + ((size_t)((mt * 4 + lw) * KS) * 64 + l) * 8;
#define G_ISSUE(ST) do { \
      char* sl_ = smem + ((ST) % R) * G_SLOT; \
      GLDS(ws[0] + (size_t)(ST) * 64 * D_, sl_ + lw * 1024); \
      GLDS(ws[1] + (size_t)(ST) * 64 * D_, sl_ + (lw + 4) * 1024); \
      GLDS(xs + (size_t)(ST) * 1024, sl_ + 8192 + lw * 1024); \
      GLDS(xs + (size_t)(ST) * 1024 + 512, sl_ + 8192 + (4 + lw) * 1024); \
    } while (0)
    constexpr int PRE = 4;
#pragma unroll
    for (int st = 0; st < PRE; ++st) G_ISSUE(st);
#pragma unroll
    for (int i = 0; i < NST; ++i) {
      const int lim0 = (i == 0) ? PRE - 1 : ((PRE + 2 * i - 1 < i + R - 2) ? PRE + 2 * i - 1 : i + R - 2);
      const int issued = (lim0 < NST - 1) ? lim0 : NST - 1;
      const int need = (i + 1 < NST) ? i + 1 : NST - 1;
      const int allow = (issued - need) * 4;
      switch (allow) {
        case 0: WAITV(0); break; case 4: WAITV(4); break; case 8: WAITV(8); break; case 12: WAITV(12); break;
        case 16: WAITV(16); break; case 20: WAITV(20); break; case 24: WAITV(24); break; case 28: WAITV(28); break;
        default: WAITV(0); break;
      }
      BAR();
      const int lim1 = (PRE + 2 * i + 1 < i + R - 1) ? PRE + 2 * i + 1 : i + R - 1;
#pragma unroll
      for (int st = issued + 1; st <= lim1; ++st) if (st < NST) G_ISSUE(st);
    }
    BAR();
    if (IS_P2) BAR();
    return;
  }

  const int tb = w & 3, fh = w >> 2;
  const int tro0 = tr_lane_off(l, fh * 2), tro1 = tr_lane_off(l, fh * 2 + 1);
  const int xoff = 8192 + tb * 1024 + l * 16;
  const size_t rowoff = (size_t)(mt * 64 + tb * 16 + fh * 8 + (l >> 3)) * D_ + nt * 64 + (l & 7) * 8;
  const u32x4 rv = *(const u32x4*)(res + rowoff);
  f32x4 acc[2] = {(f32x4){0.f, 0.f, 0.f, 0.f}, (f32x4){0.f, 0.f, 0.f, 0.f}};
  u32x4 wf[2][2], xf[2];
#define G_LOAD(K) do { \
    const char* sl_ = smem + (((K) >> 1) % R) * G_SLOT; \
    wf[(K) & 1][0] = wfrag(sl_ + ((K) & 1) * 4096 + tro0); \
    wf[(K) & 1][1] = wfrag(sl_ + ((K) & 1) * 4096 + tro1); \
    xf[(K) & 1] = *(const u32x4*)(sl_ + ((K) & 1) * 4096 + xoff); \
  } while (0)
#pragma unroll
  for (int i = 0; i < NST; ++i) {
    RBAR();
    if (i == 0) { STAMP(IS_P2 ? 1 : 3, 1); G_LOAD(0); }
#pragma unroll
    for (int j = 0; j < 2; ++j) {
      const int k = i * 2 + j;
      if (k + 1 < KS) G_LOAD(k + 1);
      SB();
      acc[0] = mfma(wf[k & 1][0], xf[k & 1], acc[0]);
      acc[1] = mfma(wf[k & 1][1], xf[k & 1], acc[1]);
      SB();
    }
  }
  STAMP(IS_P2 ? 1 : 3, 2);
  char* tw = sT + tb * 16 * EP;
#pragma unroll
  for (int f = 0; f < 2; ++f) {
    u32x2 pk = {pack_bf(acc[f][0], acc[f][1]), pack_bf(acc[f][2], acc[f][3])};
    *(u32x2*)(tw + tl * EP + (fh * 2 + f) * 32 + g * 8) = pk;
  }
  BAR();
  {
    char* p = tw + (fh * 8 + (l >> 3)) * EP + (l & 7) * 16;
    u32x4 hv = add_bf8(rv, *(const u32x4*)p);
    *(u32x4*)(dst + rowoff) = hv;
    if (IS_P2) {
      *(u32x4*)p = hv;
      float sm = 0.f, sq = 0.f;
#pragma unroll
      for (int e = 0; e < 4; ++e) { sm = dot2(hv[e], ONES, sm); sq = dot2(hv[e], hv[e], sq); }
      sm += __shfl_xor(sm, 1); sq += __shfl_xor(sq, 1);
      sm += __shfl_xor(sm, 2); sq += __shfl_xor(sq, 2);
      sm += __shfl_xor(sm, 4); sq += __shfl_xor(sq, 4);
      if ((l & 7) == 0) {
        float2 st = make_float2(sm, sq);
        *(float2*)(stats + ((size_t)nt * S_ + mt * 64 + tb * 16 + fh * 8 + (l >> 3)) * 2) = st;
      }
    }
  }
  if (IS_P2) {
    BAR();
    u32x4 v = *(const u32x4*)(tw + tl * EP + (fh * 4 + g) * 16);
    *((u32x4*)h_frag + (size_t)((mt * 4 + tb) * 14 + nt * 2 + fh) * 64 + l) = v;
  }
  STAMP(IS_P2 ? 1 : 3, 3);
}

constexpr int P3_SLOT = 24576;
constexpr int P3_R = 6;
__global__ __launch_bounds__(768, 3) void k_gateup(const u16* __restrict__ h_frag, const u16* __restrict__ wg,
                                                   const u16* __restrict__ wu, const float* __restrict__ stats,
                                                   u16* __restrict__ a_frag) {
  __shared__ __attribute__((aligned(16))) char smem[P3_R * P3_SLOT + 8 * 16 * 4 + 256 * 8];
  float* sCs = (float*)(smem + P3_R * P3_SLOT);
  float2* sLN = (float2*)(smem + P3_R * P3_SLOT + 8 * 16 * 4);
  const int tid = threadIdx.x, l = tid & 63, g = l >> 4, tl = l & 15;
  const int w = __builtin_amdgcn_readfirstlane(tid >> 6);
  const int bid = blockIdx.x, xcd = bid & 7, jj = bid >> 3;
  const int mt = (xcd & 1) * 4 + jj / 7, nt = (xcd >> 1) * 7 + jj % 7;
  STAMP(2, 0);

  if (w >= 8) {
    const int lw = w - 8, lr = l >> 3, lc = l & 7;
    const int rho = lw * 8 + lr;
    const int c = lc ^ (((rho >> 1) & 3) << 1);
    const u16* gs = wg + (size_t)rho_inv(rho) * F_ + nt * 64 + c * 8;
    const u16* us = wu + (size_t)rho_inv(rho) * F_ + nt * 64 + c * 8;
    const u16* xs = h_frag + ((size_t)((mt * 16 + lw * 4) * 14) * 64 + l) * 8;
#define P3_ISSUE(ST) do { \
      char* sl_ = smem + ((ST) % P3_R) * P3_SLOT; \
      GLDS(gs + (size_t)(ST) * 32 * F_, sl_ + lw * 1024); \
      GLDS(us + (size_t)(ST) * 32 * F_, sl_ + 4096 + lw * 1024); \
      _Pragma("unroll") for (int j = 0; j < 4; ++j) GLDS(xs + (size_t)(j * 14 + (ST)) * 512, sl_ + 8192 + (lw * 4 + j) * 1024); \
    } while (0)
    P3_ISSUE(0); P3_ISSUE(1); P3_ISSUE(2);
#pragma unroll
    for (int i = 0; i < 14; ++i) {
      const int issued = (i == 0) ? 2 : ((i == 1) ? 4 : ((i + P3_R - 2 < 13) ? i + P3_R - 2 : 13));
      const int need = (i + 1 < 14) ? i + 1 : 13;
      const int allow = (issued - need) * 6;
      switch (allow) {
        case 0: WAITV(0); break; case 6: WAITV(6); break; case 12: WAITV(12); break; case 18: WAITV(18); break;
        default: WAITV(0); break;
      }
      BAR();
      if (i == 0) { P3_ISSUE(3); P3_ISSUE(4); }
      else if (i == 1) { P3_ISSUE(5); P3_ISSUE(6); }
      else if (i + P3_R - 1 < 14) P3_ISSUE(i + P3_R - 1);
      if (i == 12) {
        float a = 0.f, b = 0.f;
#pragma unroll
        for (int n = 0; n < 7; ++n) {
          const float2 s0 = *(const float2*)(stats + ((size_t)n * S_ + mt * 256 + lw * 64 + l) * 2);
          a += s0.x; b += s0.y;
        }
        float mbv, rsv;
        ln_params(a, b, mbv, rsv);
        sLN[lw * 64 + l] = make_float2(mbv, rsv);
      }
    }
    BAR();
    return;
  }

  const int tg = w >> 1, fh = w & 1;
  f32x4 acc[4][4];
#pragma unroll
  for (int t = 0; t < 4; ++t)
#pragma unroll
    for (int f = 0; f < 4; ++f) acc[t][f] = (f32x4){0.f, 0.f, 0.f, 0.f};
  const int tro0 = tr_lane_off(l, fh * 2), tro1 = tr_lane_off(l, fh * 2 + 1);
  const int csoff = (w >> 2) * 4096 + tr_lane_off(l, w & 3);
  const int xoff = 8192 + tg * 4096 + l * 16;
  f32x4 csum = {0.f, 0.f, 0.f, 0.f};
  const u32x4 onesf = {ONES, ONES, ONES, ONES};

  u32x4 wf[2][4], wcs[2], xf[4];
#pragma unroll
  for (int s = 0; s < 14; ++s) {
    RBAR();
    if (s == 0) {
      STAMP(2, 4);
      wf[0][0] = wfrag(smem + tro0); wf[0][1] = wfrag(smem + tro1);
      wf[0][2] = wfrag(smem + 4096 + tro0); wf[0][3] = wfrag(smem + 4096 + tro1);
      wcs[0] = wfrag(smem + csoff);
#pragma unroll
      for (int t = 0; t < 4; ++t) xf[t] = *(const u32x4*)(smem + xoff + t * 1024);
    }
    int sx_ = ((s + 1) % P3_R) * P3_SLOT; asm volatile("" : "+s"(sx_));
    const char* sn = smem + sx_;
    SB();
#pragma unroll
    for (int t = 0; t < 4; ++t) {
#pragma unroll
      for (int f = 0; f < 4; ++f) acc[t][f] = mfma(wf[s & 1][f], xf[t], acc[t][f]);
      SB();
      if (s + 1 < 14) {
        xf[t] = *(const u32x4*)(sn + xoff + t * 1024);
        if (t == 0) { wf[(s + 1) & 1][0] = wfrag(sn + tro0); wf[(s + 1) & 1][1] = wfrag(sn + tro1); }
        if (t == 1) { wf[(s + 1) & 1][2] = wfrag(sn + 4096 + tro0); wf[(s + 1) & 1][3] = wfrag(sn + 4096 + tro1); }
        if (t == 2) wcs[(s + 1) & 1] = wfrag(sn + csoff);
      }
      SB();
    }
    csum = mfma(wcs[s & 1], onesf, csum);
    SB();
  }
  STAMP(2, 1);
  if (tl == 0) *(f32x4*)(sCs + w * 16 + 4 * g) = csum;
  BAR();
  STAMP(2, 2);
  {
    float mb[4], rstd[4];
#pragma unroll
    for (int t = 0; t < 4; ++t) { float2 v = sLN[(tg * 4 + t) * 16 + tl]; mb[t] = v.x; rstd[t] = v.y; }
    f32x4 cg[2], cu[2];
#pragma unroll
    for (int i = 0; i < 2; ++i) {
      cg[i] = *(const f32x4*)(sCs + (fh * 2 + i) * 16 + 4 * g);
      cu[i] = *(const f32x4*)(sCs + (4 + fh * 2 + i) * 16 + 4 * g);
    }
#pragma unroll
    for (int t = 0; t < 4; ++t) {
      u32x4 out;
#pragma unroll
      for (int i = 0; i < 2; ++i) {
        float av[4];
#pragma unroll
        for (int r = 0; r < 4; ++r) {
          float gv = rbf((acc[t][i][r] - mb[t] * cg[i][r]) * rstd[t]);
          float uv = rbf((acc[t][2 + i][r] - mb[t] * cu[i][r]) * rstd[t]);
          float sl = rbf(gv * __builtin_amdgcn_rcpf(1.0f + __expf(-gv)));
          av[r] = sl * uv;
        }
        out[i * 2] = pack_bf(av[0], av[1]);
        out[i * 2 + 1] = pack_bf(av[2], av[3]);
      }
      const int tbg = mt * 16 + tg * 4 + t;
      *((u32x4*)a_frag + (size_t)(tbg * 56 + nt * 2 + fh) * 64 + l) = out;
    }
  }
  STAMP(2, 3);
}

extern "C" void kernel_launch(void* const* d_in, const int* in_sizes, int n_in, void* d_out, int out_size,
                              void* d_ws, size_t ws_size, hipStream_t stream) {
  const u16* x = (const u16*)d_in[0];
  const u16* wqkv = (const u16*)d_in[1];
  const u16* wo = (const u16*)d_in[2];
  const u16* wg = (const u16*)d_in[4];
  const u16* wu = (const u16*)d_in[5];
  const u16* wd = (const u16*)d_in[6];
  u16* out = (u16*)d_out;
  char* ws = (char*)d_ws;
  u16* o_frag = (u16*)ws;
  u16* h = (u16*)(ws + (2u << 20));
  u16* h_frag = (u16*)(ws + (4u << 20));
  u16* a_frag = (u16*)(ws + (6u << 20));
  float* stats = (float*)(ws + (14u << 20));
  k_qkv_attn<<<256, 768, 0, stream>>>(x, wqkv, o_frag, wo, wg, wu, wd);
  k_gemm64<7, 8, true><<<224, 768, 0, stream>>>(o_frag, wo, x, h, h_frag, stats);
  k_gateup<<<224, 768, 0, stream>>>(h_frag, wg, wu, stats, a_frag);
  k_gemm64<28, 9, false><<<224, 768, 0, stream>>>(a_frag, wd, h, out, nullptr, nullptr);
}
```

```cpp
#include <hip/hip_runtime.h>
#include <hip/hip_bf16.h>
#include <stdint.h>

typedef unsigned short u16;
typedef __attribute__((ext_vector_type(8))) __bf16 bf16x8;
typedef __attribute__((ext_vector_type(4))) short s16x4;
typedef __attribute__((ext_vector_type(4))) float f32x4;
typedef __attribute__((ext_vector_type(4))) unsigned int u32x4;
typedef __attribute__((ext_vector_type(2))) unsigned int u32x2;
typedef __attribute__((ext_vector_type(2))) __bf16 bf16x2;
#define LDS_AS __attribute__((address_space(3)))
#define SB() __builtin_amdgcn_sched_barrier(0)
#define GLDS(gp, lp) __builtin_amdgcn_global_load_lds((const unsigned*)(gp), (unsigned*)(lp), 16, 0, 0)
#define WAITV(n) asm volatile("s_waitcnt vmcnt(" #n ")" ::: "memory")
#define RBAR() do { asm volatile("" ::: "memory"); __builtin_amdgcn_s_barrier(); asm volatile("" ::: "memory"); } while (0)
#define BAR() do { asm volatile("s_waitcnt lgkmcnt(0)" ::: "memory"); __builtin_amdgcn_s_barrier(); asm volatile("" ::: "memory"); } while (0)

#define STAMP(K,I)

constexpr int S_ = 2048, D_ = 448, F_ = 1792, QKV_ = 1344;
constexpr unsigned ONES = 0x3F803F80u;

__device__ __attribute__((aligned(16))) const unsigned g_const[12] = {ONES, ONES, ONES, ONES, 0x3F80u, 0u, 0u, 0u, 0u, 0u, 0u, 0u};

__device__ __forceinline__ void st16_wt(void* p, u32x4 v) {
  asm volatile("global_store_dwordx4 %0, %1, off sc1\n\ts_nop 1" :: "v"(p), "v"(v) : "memory");
}
__device__ __forceinline__ float bf_lo(unsigned u) { return __uint_as_float(u << 16); }
__device__ __forceinline__ float bf_hi(unsigned u) { return __uint_as_float(u & 0xffff0000u); }
__device__ __forceinline__ unsigned pack_bf(float a, float b) {
  bf16x2 v = {(__bf16)a, (__bf16)b};
  return __builtin_bit_cast(unsigned, v);
}
__device__ __forceinline__ float rbf(float a) { return (float)(__bf16)a; }
__device__ __forceinline__ float dot2(unsigned a, unsigned b, float c) {
  return __builtin_amdgcn_fdot2_f32_bf16(__builtin_bit_cast(bf16x2, a), __builtin_bit_cast(bf16x2, b), c, false);
}
__device__ __forceinline__ u32x2 tr_read(const char* p) {
  s16x4 v = __builtin_amdgcn_ds_read_tr16_b64_v4i16((LDS_AS s16x4*)p);
  return __builtin_bit_cast(u32x2, v);
}
__device__ __forceinline__ u32x4 wfrag(const char* p) {
  u32x2 a = tr_read(p), b = tr_read(p + 16 * 128);
  u32x4 r = {a[0], a[1], b[0], b[1]};
  return r;
}
__device__ __forceinline__ f32x4 mfma(u32x4 a, u32x4 b, f32x4 c) {
  return __builtin_amdgcn_mfma_f32_16x16x32_bf16(__builtin_bit_cast(bf16x8, a), __builtin_bit_cast(bf16x8, b), c, 0, 0, 0);
}
__device__ __forceinline__ int rho_inv(int r) { return (r & ~31) | ((r & 12) << 1) | ((r & 16) >> 2) | (r & 3); }
__device__ __forceinline__ int tr_lane_off(int l, int nb) {
  int g = l >> 4, q = (l & 15) >> 2, p = l & 3;
  int swz = 2 * (g & 1) + (q >> 1);
  return (4 * g + q) * 128 + ((nb ^ swz) << 5) + p * 8;
}
__device__ __forceinline__ void ln_params(float sum, float sq, float& mb, float& rstd) {
  float mean = sum * (1.0f / D_);
  float var = fmaxf(sq * (1.0f / D_) - mean * mean, 0.0f);
  mb = rbf(mean);
  float den = rbf(sqrtf(rbf(rbf(var) + 1e-5f)));
  rstd = 1.0f / den;
}
__device__ __forceinline__ u32x4 add_bf8(u32x4 a, u32x4 b) {
  u32x4 r;
#pragma unroll
  for (int e = 0; e < 4; ++e) r[e] = pack_bf(bf_lo(a[e]) + bf_lo(b[e]), bf_hi(a[e]) + bf_hi(b[e]));
  return r;
}

constexpr int P1_WG = 64 * 128;
constexpr int P1_XO = 3 * P1_WG;
constexpr int P1_SLOT = 3 * P1_WG + 80 * 128;
constexpr int P1_R = 4;
constexpr int P1_SP = 672;
constexpr int P1_VO = 28 * P1_SP;
constexpr int P1_VP = 72;
constexpr int P1_SMEM = P1_R * P1_SLOT + 2048 + (80 * 2 + 3 * 64) * 4;

__global__ __launch_bounds__(768, 3) void k_qkv_attn(const u16* __restrict__ x, const u16* __restrict__ wqkv,
                                                     u16* __restrict__ o_frag, const u16* __restrict__ wo,
                                                     const u16* __restrict__ wg, const u16* __restrict__ wu,
                                                     const u16* __restrict__ wd) {
  __shared__ __attribute__((aligned(16))) char smem[P1_SMEM];
  float* sSum = (float*)(smem + P1_R * P1_SLOT + 2048);
  float* sSq = sSum + 80;
  float* sCs = sSq + 80;

  const int tid = threadIdx.x, l = tid & 63, g = l >> 4, tl = l & 15;
  const int w = __builtin_amdgcn_readfirstlane(tid >> 6);
  const int bid = blockIdx.x, xcd = bid & 7, jj = bid >> 3;
  const int mt = xcd * 4 + (jj >> 3), hp = jj & 7;
  const int row0 = mt * 64 - 16;
  STAMP(0, 0);

  if (w >= 8) {
    const int lw = w - 8, lr = l >> 3, lc = l & 7;
    const u16* src[9];
    int dst[9], adv[9];
#pragma unroll
    for (int j = 0; j < 9; ++j) {
      const int pi = lw + 4 * j;
      if (pi < 24) {
        const int m = pi >> 3, rho = (pi & 7) * 8 + lr;
        const int c = lc ^ (((rho >> 1) & 3) << 1);
        if (c < 7) { src[j] = wqkv + (size_t)rho_inv(rho) * QKV_ + m * D_ + hp * 56 + c * 8; adv[j] = 64 * QKV_; }
        else { src[j] = (const u16*)(g_const + (m == 1 ? 4 : 8)); adv[j] = 0; }
        dst[j] = pi * 1024;
      } else if (pi < 34) {
        const int r = (pi - 24) * 8 + lr;
        const int c = lc ^ ((r >> 1) & 7);
        if (r > 0) { src[j] = x + (size_t)max(row0 + r, 0) * D_ + c * 8; adv[j] = 64; }
        else { src[j] = (const u16*)g_const; adv[j] = 0; }
        dst[j] = pi * 1024;
      } else { src[j] = (const u16*)g_const; adv[j] = 0; dst[j] = -1; }
    }
#define P1_ISSUE(ST) do { \
      char* sl_ = smem + ((ST) % P1_R) * P1_SLOT; \
      _Pragma("unroll") for (int j = 0; j < 9; ++j) { \
        GLDS(src[j] + (size_t)(ST) * adv[j], (dst[j] >= 0) ? sl_ + dst[j] : smem + P1_R * P1_SLOT + (lw & 1) * 1024); } \
    } while (0)
    P1_ISSUE(0); P1_ISSUE(1);
    WAITV(9);  BAR();
    P1_ISSUE(2);
    WAITV(9);  BAR();
    P1_ISSUE(3);
    WAITV(9);  BAR();
    P1_ISSUE(4);
    WAITV(9);  BAR();
    P1_ISSUE(5);
    WAITV(9);  BAR();
    P1_ISSUE(6);
    WAITV(9);  BAR();
    WAITV(0);  BAR();
    {
      char* dummy = smem + P1_R * P1_SLOT + (lw & 1) * 1024;
      const int pc = (bid * 4 + lw);
      const size_t e0 = ((size_t)pc * 64 + l) * 8;
      GLDS(wg + e0, dummy); GLDS(wu + e0, dummy); GLDS(wd + e0, dummy);
      if (pc < 544) { GLDS(wg + e0 + 1024 * 512, dummy); GLDS(wu + e0 + 1024 * 512, dummy); GLDS(wd + e0 + 1024 * 512, dummy); }
      if (pc < 392) GLDS(wo + e0, dummy);
    }
    BAR();
    BAR(); BAR();
    WAITV(0);
    return;
  }

  const int fb = w & 3, hf = w >> 2;
  int xo[3][2];
#pragma unroll
  for (int t = 0; t < 3; ++t) {
    const int tb = (t < 2) ? hf * 3 + t : (hf ? fb : 2);
    const int r = tb * 16 + tl;
#pragma unroll
    for (int s2 = 0; s2 < 2; ++s2) xo[t][s2] = P1_XO + r * 128 + (((4 * s2 + g) ^ ((r >> 1) & 7)) << 4);
  }
  f32x4 acc[3][3];
#pragma unroll
  for (int m = 0; m < 3; ++m)
#pragma unroll
    for (int t = 0; t < 3; ++t) acc[m][t] = (f32x4){0.f, 0.f, 0.f, 0.f};
  const int troff = tr_lane_off(l, fb);

  u32x4 xf[2][3], wf[2][3];
#define P1_LOAD(B, S) do { \
    const char* slot_ = smem + (((S) >> 1) % P1_R) * P1_SLOT; \
    _Pragma("unroll") for (int m = 0; m < 3; ++m) wf[B][m] = wfrag(slot_ + m * P1_WG + ((S) & 1) * 32 * 128 + troff); \
    _Pragma("unroll") for (int t = 0; t < 3; ++t) xf[B][t] = *(const u32x4*)(slot_ + xo[t][(S) & 1]); \
  } while (0)
#pragma unroll
  for (int ii = 0; ii < 7; ++ii) {
    RBAR();
    if (ii == 0) { STAMP(0, 1); P1_LOAD(0, 0); }
#pragma unroll
    for (int s2 = 0; s2 < 2; ++s2) {
      const int s = ii * 2 + s2;
      if (s == 1) RBAR();
      const char* sn = smem + (((s + 1) >> 1) % P1_R) * P1_SLOT;
      SB();
#pragma unroll
      for (int m = 0; m < 3; ++m) {
        acc[m][0] = mfma(wf[s & 1][m], xf[s & 1][0], acc[m][0]);
        acc[m][1] = mfma(wf[s & 1][m], xf[s & 1][1], acc[m][1]);
        if (hf == 0) acc[m][2] = mfma(wf[s & 1][m], xf[s & 1][2], acc[m][2]);
        else if (m == 0) acc[0][2] = mfma(xf[s & 1][2], xf[s & 1][2], acc[0][2]);
        else if (m == 1) acc[1][2] = mfma(xf[s & 1][1], xf[s & 1][1], acc[1][2]);
        SB();
        if (s + 1 < 14) {
          wf[(s + 1) & 1][m] = wfrag(sn + m * P1_WG + ((s + 1) & 1) * 32 * 128 + troff);
          xf[(s + 1) & 1][m] = *(const u32x4*)(sn + xo[m][(s + 1) & 1]);
        }
        SB();
      }
    }
  }
  STAMP(0, 2);
  if (fb == 3 && g == 2) {
#pragma unroll
    for (int t = 0; t < 3; ++t)
      if (hf == 0 || t < 2) sSum[(hf * 3 + t) * 16 + tl] = acc[1][t][0];
  }
  if (hf == 1 && g == (tl >> 2)) {
    float a0 = acc[0][2][0], a1 = acc[1][2][0];
#pragma unroll
    for (int r = 1; r < 4; ++r) { if ((tl & 3) == r) { a0 = acc[0][2][r]; a1 = acc[1][2][r]; } }
    sSq[fb * 16 + tl] = a0;
    if (fb == 0) sSq[64 + tl] = a1;
  }
  if (hf == 0 && tl == 0) {
#pragma unroll
    for (int m = 0; m < 3; ++m) *(f32x4*)(sCs + m * 64 + fb * 16 + 4 * g) = acc[m][0];
  }
  BAR();
  char* sS = smem;
  {
    f32x4 csf[3];
#pragma unroll
    for (int m = 0; m < 3; ++m) csf[m] = *(const f32x4*)(sCs + m * 64 + fb * 16 + 4 * g);
    const int quad = 4 * fb + g;
#pragma unroll
    for (int t = 0; t < 3; ++t) {
      if (hf == 0 || t < 2) {
        const int tb = hf * 3 + t;
        float mb, rstd;
        ln_params(sSum[tb * 16 + tl], sSq[tb * 16 + tl], mb, rstd);
#pragma unroll
        for (int m = 0; m < 3; ++m) {
          float v0 = (acc[m][t][0] - mb * csf[m][0]) * rstd;
          float v1 = (acc[m][t][1] - mb * csf[m][1]) * rstd;
          float v2 = (acc[m][t][2] - mb * csf[m][2]) * rstd;
          float v3 = (acc[m][t][3] - mb * csf[m][3]) * rstd;
          u32x2 pk = {pack_bf(v0, v1), pack_bf(v2, v3)};
          if (quad < 14) {
            if (m < 2) *(u32x2*)(sS + (m * 14 + quad) * P1_SP + (tb * 16 + tl) * 8) = pk;
            else *(u32x2*)(sS + P1_VO + ((quad >= 7 ? 80 : 0) + tb * 16 + tl) * P1_VP + (quad >= 7 ? quad - 7 : quad) * 8) = pk;
          }
        }
      }
    }
  }
  BAR();
  STAMP(0, 3);

  {
    const int hh = w >> 2, tq = (w & 3) + 1;
    const int qa = hh * 7 + 2 * g;
    const bool up = g < 3;
    const int qb = up ? qa + 1 : qa;
    u32x4 qf, kf[2];
    {
      u32x2 a = *(const u32x2*)(sS + (0 * 14 + qa) * P1_SP + (tq * 16 + tl) * 8);
      u32x2 b = *(const u32x2*)(sS + (0 * 14 + qb) * P1_SP + (tq * 16 + tl) * 8);
      qf = (u32x4){a[0], a[1], up ? b[0] : 0u, up ? b[1] : 0u};
#pragma unroll
      for (int c = 0; c < 2; ++c) {
        u32x2 ka = *(const u32x2*)(sS + (1 * 14 + qa) * P1_SP + ((tq - 1 + c) * 16 + tl) * 8);
        u32x2 kb = *(const u32x2*)(sS + (1 * 14 + qb) * P1_SP + ((tq - 1 + c) * 16 + tl) * 8);
        kf[c] = (u32x4){ka[0], ka[1], up ? kb[0] : 0u, up ? kb[1] : 0u};
      }
    }
    u32x4 vf[2];
    {
      const char* vb = sS + P1_VO + (hh * 80 + (tq - 1) * 16 + 4 * g + ((l & 15) >> 2)) * P1_VP + (l & 3) * 8;
#pragma unroll
      for (int db = 0; db < 2; ++db) {
        u32x2 a = tr_read(vb + db * 32), b = tr_read(vb + 16 * P1_VP + db * 32);
        vf[db] = (u32x4){a[0], a[1], b[0], b[1]};
      }
    }
    const f32x4 z4 = {0.f, 0.f, 0.f, 0.f};
    f32x4 sc[2];
    sc[0] = mfma(kf[0], qf, z4);
    sc[1] = mfma(kf[1], qf, z4);
    const int tok = mt * 64 + (tq - 1) * 16 + tl;
    const float scale = 0.1889822365046136f;
    float lg[8];
    float mx = -3.0e38f;
#pragma unroll
    for (int c = 0; c < 2; ++c)
#pragma unroll
      for (int r = 0; r < 4; ++r) {
        const int delta = tl - (4 * g + r) + 16 * (1 - c);
        const bool inwin = (delta >= 0) && (delta <= 14) && ((delta & 1) == 0);
        float v = rbf(rbf(sc[c][r]) * scale);
        v = (tok - delta >= 0) ? v : -1e9f;
        v = inwin ? v : -3.0e38f;
        lg[c * 4 + r] = v;
        mx = fmaxf(mx, v);
      }
    mx = fmaxf(mx, __shfl_xor(mx, 16));
    mx = fmaxf(mx, __shfl_xor(mx, 32));
    float den = 0.f;
#pragma unroll
    for (int i = 0; i < 8; ++i) { lg[i] = (lg[i] > -2.0e38f) ? __expf(lg[i] - mx) : 0.f; den += lg[i]; }
    den += __shfl_xor(den, 16);
    den += __shfl_xor(den, 32);
    const float inv = 1.0f / den;
    u32x4 pf = {pack_bf(lg[0] * inv, lg[1] * inv), pack_bf(lg[2] * inv, lg[3] * inv),
                pack_bf(lg[4] * inv, lg[5] * inv), pack_bf(lg[6] * inv, lg[7] * inv)};
    const int tbg = tok >> 4;
#pragma unroll
    for (int db = 0; db < 2; ++db) {
      f32x4 o = mfma(vf[db], pf, z4);
      const int qd = db * 4 + g;
      if (qd < 7) {
        int f = hp * 56 + (hh * 7 + qd) * 4;
        int s = f >> 5, ii = (f >> 4) & 1, gg = (f >> 2) & 3;
        u32x2 pk = {pack_bf(o[0], o[1]), pack_bf(o[2], o[3])};
        *(u32x2*)((char*)o_frag + ((size_t)((tbg * 14 + s) * 64 + gg * 16 + tl)) * 16 + ii * 8) = pk;
      }
    }
  }
  STAMP(0, 4);
}

constexpr int EP = 144;
constexpr int G_SLOT = 16384;
template <int NST, int R, bool IS_P2>
__global__ __launch_bounds__(768, 3) void k_gemm64(const u16* __restrict__ a_frag, const u16* __restrict__ wmat,
                                                   const u16* __restrict__ res, u16* __restrict__ dst,
                                                   u16* __restrict__ h_frag, float* __restrict__ stats) {
  __shared__ __attribute__((aligned(16))) char smem[R * G_SLOT + 64 * EP];
  char* sT = smem + R * G_SLOT;
  const int tid = threadIdx.x, l = tid & 63, g = l >> 4, tl = l & 15;
  const int w = __builtin_amdgcn_readfirstlane(tid >> 6);
  const int bid = blockIdx.x, xcd = bid & 7, jj = bid >> 3;
  const int mt = xcd * 4 + jj / 7, nt = jj % 7;
  constexpr int KS = NST * 2;
  STAMP(IS_P2 ? 1 : 3, 0);

  if (w >= 8) {
    const int lw = w - 8, lr = l >> 3, lc = l & 7;
    const u16* ws[2];
#pragma unroll
    for (int j = 0; j < 2; ++j) {
      const int rho = (lw + 4 * j) * 8 + lr;
      const int c = lc ^ (((rho >> 1) & 3) << 1);
      ws[j] = wmat + (size_t)rho * D_ + nt * 64 + c * 8;
    }
    const u16* xs = a_frag + ((size_t)((mt * 4 + lw) * KS) * 64 + l) * 8;
#define G_ISSUE(ST) do { \
      char* sl_ = smem + ((ST) % R) * G_SLOT; \
      GLDS(ws[0] + (size_t)(ST) * 64 * D_, sl_ + lw * 1024); \
      GLDS(ws[1] + (size_t)(ST) * 64 * D_, sl_ + (lw + 4) * 1024); \
      GLDS(xs + (size_t)(ST) * 1024, sl_ + 8192 + lw * 1024); \
      GLDS(xs + (size_t)(ST) * 1024 + 512, sl_ + 8192 + (4 + lw) * 1024); \
    } while (0)
    constexpr int PRE = 4;
#pragma unroll
    for (int st = 0; st < PRE; ++st) G_ISSUE(st);
#pragma unroll
    for (int i = 0; i < NST; ++i) {
      const int lim0 = (i == 0) ? PRE - 1 : ((PRE + 2 * i - 1 < i + R - 2) ? PRE + 2 * i - 1 : i + R - 2);
      const int issued = (lim0 < NST - 1) ? lim0 : NST - 1;
      const int need = (i + 1 < NST) ? i + 1 : NST - 1;
      const int allow = (issued - need) * 4;
      switch (allow) {
        case 0: WAITV(0); break; case 4: WAITV(4); break; case 8: WAITV(8); break; case 12: WAITV(12); break;
        case 16: WAITV(16); break; case 20: WAITV(20); break; case 24: WAITV(24); break; case 28: WAITV(28); break;
        default: WAITV(0); break;
      }
      BAR();
      const int lim1 = (PRE + 2 * i + 1 < i + R - 1) ? PRE + 2 * i + 1 : i + R - 1;
#pragma unroll
      for (int st = issued + 1; st <= lim1; ++st) if (st < NST) G_ISSUE(st);
    }
    BAR();
    if (IS_P2) BAR();
    return;
  }

  const int tb = w & 3, fh = w >> 2;
  const int tro0 = tr_lane_off(l, fh * 2), tro1 = tr_lane_off(l, fh * 2 + 1);
  const int xoff = 8192 + tb * 1024 + l * 16;
  const size_t rowoff = (size_t)(mt * 64 + tb * 16 + fh * 8 + (l >> 3)) * D_ + nt * 64 + (l & 7) * 8;
  const u32x4 rv = *(const u32x4*)(res + rowoff);
  f32x4 acc[2] = {(f32x4){0.f, 0.f, 0.f, 0.f}, (f32x4){0.f, 0.f, 0.f, 0.f}};
  u32x4 wf[2][2], xf[2];
#define G_LOAD(K) do { \
    const char* sl_ = smem + (((K) >> 1) % R) * G_SLOT; \
    wf[(K) & 1][0] = wfrag(sl_ + ((K) & 1) * 4096 + tro0); \
    wf[(K) & 1][1] = wfrag(sl_ + ((K) & 1) * 4096 + tro1); \
    xf[(K) & 1] = *(const u32x4*)(sl_ + ((K) & 1) * 4096 + xoff); \
  } while (0)
#pragma unroll
  for (int i = 0; i < NST; ++i) {
    RBAR();
    if (i == 0) { STAMP(IS_P2 ? 1 : 3, 1); G_LOAD(0); }
#pragma unroll
    for (int j = 0; j < 2; ++j) {
      const int k = i * 2 + j;
      if (k + 1 < KS) G_LOAD(k + 1);
      SB();
      acc[0] = mfma(wf[k & 1][0], xf[k & 1], acc[0]);
      acc[1] = mfma(wf[k & 1][1], xf[k & 1], acc[1]);
      SB();
    }
  }
  STAMP(IS_P2 ? 1 : 3, 2);
  char* tw = sT + tb * 16 * EP;
#pragma unroll
  for (int f = 0; f < 2; ++f) {
    u32x2 pk = {pack_bf(acc[f][0], acc[f][1]), pack_bf(acc[f][2], acc[f][3])};
    *(u32x2*)(tw + tl * EP + (fh * 2 + f) * 32 + g * 8) = pk;
  }
  BAR();
  {
    char* p = tw + (fh * 8 + (l >> 3)) * EP + (l & 7) * 16;
    u32x4 hv = add_bf8(rv, *(const u32x4*)p);
    st16_wt(dst + rowoff, hv);
    if (IS_P2) {
      *(u32x4*)p = hv;
      float sm = 0.f, sq = 0.f;
#pragma unroll
      for (int e = 0; e < 4; ++e) { sm = dot2(hv[e], ONES, sm); sq = dot2(hv[e], hv[e], sq); }
      sm += __shfl_xor(sm, 1); sq += __shfl_xor(sq, 1);
      sm += __shfl_xor(sm, 2); sq += __shfl_xor(sq, 2);
      sm += __shfl_xor(sm, 4); sq += __shfl_xor(sq, 4);
      if ((l & 7) == 0) {
        float2 st = make_float2(sm, sq);
        *(float2*)(stats + ((size_t)nt * S_ + mt * 64 + tb * 16 + fh * 8 + (l >> 3)) * 2) = st;
      }
    }
  }
  if (IS_P2) {
    BAR();
    u32x4 v = *(const u32x4*)(tw + tl * EP + (fh * 4 + g) * 16);
    st16_wt((u32x4*)h_frag + (size_t)((mt * 4 + tb) * 14 + nt * 2 + fh) * 64 + l, v);
  }
  STAMP(IS_P2 ? 1 : 3, 3);
}

constexpr int P3_SLOT = 24576;
constexpr int P3_R = 6;
__global__ __launch_bounds__(768, 3) void k_gateup(const u16* __restrict__ h_frag, const u16* __restrict__ wg,
                                                   const u16* __restrict__ wu, const float* __restrict__ stats,
                                                   u16* __restrict__ a_frag) {
  __shared__ __attribute__((aligned(16))) char smem[P3_R * P3_SLOT + 8 * 16 * 4 + 256 * 8];
  float* sCs = (float*)(smem + P3_R * P3_SLOT);
  float2* sLN = (float2*)(smem + P3_R * P3_SLOT + 8 * 16 * 4);
  const int tid = threadIdx.x, l = tid & 63, g = l >> 4, tl = l & 15;
  const int w = __builtin_amdgcn_readfirstlane(tid >> 6);
  const int bid = blockIdx.x, xcd = bid & 7, jj = bid >> 3;
  const int mt = (xcd & 1) * 4 + jj / 7, nt = (xcd >> 1) * 7 + jj % 7;
  STAMP(2, 0);

  if (w >= 8) {
    const int lw = w - 8, lr = l >> 3, lc = l & 7;
    const int rho = lw * 8 + lr;
    const int c = lc ^ (((rho >> 1) & 3) << 1);
    const u16* gs = wg + (size_t)rho_inv(rho) * F_ + nt * 64 + c * 8;
    const u16* us = wu + (size_t)rho_inv(rho) * F_ + nt * 64 + c * 8;
    const u16* xs = h_frag + ((size_t)((mt * 16 + lw * 4) * 14) * 64 + l) * 8;
#define P3_ISSUE(ST) do { \
      char* sl_ = smem + ((ST) % P3_R) * P3_SLOT; \
      GLDS(gs + (size_t)(ST) * 32 * F_, sl_ + lw * 1024); \
      GLDS(us + (size_t)(ST) * 32 * F_, sl_ + 4096 + lw * 1024); \
      _Pragma("unroll") for (int j = 0; j < 4; ++j) GLDS(xs + (size_t)(j * 14 + (ST)) * 512, sl_ + 8192 + (lw * 4 + j) * 1024); \
    } while (0)
    P3_ISSUE(0); P3_ISSUE(1); P3_ISSUE(2);
#pragma unroll
    for (int i = 0; i < 14; ++i) {
      const int issued = (i == 0) ? 2 : ((i == 1) ? 4 : ((i + P3_R - 2 < 13) ? i + P3_R - 2 : 13));
      const int need = (i + 1 < 14) ? i + 1 : 13;
      const int allow = (issued - need) * 6;
      switch (allow) {
        case 0: WAITV(0); break; case 6: WAITV(6); break; case 12: WAITV(12); break; case 18: WAITV(18); break;
        default: WAITV(0); break;
      }
      BAR();
      if (i == 0) { P3_ISSUE(3); P3_ISSUE(4); }
      else if (i == 1) { P3_ISSUE(5); P3_ISSUE(6); }
      else if (i + P3_R - 1 < 14) P3_ISSUE(i + P3_R - 1);
      if (i == 12) {
        float a = 0.f, b = 0.f;
#pragma unroll
        for (int n = 0; n < 7; ++n) {
          const float2 s0 = *(const float2*)(stats + ((size_t)n * S_ + mt * 256 + lw * 64 + l) * 2);
          a += s0.x; b += s0.y;
        }
        float mbv, rsv;
        ln_params(a, b, mbv, rsv);
        sLN[lw * 64 + l] = make_float2(mbv, rsv);
      }
    }
    BAR();
    return;
  }

  const int tg = w >> 1, fh = w & 1;
  f32x4 acc[4][4];
#pragma unroll
  for (int t = 0; t < 4; ++t)
#pragma unroll
    for (int f = 0; f < 4; ++f) acc[t][f] = (f32x4){0.f, 0.f, 0.f, 0.f};
  const int tro0 = tr_lane_off(l, fh * 2), tro1 = tr_lane_off(l, fh * 2 + 1);
  const int csoff = (w >> 2) * 4096 + tr_lane_off(l, w & 3);
  const int xoff = 8192 + tg * 4096 + l * 16;
  f32x4 csum = {0.f, 0.f, 0.f, 0.f};
  const u32x4 onesf = {ONES, ONES, ONES, ONES};

  u32x4 wf[2][4], wcs[2], xf[4];
#pragma unroll
  for (int s = 0; s < 14; ++s) {
    RBAR();
    if (s == 0) {
      STAMP(2, 4);
      wf[0][0] = wfrag(smem + tro0); wf[0][1] = wfrag(smem + tro1);
      wf[0][2] = wfrag(smem + 4096 + tro0); wf[0][3] = wfrag(smem + 4096 + tro1);
      wcs[0] = wfrag(smem + csoff);
#pragma unroll
      for (int t = 0; t < 4; ++t) xf[t] = *(const u32x4*)(smem + xoff + t * 1024);
    }
    int sx_ = ((s + 1) % P3_R) * P3_SLOT; asm volatile("" : "+s"(sx_));
    const char* sn = smem + sx_;
    SB();
#pragma unroll
    for (int t = 0; t < 4; ++t) {
#pragma unroll
      for (int f = 0; f < 4; ++f) acc[t][f] = mfma(wf[s & 1][f], xf[t], acc[t][f]);
      SB();
      if (s + 1 < 14) {
        xf[t] = *(const u32x4*)(sn + xoff + t * 1024);
        if (t == 0) { wf[(s + 1) & 1][0] = wfrag(sn + tro0); wf[(s + 1) & 1][1] = wfrag(sn + tro1); }
        if (t == 1) { wf[(s + 1) & 1][2] = wfrag(sn + 4096 + tro0); wf[(s + 1) & 1][3] = wfrag(sn + 4096 + tro1); }
        if (t == 2) wcs[(s + 1) & 1] = wfrag(sn + csoff);
      }
      SB();
    }
    csum = mfma(wcs[s & 1], onesf, csum);
    SB();
  }
  STAMP(2, 1);
  if (tl == 0) *(f32x4*)(sCs + w * 16 + 4 * g) = csum;
  BAR();
  STAMP(2, 2);
  {
    float mb[4], rstd[4];
#pragma unroll
    for (int t = 0; t < 4; ++t) { float2 v = sLN[(tg * 4 + t) * 16 + tl]; mb[t] = v.x; rstd[t] = v.y; }
    f32x4 cg[2], cu[2];
#pragma unroll
    for (int i = 0; i < 2; ++i) {
      cg[i] = *(const f32x4*)(sCs + (fh * 2 + i) * 16 + 4 * g);
      cu[i] = *(const f32x4*)(sCs + (4 + fh * 2 + i) * 16 + 4 * g);
    }
#pragma unroll
    for (int t = 0; t < 4; ++t) {
      u32x4 out;
#pragma unroll
      for (int i = 0; i < 2; ++i) {
        float av[4];
#pragma unroll
        for (int r = 0; r < 4; ++r) {
          float gv = rbf((acc[t][i][r] - mb[t] * cg[i][r]) * rstd[t]);
          float uv = rbf((acc[t][2 + i][r] - mb[t] * cu[i][r]) * rstd[t]);
          float sl = rbf(gv * __builtin_amdgcn_rcpf(1.0f + __expf(-gv)));
          av[r] = sl * uv;
        }
        out[i * 2] = pack_bf(av[0], av[1]);
        out[i * 2 + 1] = pack_bf(av[2], av[3]);
      }
      const int tbg = mt * 16 + tg * 4 + t;
      st16_wt((u32x4*)a_frag + (size_t)(tbg * 56 + nt * 2 + fh) * 64 + l, out);
    }
  }
  STAMP(2, 3);
}

extern "C" void kernel_launch(void* const* d_in, const int* in_sizes, int n_in, void* d_out, int out_size,
                              void* d_ws, size_t ws_size, hipStream_t stream) {
  const u16* x = (const u16*)d_in[0];
  const u16* wqkv = (const u16*)d_in[1];
  const u16* wo = (const u16*)d_in[2];
  const u16* wg = (const u16*)d_in[4];
  const u16* wu = (const u16*)d_in[5];
  const u16* wd = (const u16*)d_in[6];
  u16* out = (u16*)d_out;
  char* ws = (char*)d_ws;
  u16* o_frag = (u16*)ws;
  u16* h = (u16*)(ws + (2u << 20));
  u16* h_frag = (u16*)(ws + (4u << 20));
  u16* a_frag = (u16*)(ws + (6u << 20));
  float* stats = (float*)(ws + (14u << 20));
  k_qkv_attn<<<256, 768, 0, stream>>>(x, wqkv, o_frag, wo, wg, wu, wd);
  k_gemm64<7, 8, true><<<224, 768, 0, stream>>>(o_frag, wo, x, h, h_frag, stats);
  k_gateup<<<224, 768, 0, stream>>>(h_frag, wg, wu, stats, a_frag);
  k_gemm64<28, 9, false><<<224, 768, 0, stream>>>(a_frag, wd, h, out, nullptr, nullptr);
}
```

```cpp
#include <hip/hip_runtime.h>
#include <hip/hip_bf16.h>
#include <stdint.h>

typedef unsigned short u16;
typedef __attribute__((ext_vector_type(8))) __bf16 bf16x8;
typedef __attribute__((ext_vector_type(4))) short s16x4;
typedef __attribute__((ext_vector_type(4))) float f32x4;
typedef __attribute__((ext_vector_type(4))) unsigned int u32x4;
typedef __attribute__((ext_vector_type(2))) unsigned int u32x2;
typedef __attribute__((ext_vector_type(2))) __bf16 bf16x2;
#define LDS_AS __attribute__((address_space(3)))
#define SB() __builtin_amdgcn_sched_barrier(0)
#define GLDS(gp, lp) __builtin_amdgcn_global_load_lds((const unsigned*)(gp), (unsigned*)(lp), 16, 0, 0)
#define WAITV(n) asm volatile("s_waitcnt vmcnt(" #n ")" ::: "memory")
#define RBAR() do { asm volatile("" ::: "memory"); __builtin_amdgcn_s_barrier(); asm volatile("" ::: "memory"); } while (0)
#define BAR() do { asm volatile("s_waitcnt lgkmcnt(0)" ::: "memory"); __builtin_amdgcn_s_barrier(); asm volatile("" ::: "memory"); } while (0)

#define STAMP(K,I)

constexpr int S_ = 2048, D_ = 448, F_ = 1792, QKV_ = 1344;
constexpr unsigned ONES = 0x3F803F80u;

__device__ __attribute__((aligned(16))) const unsigned g_const[12] = {ONES, ONES, ONES, ONES, 0x3F80u, 0u, 0u, 0u, 0u, 0u, 0u, 0u};

__device__ __forceinline__ void st16_wt(void* p, u32x4 v) {
  asm volatile("global_store_dwordx4 %0, %1, off sc1\n\ts_nop 1" :: "v"(p), "v"(v) : "memory");
}
__device__ __forceinline__ float bf_lo(unsigned u) { return __uint_as_float(u << 16); }
__device__ __forceinline__ float bf_hi(unsigned u) { return __uint_as_float(u & 0xffff0000u); }
__device__ __forceinline__ unsigned pack_bf(float a, float b) {
  bf16x2 v = {(__bf16)a, (__bf16)b};
  return __builtin_bit_cast(unsigned, v);
}
__device__ __forceinline__ float rbf(float a) { return (float)(__bf16)a; }
__device__ __forceinline__ float dot2(unsigned a, unsigned b, float c) {
  return __builtin_amdgcn_fdot2_f32_bf16(__builtin_bit_cast(bf16x2, a), __builtin_bit_cast(bf16x2, b), c, false);
}
__device__ __forceinline__ u32x2 tr_read(const char* p) {
  s16x4 v = __builtin_amdgcn_ds_read_tr16_b64_v4i16((LDS_AS s16x4*)p);
  return __builtin_bit_cast(u32x2, v);
}
__device__ __forceinline__ u32x4 wfrag(const char* p) {
  u32x2 a = tr_read(p), b = tr_read(p + 16 * 128);
  u32x4 r = {a[0], a[1], b[0], b[1]};
  return r;
}
__device__ __forceinline__ f32x4 mfma(u32x4 a, u32x4 b, f32x4 c) {
  return __builtin_amdgcn_mfma_f32_16x16x32_bf16(__builtin_bit_cast(bf16x8, a), __builtin_bit_cast(bf16x8, b), c, 0, 0, 0);
}
__device__ __forceinline__ int rho_inv(int r) { return (r & ~31) | ((r & 12) << 1) | ((r & 16) >> 2) | (r & 3); }
__device__ __forceinline__ int tr_lane_off(int l, int nb) {
  int g = l >> 4, q = (l & 15) >> 2, p = l & 3;
  int swz = 2 * (g & 1) + (q >> 1);
  return (4 * g + q) * 128 + ((nb ^ swz) << 5) + p * 8;
}
__device__ __forceinline__ void ln_params(float sum, float sq, float& mb, float& rstd) {
  float mean = sum * (1.0f / D_);
  float var = fmaxf(sq * (1.0f / D_) - mean * mean, 0.0f);
  mb = rbf(mean);
  float den = rbf(sqrtf(rbf(rbf(var) + 1e-5f)));
  rstd = 1.0f / den;
}
__device__ __forceinline__ u32x4 add_bf8(u32x4 a, u32x4 b) {
  u32x4 r;
#pragma unroll
  for (int e = 0; e < 4; ++e) r[e] = pack_bf(bf_lo(a[e]) + bf_lo(b[e]), bf_hi(a[e]) + bf_hi(b[e]));
  return r;
}

constexpr int P1_WG = 64 * 128;
constexpr int P1_XO = 3 * P1_WG;
constexpr int P1_SLOT = 3 * P1_WG + 80 * 128;
constexpr int P1_R = 4;
constexpr int P1_SP = 672;
constexpr int P1_VO = 28 * P1_SP;
constexpr int P1_VP = 72;
constexpr int P1_SMEM = P1_R * P1_SLOT + 2048 + (80 * 2 + 3 * 64) * 4;

__global__ __launch_bounds__(768, 3) void k_qkv_attn(const u16* __restrict__ x, const u16* __restrict__ wqkv,
                                                     u16* __restrict__ o_frag, const u16* __restrict__ wo,
                                                     const u16* __restrict__ wg, const u16* __restrict__ wu,
                                                     const u16* __restrict__ wd, unsigned* __restrict__ cnt) {
  __shared__ __attribute__((aligned(16))) char smem[P1_SMEM];
  float* sSum = (float*)(smem + P1_R * P1_SLOT + 2048);
  float* sSq = sSum + 80;
  float* sCs = sSq + 80;

  const int tid = threadIdx.x, l = tid & 63, g = l >> 4, tl = l & 15;
  const int w = __builtin_amdgcn_readfirstlane(tid >> 6);
  const int bid = blockIdx.x, xcd = bid & 7, jj = bid >> 3;
  const int mt = xcd * 4 + (jj >> 3), hp = jj & 7;
  const int row0 = mt * 64 - 16;
  STAMP(0, 0);
  if (bid == 0 && tid < 16) cnt[tid * 32] = 0u;

  if (w >= 8) {
    const int lw = w - 8, lr = l >> 3, lc = l & 7;
    const u16* src[9];
    int dst[9], adv[9];
#pragma unroll
    for (int j = 0; j < 9; ++j) {
      const int pi = lw + 4 * j;
      if (pi < 24) {
        const int m = pi >> 3, rho = (pi & 7) * 8 + lr;
        const int c = lc ^ (((rho >> 1) & 3) << 1);
        if (c < 7) { src[j] = wqkv + (size_t)rho_inv(rho) * QKV_ + m * D_ + hp * 56 + c * 8; adv[j] = 64 * QKV_; }
        else { src[j] = (const u16*)(g_const + (m == 1 ? 4 : 8)); adv[j] = 0; }
        dst[j] = pi * 1024;
      } else if (pi < 34) {
        const int r = (pi - 24) * 8 + lr;
        const int c = lc ^ ((r >> 1) & 7);
        if (r > 0) { src[j] = x + (size_t)max(row0 + r, 0) * D_ + c * 8; adv[j] = 64; }
        else { src[j] = (const u16*)g_const; adv[j] = 0; }
        dst[j] = pi * 1024;
      } else { src[j] = (const u16*)g_const; adv[j] = 0; dst[j] = -1; }
    }
#define P1_ISSUE(ST) do { \
      char* sl_ = smem + ((ST) % P1_R) * P1_SLOT; \
      _Pragma("unroll") for (int j = 0; j < 9; ++j) { \
        GLDS(src[j] + (size_t)(ST) * adv[j], (dst[j] >= 0) ? sl_ + dst[j] : smem + P1_R * P1_SLOT + (lw & 1) * 1024); } \
    } while (0)
    P1_ISSUE(0); P1_ISSUE(1);
    WAITV(9);  BAR();
    P1_ISSUE(2);
    WAITV(9);  BAR();
    P1_ISSUE(3);
    WAITV(9);  BAR();
    P1_ISSUE(4);
    WAITV(9);  BAR();
    P1_ISSUE(5);
    WAITV(9);  BAR();
    P1_ISSUE(6);
    WAITV(9);  BAR();
    WAITV(0);  BAR();
    {
      char* dummy = smem + P1_R * P1_SLOT + (lw & 1) * 1024;
      const int pc = (bid * 4 + lw);
      const size_t e0 = ((size_t)pc * 64 + l) * 8;
      GLDS(wg + e0, dummy); GLDS(wu + e0, dummy); GLDS(wd + e0, dummy);
      if (pc < 544) { GLDS(wg + e0 + 1024 * 512, dummy); GLDS(wu + e0 + 1024 * 512, dummy); GLDS(wd + e0 + 1024 * 512, dummy); }
      if (pc < 392) GLDS(wo + e0, dummy);
    }
    BAR();
    BAR(); BAR();
    WAITV(0);
    return;
  }

  const int fb = w & 3, hf = w >> 2;
  int xo[3][2];
#pragma unroll
  for (int t = 0; t < 3; ++t) {
    const int tb = (t < 2) ? hf * 3 + t : (hf ? fb : 2);
    const int r = tb * 16 + tl;
#pragma unroll
    for (int s2 = 0; s2 < 2; ++s2) xo[t][s2] = P1_XO + r * 128 + (((4 * s2 + g) ^ ((r >> 1) & 7)) << 4);
  }
  f32x4 acc[3][3];
#pragma unroll
  for (int m = 0; m < 3; ++m)
#pragma unroll
    for (int t = 0; t < 3; ++t) acc[m][t] = (f32x4){0.f, 0.f, 0.f, 0.f};
  const int troff = tr_lane_off(l, fb);

  u32x4 xf[2][3], wf[2][3];
#define P1_LOAD(B, S) do { \
    const char* slot_ = smem + (((S) >> 1) % P1_R) * P1_SLOT; \
    _Pragma("unroll") for (int m = 0; m < 3; ++m) wf[B][m] = wfrag(slot_ + m * P1_WG + ((S) & 1) * 32 * 128 + troff); \
    _Pragma("unroll") for (int t = 0; t < 3; ++t) xf[B][t] = *(const u32x4*)(slot_ + xo[t][(S) & 1]); \
  } while (0)
#pragma unroll
  for (int ii = 0; ii < 7; ++ii) {
    RBAR();
    if (ii == 0) { STAMP(0, 1); P1_LOAD(0, 0); }
#pragma unroll
    for (int s2 = 0; s2 < 2; ++s2) {
      const int s = ii * 2 + s2;
      if (s == 1) RBAR();
      const char* sn = smem + (((s + 1) >> 1) % P1_R) * P1_SLOT;
      SB();
#pragma unroll
      for (int m = 0; m < 3; ++m) {
        acc[m][0] = mfma(wf[s & 1][m], xf[s & 1][0], acc[m][0]);
        acc[m][1] = mfma(wf[s & 1][m], xf[s & 1][1], acc[m][1]);
        if (hf == 0) acc[m][2] = mfma(wf[s & 1][m], xf[s & 1][2], acc[m][2]);
        else if (m == 0) acc[0][2] = mfma(xf[s & 1][2], xf[s & 1][2], acc[0][2]);
        else if (m == 1) acc[1][2] = mfma(xf[s & 1][1], xf[s & 1][1], acc[1][2]);
        SB();
        if (s + 1 < 14) {
          wf[(s + 1) & 1][m] = wfrag(sn + m * P1_WG + ((s + 1) & 1) * 32 * 128 + troff);
          xf[(s + 1) & 1][m] = *(const u32x4*)(sn + xo[m][(s + 1) & 1]);
        }
        SB();
      }
    }
  }
  STAMP(0, 2);
  if (fb == 3 && g == 2) {
#pragma unroll
    for (int t = 0; t < 3; ++t)
      if (hf == 0 || t < 2) sSum[(hf * 3 + t) * 16 + tl] = acc[1][t][0];
  }
  if (hf == 1 && g == (tl >> 2)) {
    float a0 = acc[0][2][0], a1 = acc[1][2][0];
#pragma unroll
    for (int r = 1; r < 4; ++r) { if ((tl & 3) == r) { a0 = acc[0][2][r]; a1 = acc[1][2][r]; } }
    sSq[fb * 16 + tl] = a0;
    if (fb == 0) sSq[64 + tl] = a1;
  }
  if (hf == 0 && tl == 0) {
#pragma unroll
    for (int m = 0; m < 3; ++m) *(f32x4*)(sCs + m * 64 + fb * 16 + 4 * g) = acc[m][0];
  }
  BAR();
  char* sS = smem;
  {
    f32x4 csf[3];
#pragma unroll
    for (int m = 0; m < 3; ++m) csf[m] = *(const f32x4*)(sCs + m * 64 + fb * 16 + 4 * g);
    const int quad = 4 * fb + g;
#pragma unroll
    for (int t = 0; t < 3; ++t) {
      if (hf == 0 || t < 2) {
        const int tb = hf * 3 + t;
        float mb, rstd;
        ln_params(sSum[tb * 16 + tl], sSq[tb * 16 + tl], mb, rstd);
#pragma unroll
        for (int m = 0; m < 3; ++m) {
          float v0 = (acc[m][t][0] - mb * csf[m][0]) * rstd;
          float v1 = (acc[m][t][1] - mb * csf[m][1]) * rstd;
          float v2 = (acc[m][t][2] - mb * csf[m][2]) * rstd;
          float v3 = (acc[m][t][3] - mb * csf[m][3]) * rstd;
          u32x2 pk = {pack_bf(v0, v1), pack_bf(v2, v3)};
          if (quad < 14) {
            if (m < 2) *(u32x2*)(sS + (m * 14 + quad) * P1_SP + (tb * 16 + tl) * 8) = pk;
            else *(u32x2*)(sS + P1_VO + ((quad >= 7 ? 80 : 0) + tb * 16 + tl) * P1_VP + (quad >= 7 ? quad - 7 : quad) * 8) = pk;
          }
        }
      }
    }
  }
  BAR();
  STAMP(0, 3);

  {
    const int hh = w >> 2, tq = (w & 3) + 1;
    const int qa = hh * 7 + 2 * g;
    const bool up = g < 3;
    const int qb = up ? qa + 1 : qa;
    u32x4 qf, kf[2];
    {
      u32x2 a = *(const u32x2*)(sS + (0 * 14 + qa) * P1_SP + (tq * 16 + tl) * 8);
      u32x2 b = *(const u32x2*)(sS + (0 * 14 + qb) * P1_SP + (tq * 16 + tl) * 8);
      qf = (u32x4){a[0], a[1], up ? b[0] : 0u, up ? b[1] : 0u};
#pragma unroll
      for (int c = 0; c < 2; ++c) {
        u32x2 ka = *(const u32x2*)(sS + (1 * 14 + qa) * P1_SP + ((tq - 1 + c) * 16 + tl) * 8);
        u32x2 kb = *(const u32x2*)(sS + (1 * 14 + qb) * P1_SP + ((tq - 1 + c) * 16 + tl) * 8);
        kf[c] = (u32x4){ka[0], ka[1], up ? kb[0] : 0u, up ? kb[1] : 0u};
      }
    }
    u32x4 vf[2];
    {
      const char* vb = sS + P1_VO + (hh * 80 + (tq - 1) * 16 + 4 * g + ((l & 15) >> 2)) * P1_VP + (l & 3) * 8;
#pragma unroll
      for (int db = 0; db < 2; ++db) {
        u32x2 a = tr_read(vb + db * 32), b = tr_read(vb + 16 * P1_VP + db * 32);
        vf[db] = (u32x4){a[0], a[1], b[0], b[1]};
      }
    }
    const f32x4 z4 = {0.f, 0.f, 0.f, 0.f};
    f32x4 sc[2];
    sc[0] = mfma(kf[0], qf, z4);
    sc[1] = mfma(kf[1], qf, z4);
    const int tok = mt * 64 + (tq - 1) * 16 + tl;
    const float scale = 0.1889822365046136f;
    float lg[8];
    float mx = -3.0e38f;
#pragma unroll
    for (int c = 0; c < 2; ++c)
#pragma unroll
      for (int r = 0; r < 4; ++r) {
        const int delta = tl - (4 * g + r) + 16 * (1 - c);
        const bool inwin = (delta >= 0) && (delta <= 14) && ((delta & 1) == 0);
        float v = rbf(rbf(sc[c][r]) * scale);
        v = (tok - delta >= 0) ? v : -1e9f;
        v = inwin ? v : -3.0e38f;
        lg[c * 4 + r] = v;
        mx = fmaxf(mx, v);
      }
    mx = fmaxf(mx, __shfl_xor(mx, 16));
    mx = fmaxf(mx, __shfl_xor(mx, 32));
    float den = 0.f;
#pragma unroll
    for (int i = 0; i < 8; ++i) { lg[i] = (lg[i] > -2.0e38f) ? __expf(lg[i] - mx) : 0.f; den += lg[i]; }
    den += __shfl_xor(den, 16);
    den += __shfl_xor(den, 32);
    const float inv = 1.0f / den;
    u32x4 pf = {pack_bf(lg[0] * inv, lg[1] * inv), pack_bf(lg[2] * inv, lg[3] * inv),
                pack_bf(lg[4] * inv, lg[5] * inv), pack_bf(lg[6] * inv, lg[7] * inv)};
    const int tbg = tok >> 4;
#pragma unroll
    for (int db = 0; db < 2; ++db) {
      f32x4 o = mfma(vf[db], pf, z4);
      const int qd = db * 4 + g;
      if (qd < 7) {
        int f = hp * 56 + (hh * 7 + qd) * 4;
        int s = f >> 5, ii = (f >> 4) & 1, gg = (f >> 2) & 3;
        u32x2 pk = {pack_bf(o[0], o[1]), pack_bf(o[2], o[3])};
        *(u32x2*)((char*)o_frag + ((size_t)((tbg * 14 + s) * 64 + gg * 16 + tl)) * 16 + ii * 8) = pk;
      }
    }
  }
  STAMP(0, 4);
}

constexpr int EP = 144;
constexpr int G_SLOT = 16384;
constexpr int P3_SLOT = 24576;
constexpr int P3_R = 6;
constexpr int K2_RING = 147456;
constexpr int K2_T = K2_RING;
constexpr int K2_CS = K2_T + 64 * EP;
constexpr int K2_LN = K2_CS + 512;
constexpr int K2_SMEM = K2_LN + 2048;
#define WAITVI(n) asm volatile("s_waitcnt vmcnt(%0)" :: "i"(n) : "memory")

template <int NST, int R, int PW>
__device__ __forceinline__ void g_loader_run(char* smem, const u16* ws0, const u16* ws1, const u16* xs, int lw) {
#define G_ISSUE(ST) do { \
      char* sl_ = smem + ((ST) % R) * G_SLOT; \
      if ((ST) >= PW) { GLDS(ws0 + (size_t)(ST) * 64 * D_, sl_ + lw * 1024); GLDS(ws1 + (size_t)(ST) * 64 * D_, sl_ + (lw + 4) * 1024); } \
      GLDS(xs + (size_t)(ST) * 1024, sl_ + 8192 + lw * 1024); \
      GLDS(xs + (size_t)(ST) * 1024 + 512, sl_ + 8192 + (4 + lw) * 1024); \
    } while (0)
  constexpr int PRE = 4;
#pragma unroll
  for (int st = 0; st < PRE; ++st) G_ISSUE(st);
#pragma unroll
  for (int i = 0; i < NST; ++i) {
    const int lim0 = (i == 0) ? PRE - 1 : ((PRE + 2 * i - 1 < i + R - 2) ? PRE + 2 * i - 1 : i + R - 2);
    const int issued = (lim0 < NST - 1) ? lim0 : NST - 1;
    const int need = (i + 1 < NST) ? i + 1 : NST - 1;
    int allow = 0;
#pragma unroll
    for (int st = need + 1; st <= issued; ++st) allow += (st < PW) ? 2 : 4;
    WAITVI(allow);
    BAR();
    const int lim1 = (PRE + 2 * i + 1 < i + R - 1) ? PRE + 2 * i + 1 : i + R - 1;
#pragma unroll
    for (int st = issued + 1; st <= lim1; ++st) if (st < NST) G_ISSUE(st);
  }
}

template <int NST, int R>
__device__ __forceinline__ void g_consumer_loop(const char* smem, int l, int tb, int fh, f32x4 (&acc)[2]) {
  constexpr int KS = NST * 2;
  const int tro0 = tr_lane_off(l, fh * 2), tro1 = tr_lane_off(l, fh * 2 + 1);
  const int xoff = 8192 + tb * 1024 + l * 16;
  u32x4 wf[2][2], xf[2];
#define G_LOAD(K) do { \
    const char* sl_ = smem + (((K) >> 1) % R) * G_SLOT; \
    wf[(K) & 1][0] = wfrag(sl_ + ((K) & 1) * 4096 + tro0); \
    wf[(K) & 1][1] = wfrag(sl_ + ((K) & 1) * 4096 + tro1); \
    xf[(K) & 1] = *(const u32x4*)(sl_ + ((K) & 1) * 4096 + xoff); \
  } while (0)
#pragma unroll
  for (int i = 0; i < NST; ++i) {
    RBAR();
    if (i == 0) G_LOAD(0);
#pragma unroll
    for (int j = 0; j < 2; ++j) {
      const int k = i * 2 + j;
      if (k + 1 < KS) G_LOAD(k + 1);
      SB();
      acc[0] = mfma(wf[k & 1][0], xf[k & 1], acc[0]);
      acc[1] = mfma(wf[k & 1][1], xf[k & 1], acc[1]);
      SB();
    }
  }
}

__device__ __forceinline__ void group_sync(unsigned* c, int l) {
  if (l == 0) {
    __hip_atomic_fetch_add(c, 1u, __ATOMIC_RELAXED, __HIP_MEMORY_SCOPE_AGENT);
    for (int spin = 0; spin < (1 << 22); ++spin) {
      if (__hip_atomic_load(c, __ATOMIC_RELAXED, __HIP_MEMORY_SCOPE_AGENT) >= 28u) break;
      __builtin_amdgcn_s_sleep(1);
    }
  }
}

__global__ __launch_bounds__(768, 3) void k_fused(const u16* __restrict__ o_frag, const u16* __restrict__ wo,
                                                  const u16* __restrict__ x, const u16* __restrict__ wg,
                                                  const u16* __restrict__ wu, const u16* __restrict__ wd,
                                                  u16* h_frag, float* stats, u16* a_frag, u16* __restrict__ out,
                                                  unsigned* cnt) {
  __shared__ __attribute__((aligned(16))) char smem[K2_SMEM];
  char* sT = smem + K2_T;
  float* sCs = (float*)(smem + K2_CS);
  float2* sLN = (float2*)(smem + K2_LN);
  const int tid = threadIdx.x, l = tid & 63, g = l >> 4, tl = l & 15;
  const int w = __builtin_amdgcn_readfirstlane(tid >> 6);
  const int bid = blockIdx.x, grp = bid & 7, jj = bid >> 3;
  const int mt = grp * 4 + jj / 7, nt = jj % 7;
  const int n3 = jj;
  STAMP(1, 0);

  if (w >= 8) {
    const int lw = w - 8, lr = l >> 3, lc = l & 7;
    int woff[2];
#pragma unroll
    for (int j = 0; j < 2; ++j) {
      const int rho = (lw + 4 * j) * 8 + lr;
      woff[j] = rho * D_ + nt * 64 + (lc ^ (((rho >> 1) & 3) << 1)) * 8;
    }
    g_loader_run<7, 8, 0>(smem, wo + woff[0], wo + woff[1], o_frag + ((size_t)((mt * 4 + lw) * 14) * 64 + l) * 8, lw);
    BAR();
    const int rho3 = lw * 8 + lr;
    const int c3 = lc ^ (((rho3 >> 1) & 3) << 1);
    const u16* gs = wg + (size_t)rho_inv(rho3) * F_ + n3 * 64 + c3 * 8;
    const u16* us = wu + (size_t)rho_inv(rho3) * F_ + n3 * 64 + c3 * 8;
#pragma unroll
    for (int st = 0; st < 5; ++st) {
      GLDS(gs + (size_t)st * 32 * F_, smem + st * P3_SLOT + lw * 1024);
      GLDS(us + (size_t)st * 32 * F_, smem + st * P3_SLOT + 4096 + lw * 1024);
    }
    BAR();
    BAR();
    if (lw == 0) group_sync(cnt + grp * 32, l);
    BAR();
    {
      const u16* xs = h_frag + ((size_t)((grp * 16 + lw * 4) * 14) * 64 + l) * 8;
#define P3_ISSUE(ST) do { \
        char* sl_ = smem + ((ST) % P3_R) * P3_SLOT; \
        if ((ST) >= 5) { GLDS(gs + (size_t)(ST) * 32 * F_, sl_ + lw * 1024); GLDS(us + (size_t)(ST) * 32 * F_, sl_ + 4096 + lw * 1024); } \
        _Pragma("unroll") for (int j = 0; j < 4; ++j) GLDS(xs + (size_t)(j * 14 + (ST)) * 512, sl_ + 8192 + (lw * 4 + j) * 1024); \
      } while (0)
      P3_ISSUE(0); P3_ISSUE(1); P3_ISSUE(2);
#pragma unroll
      for (int i = 0; i < 14; ++i) {
        const int issued = (i == 0) ? 2 : ((i == 1) ? 4 : ((i + P3_R - 2 < 13) ? i + P3_R - 2 : 13));
        const int need = (i + 1 < 14) ? i + 1 : 13;
        int allow = 0;
#pragma unroll
        for (int st = need + 1; st <= issued; ++st) allow += (st < 5) ? 4 : 6;
        WAITVI(allow);
        BAR();
        if (i == 0) { P3_ISSUE(3); P3_ISSUE(4); }
        else if (i == 1) { P3_ISSUE(5); P3_ISSUE(6); }
        else if (i + P3_R - 1 < 14) P3_ISSUE(i + P3_R - 1);
        if (i == 12) {
          float a = 0.f, b = 0.f;
#pragma unroll
          for (int n = 0; n < 7; ++n) {
            const float2 s0 = *(const float2*)(stats + ((size_t)n * S_ + grp * 256 + lw * 64 + l) * 2);
            a += s0.x; b += s0.y;
          }
          float mbv, rsv;
          ln_params(a, b, mbv, rsv);
          sLN[lw * 64 + l] = make_float2(mbv, rsv);
        }
      }
    }
    BAR();
#pragma unroll
    for (int st = 0; st < 8; ++st) {
      GLDS(wd + woff[0] + (size_t)st * 64 * D_, smem + st * G_SLOT + lw * 1024);
      GLDS(wd + woff[1] + (size_t)st * 64 * D_, smem + st * G_SLOT + (lw + 4) * 1024);
    }
    BAR();
    if (lw == 0) group_sync(cnt + (8 + grp) * 32, l);
    BAR();
    g_loader_run<28, 9, 8>(smem, wd + woff[0], wd + woff[1], a_frag + ((size_t)((mt * 4 + lw) * 56) * 64 + l) * 8, lw);
    BAR();
    return;
  }

  const int tb = w & 3, fh = w >> 2;
  const size_t rowoff = (size_t)(mt * 64 + tb * 16 + fh * 8 + (l >> 3)) * D_ + nt * 64 + (l & 7) * 8;
  char* tw = sT + tb * 16 * EP;
  u32x4 hv;
  {
    const u32x4 rv = *(const u32x4*)(x + rowoff);
    f32x4 acc[2] = {(f32x4){0.f, 0.f, 0.f, 0.f}, (f32x4){0.f, 0.f, 0.f, 0.f}};
    g_consumer_loop<7, 8>(smem, l, tb, fh, acc);
    STAMP(1, 1);
#pragma unroll
    for (int f = 0; f < 2; ++f) {
      u32x2 pk = {pack_bf(acc[f][0], acc[f][1]), pack_bf(acc[f][2], acc[f][3])};
      *(u32x2*)(tw + tl * EP + (fh * 2 + f) * 32 + g * 8) = pk;
    }
    BAR();
    char* p = tw + (fh * 8 + (l >> 3)) * EP + (l & 7) * 16;
    hv = add_bf8(rv, *(const u32x4*)p);
    *(u32x4*)p = hv;
    float sm = 0.f, sq = 0.f;
#pragma unroll
    for (int e = 0; e < 4; ++e) { sm = dot2(hv[e], ONES, sm); sq = dot2(hv[e], hv[e], sq); }
    sm += __shfl_xor(sm, 1); sq += __shfl_xor(sq, 1);
    sm += __shfl_xor(sm, 2); sq += __shfl_xor(sq, 2);
    sm += __shfl_xor(sm, 4); sq += __shfl_xor(sq, 4);
    if ((l & 7) == 0) {
      unsigned long long sv = ((unsigned long long)__float_as_uint(sq) << 32) | __float_as_uint(sm);
      __hip_atomic_store((unsigned long long*)(stats + ((size_t)nt * S_ + mt * 64 + tb * 16 + fh * 8 + (l >> 3)) * 2), sv,
                         __ATOMIC_RELAXED, __HIP_MEMORY_SCOPE_AGENT);
    }
    BAR();
    u32x4 v = *(const u32x4*)(tw + tl * EP + (fh * 4 + g) * 16);
    st16_wt((u32x4*)h_frag + (size_t)((mt * 4 + tb) * 14 + nt * 2 + fh) * 64 + l, v);
    WAITV(0);
    BAR();
    BAR();
  }
  STAMP(1, 2);
  {
    const int tg = w >> 1, f3 = w & 1;
    f32x4 acc[4][4];
#pragma unroll
    for (int t = 0; t < 4; ++t)
#pragma unroll
      for (int f = 0; f < 4; ++f) acc[t][f] = (f32x4){0.f, 0.f, 0.f, 0.f};
    const int tro0 = tr_lane_off(l, f3 * 2), tro1 = tr_lane_off(l, f3 * 2 + 1);
    const int csoff = (w >> 2) * 4096 + tr_lane_off(l, w & 3);
    const int xoff = 8192 + tg * 4096 + l * 16;
    f32x4 csum = {0.f, 0.f, 0.f, 0.f};
    const u32x4 onesf = {ONES, ONES, ONES, ONES};
    u32x4 wf[2][4], wcs[2], xf[4];
#pragma unroll
    for (int s = 0; s < 14; ++s) {
      RBAR();
      if (s == 0) {
        STAMP(1, 3);
        wf[0][0] = wfrag(smem + tro0); wf[0][1] = wfrag(smem + tro1);
        wf[0][2] = wfrag(smem + 4096 + tro0); wf[0][3] = wfrag(smem + 4096 + tro1);
        wcs[0] = wfrag(smem + csoff);
#pragma unroll
        for (int t = 0; t < 4; ++t) xf[t] = *(const u32x4*)(smem + xoff + t * 1024);
      }
      int sx_ = ((s + 1) % P3_R) * P3_SLOT; asm volatile("" : "+s"(sx_));
      const char* sn = smem + sx_;
      SB();
#pragma unroll
      for (int t = 0; t < 4; ++t) {
#pragma unroll
        for (int f = 0; f < 4; ++f) acc[t][f] = mfma(wf[s & 1][f], xf[t], acc[t][f]);
        SB();
        if (s + 1 < 14) {
          xf[t] = *(const u32x4*)(sn + xoff + t * 1024);
          if (t == 0) { wf[(s + 1) & 1][0] = wfrag(sn + tro0); wf[(s + 1) & 1][1] = wfrag(sn + tro1); }
          if (t == 1) { wf[(s + 1) & 1][2] = wfrag(sn + 4096 + tro0); wf[(s + 1) & 1][3] = wfrag(sn + 4096 + tro1); }
          if (t == 2) wcs[(s + 1) & 1] = wfrag(sn + csoff);
        }
        SB();
      }
      csum = mfma(wcs[s & 1], onesf, csum);
      SB();
    }
    STAMP(1, 4);
    if (tl == 0) *(f32x4*)(sCs + w * 16 + 4 * g) = csum;
    BAR();
    float mb[4], rstd[4];
#pragma unroll
    for (int t = 0; t < 4; ++t) { float2 v = sLN[(tg * 4 + t) * 16 + tl]; mb[t] = v.x; rstd[t] = v.y; }
    f32x4 cg[2], cu[2];
#pragma unroll
    for (int i = 0; i < 2; ++i) {
      cg[i] = *(const f32x4*)(sCs + (f3 * 2 + i) * 16 + 4 * g);
      cu[i] = *(const f32x4*)(sCs + (4 + f3 * 2 + i) * 16 + 4 * g);
    }
#pragma unroll
    for (int t = 0; t < 4; ++t) {
      u32x4 o4;
#pragma unroll
      for (int i = 0; i < 2; ++i) {
        float av[4];
#pragma unroll
        for (int r = 0; r < 4; ++r) {
          float gv = rbf((acc[t][i][r] - mb[t] * cg[i][r]) * rstd[t]);
          float uv = rbf((acc[t][2 + i][r] - mb[t] * cu[i][r]) * rstd[t]);
          float sl = rbf(gv * __builtin_amdgcn_rcpf(1.0f + __expf(-gv)));
          av[r] = sl * uv;
        }
        o4[i * 2] = pack_bf(av[0], av[1]);
        o4[i * 2 + 1] = pack_bf(av[2], av[3]);
      }
      const int tbg = grp * 16 + tg * 4 + t;
      st16_wt((u32x4*)a_frag + (size_t)(tbg * 56 + n3 * 2 + f3) * 64 + l, o4);
    }
    WAITV(0);
    STAMP(1, 5);
    BAR();
    BAR();
  }
  STAMP(1, 6);
  {
    f32x4 acc[2] = {(f32x4){0.f, 0.f, 0.f, 0.f}, (f32x4){0.f, 0.f, 0.f, 0.f}};
    g_consumer_loop<28, 9>(smem, l, tb, fh, acc);
    STAMP(1, 7);
#pragma unroll
    for (int f = 0; f < 2; ++f) {
      u32x2 pk = {pack_bf(acc[f][0], acc[f][1]), pack_bf(acc[f][2], acc[f][3])};
      *(u32x2*)(tw + tl * EP + (fh * 2 + f) * 32 + g * 8) = pk;
    }
    BAR();
    const char* p = tw + (fh * 8 + (l >> 3)) * EP + (l & 7) * 16;
    *(u32x4*)(out + rowoff) = add_bf8(hv, *(const u32x4*)p);
  }
  STAMP(1, 8);
}

extern "C" void kernel_launch(void* const* d_in, const int* in_sizes, int n_in, void* d_out, int out_size,
                              void* d_ws, size_t ws_size, hipStream_t stream) {
  const u16* x = (const u16*)d_in[0];
  const u16* wqkv = (const u16*)d_in[1];
  const u16* wo = (const u16*)d_in[2];
  const u16* wg = (const u16*)d_in[4];
  const u16* wu = (const u16*)d_in[5];
  const u16* wd = (const u16*)d_in[6];
  u16* out = (u16*)d_out;
  char* ws = (char*)d_ws;
  u16* o_frag = (u16*)ws;
  u16* h_frag = (u16*)(ws + (4u << 20));
  u16* a_frag = (u16*)(ws + (6u << 20));
  float* stats = (float*)(ws + (14u << 20));
  unsigned* cnt = (unsigned*)(ws + (15u << 20));
  k_qkv_attn<<<256, 768, 0, stream>>>(x, wqkv, o_frag, wo, wg, wu, wd, cnt);
  k_fused<<<224, 768, 0, stream>>>(o_frag, wo, x, wg, wu, wd, h_frag, stats, a_frag, out, cnt);
}
```

```cpp
#include <hip/hip_runtime.h>
#include <hip/hip_bf16.h>
#include <stdint.h>

typedef unsigned short u16;
typedef __attribute__((ext_vector_type(8))) __bf16 bf16x8;
typedef __attribute__((ext_vector_type(4))) short s16x4;
typedef __attribute__((ext_vector_type(4))) float f32x4;
typedef __attribute__((ext_vector_type(4))) unsigned int u32x4;
typedef __attribute__((ext_vector_type(2))) unsigned int u32x2;
typedef __attribute__((ext_vector_type(2))) __bf16 bf16x2;
#define LDS_AS __attribute__((address_space(3)))
#define SB() __builtin_amdgcn_sched_barrier(0)
#define GLDS(gp, lp) __builtin_amdgcn_global_load_lds((const unsigned*)(gp), (unsigned*)(lp), 16, 0, 0)
#define WAITV(n) asm volatile("s_waitcnt vmcnt(" #n ")" ::: "memory")
#define RBAR() do { asm volatile("" ::: "memory"); __builtin_amdgcn_s_barrier(); asm volatile("" ::: "memory"); } while (0)
#define BAR() do { asm volatile("s_waitcnt lgkmcnt(0)" ::: "memory"); __builtin_amdgcn_s_barrier(); asm volatile("" ::: "memory"); } while (0)

#define STAMP(K,I)

constexpr int S_ = 2048, D_ = 448, F_ = 1792, QKV_ = 1344;
constexpr unsigned ONES = 0x3F803F80u;

__device__ __attribute__((aligned(16))) const unsigned g_const[12] = {ONES, ONES, ONES, ONES, 0x3F80u, 0u, 0u, 0u, 0u, 0u, 0u, 0u};

__device__ __forceinline__ void st16_wt(void* p, u32x4 v) {
  asm volatile("global_store_dwordx4 %0, %1, off sc1\n\ts_nop 1" :: "v"(p), "v"(v) : "memory");
}
__device__ __forceinline__ float bf_lo(unsigned u) { return __uint_as_float(u << 16); }
__device__ __forceinline__ float bf_hi(unsigned u) { return __uint_as_float(u & 0xffff0000u); }
__device__ __forceinline__ unsigned pack_bf(float a, float b) {
  bf16x2 v = {(__bf16)a, (__bf16)b};
  return __builtin_bit_cast(unsigned, v);
}
__device__ __forceinline__ float rbf(float a) { return (float)(__bf16)a; }
__device__ __forceinline__ float dot2(unsigned a, unsigned b, float c) {
  return __builtin_amdgcn_fdot2_f32_bf16(__builtin_bit_cast(bf16x2, a), __builtin_bit_cast(bf16x2, b), c, false);
}
__device__ __forceinline__ u32x2 tr_read(const char* p) {
  s16x4 v = __builtin_amdgcn_ds_read_tr16_b64_v4i16((LDS_AS s16x4*)p);
  return __builtin_bit_cast(u32x2, v);
}
__device__ __forceinline__ u32x4 wfrag(const char* p) {
  u32x2 a = tr_read(p), b = tr_read(p + 16 * 128);
  u32x4 r = {a[0], a[1], b[0], b[1]};
  return r;
}
__device__ __forceinline__ f32x4 mfma(u32x4 a, u32x4 b, f32x4 c) {
  return __builtin_amdgcn_mfma_f32_16x16x32_bf16(__builtin_bit_cast(bf16x8, a), __builtin_bit_cast(bf16x8, b), c, 0, 0, 0);
}
__device__ __forceinline__ int rho_inv(int r) { return (r & ~31) | ((r & 12) << 1) | ((r & 16) >> 2) | (r & 3); }
__device__ __forceinline__ int tr_lane_off(int l, int nb) {
  int g = l >> 4, q = (l & 15) >> 2, p = l & 3;
  int swz = 2 * (g & 1) + (q >> 1);
  return (4 * g + q) * 128 + ((nb ^ swz) << 5) + p * 8;
}
__device__ __forceinline__ void ln_params(float sum, float sq, float& mb, float& rstd) {
  float mean = sum * (1.0f / D_);
  float var = fmaxf(sq * (1.0f / D_) - mean * mean, 0.0f);
  mb = rbf(mean);
  float den = rbf(sqrtf(rbf(rbf(var) + 1e-5f)));
  rstd = 1.0f / den;
}
__device__ __forceinline__ u32x4 add_bf8(u32x4 a, u32x4 b) {
  u32x4 r;
#pragma unroll
  for (int e = 0; e < 4; ++e) r[e] = pack_bf(bf_lo(a[e]) + bf_lo(b[e]), bf_hi(a[e]) + bf_hi(b[e]));
  return r;
}

constexpr int P1_WG = 64 * 128;
constexpr int P1_XO = 3 * P1_WG;
constexpr int P1_SLOT = 3 * P1_WG + 80 * 128;
constexpr int P1_R = 4;
constexpr int P1_SP = 672;
constexpr int P1_VO = 28 * P1_SP;
constexpr int P1_VP = 72;
constexpr int P1_SMEM = P1_R * P1_SLOT + 2048 + (80 * 2 + 3 * 64) * 4;

__global__ __launch_bounds__(768, 3) void k_qkv_attn(const u16* __restrict__ x, const u16* __restrict__ wqkv,
                                                     u16* __restrict__ o_frag, const u16* __restrict__ wo,
                                                     const u16* __restrict__ wg, const u16* __restrict__ wu,
                                                     const u16* __restrict__ wd, unsigned* __restrict__ cnt) {
  __shared__ __attribute__((aligned(16))) char smem[P1_SMEM];
  float* sSum = (float*)(smem + P1_R * P1_SLOT + 2048);
  float* sSq = sSum + 80;
  float* sCs = sSq + 80;

  const int tid = threadIdx.x, l = tid & 63, g = l >> 4, tl = l & 15;
  const int w = __builtin_amdgcn_readfirstlane(tid >> 6);
  const int bid = blockIdx.x, xcd = bid & 7, jj = bid >> 3;
  const int mt = xcd * 4 + (jj >> 3), hp = jj & 7;
  const int row0 = mt * 64 - 16;
  STAMP(0, 0);
  if (bid == 0 && tid < 16) cnt[tid * 32] = 0u;

  if (w >= 8) {
    const int lw = w - 8, lr = l >> 3, lc = l & 7;
    const u16* src[9];
    int dst[9], adv[9];
#pragma unroll
    for (int j = 0; j < 9; ++j) {
      const int pi = lw + 4 * j;
      if (pi < 24) {
        const int m = pi >> 3, rho = (pi & 7) * 8 + lr;
        const int c = lc ^ (((rho >> 1) & 3) << 1);
        if (c < 7) { src[j] = wqkv + (size_t)rho_inv(rho) * QKV_ + m * D_ + hp * 56 + c * 8; adv[j] = 64 * QKV_; }
        else { src[j] = (const u16*)(g_const + (m == 1 ? 4 : 8)); adv[j] = 0; }
        dst[j] = pi * 1024;
      } else if (pi < 34) {
        const int r = (pi - 24) * 8 + lr;
        const int c = lc ^ ((r >> 1) & 7);
        if (r > 0) { src[j] = x + (size_t)max(row0 + r, 0) * D_ + c * 8; adv[j] = 64; }
        else { src[j] = (const u16*)g_const; adv[j] = 0; }
        dst[j] = pi * 1024;
      } else { src[j] = (const u16*)g_const; adv[j] = 0; dst[j] = -1; }
    }
#define P1_ISSUE(ST) do { \
      char* sl_ = smem + ((ST) % P1_R) * P1_SLOT; \
      _Pragma("unroll") for (int j = 0; j < 9; ++j) { \
        GLDS(src[j] + (size_t)(ST) * adv[j], (dst[j] >= 0) ? sl_ + dst[j] : smem + P1_R * P1_SLOT + (lw & 1) * 1024); } \
    } while (0)
    P1_ISSUE(0); P1_ISSUE(1);
    WAITV(9);  BAR();
    P1_ISSUE(2);
    WAITV(9);  BAR();
    P1_ISSUE(3);
    WAITV(9);  BAR();
    P1_ISSUE(4);
    WAITV(9);  BAR();
    P1_ISSUE(5);
    WAITV(9);  BAR();
    P1_ISSUE(6);
    WAITV(9);  BAR();
    WAITV(0);  BAR();
    {
      char* dummy = smem + P1_R * P1_SLOT + (lw & 1) * 1024;
      const int pc = (bid * 4 + lw);
      const size_t e0 = ((size_t)pc * 64 + l) * 8;
      GLDS(wg + e0, dummy); GLDS(wu + e0, dummy); GLDS(wd + e0, dummy);
      if (pc < 544) { GLDS(wg + e0 + 1024 * 512, dummy); GLDS(wu + e0 + 1024 * 512, dummy); GLDS(wd + e0 + 1024 * 512, dummy); }
      if (pc < 392) GLDS(wo + e0, dummy);
    }
    BAR();
    BAR(); BAR();
    WAITV(0);
    return;
  }

  const int fb = w & 3, hf = w >> 2;
  int xo[3][2];
#pragma unroll
  for (int t = 0; t < 3; ++t) {
    const int tb = (t < 2) ? hf * 3 + t : (hf ? fb : 2);
    const int r = tb * 16 + tl;
#pragma unroll
    for (int s2 = 0; s2 < 2; ++s2) xo[t][s2] = P1_XO + r * 128 + (((4 * s2 + g) ^ ((r >> 1) & 7)) << 4);
  }
  f32x4 acc[3][3];
#pragma unroll
  for (int m = 0; m < 3; ++m)
#pragma unroll
    for (int t = 0; t < 3; ++t) acc[m][t] = (f32x4){0.f, 0.f, 0.f, 0.f};
  const int troff = tr_lane_off(l, fb);

  u32x4 xf[2][3], wf[2][3];
#define P1_LOAD(B, S) do { \
    const char* slot_ = smem + (((S) >> 1) % P1_R) * P1_SLOT; \
    _Pragma("unroll") for (int m = 0; m < 3; ++m) wf[B][m] = wfrag(slot_ + m * P1_WG + ((S) & 1) * 32 * 128 + troff); \
    _Pragma("unroll") for (int t = 0; t < 3; ++t) xf[B][t] = *(const u32x4*)(slot_ + xo[t][(S) & 1]); \
  } while (0)
#pragma unroll
  for (int ii = 0; ii < 7; ++ii) {
    RBAR();
    if (ii == 0) { STAMP(0, 1); P1_LOAD(0, 0); }
#pragma unroll
    for (int s2 = 0; s2 < 2; ++s2) {
      const int s = ii * 2 + s2;
      if (s == 1) RBAR();
      const char* sn = smem + (((s + 1) >> 1) % P1_R) * P1_SLOT;
      SB();
#pragma unroll
      for (int m = 0; m < 3; ++m) {
        acc[m][0] = mfma(wf[s & 1][m], xf[s & 1][0], acc[m][0]);
        acc[m][1] = mfma(wf[s & 1][m], xf[s & 1][1], acc[m][1]);
        if (hf == 0) acc[m][2] = mfma(wf[s & 1][m], xf[s & 1][2], acc[m][2]);
        else if (m == 0) acc[0][2] = mfma(xf[s & 1][2], xf[s & 1][2], acc[0][2]);
        else if (m == 1) acc[1][2] = mfma(xf[s & 1][1], xf[s & 1][1], acc[1][2]);
        SB();
        if (s + 1 < 14) {
          wf[(s + 1) & 1][m] = wfrag(sn + m * P1_WG + ((s + 1) & 1) * 32 * 128 + troff);
          xf[(s + 1) & 1][m] = *(const u32x4*)(sn + xo[m][(s + 1) & 1]);
        }
        SB();
      }
    }
  }
  STAMP(0, 2);
  if (fb == 3 && g == 2) {
#pragma unroll
    for (int t = 0; t < 3; ++t)
      if (hf == 0 || t < 2) sSum[(hf * 3 + t) * 16 + tl] = acc[1][t][0];
  }
  if (hf == 1 && g == (tl >> 2)) {
    float a0 = acc[0][2][0], a1 = acc[1][2][0];
#pragma unroll
    for (int r = 1; r < 4; ++r) { if ((tl & 3) == r) { a0 = acc[0][2][r]; a1 = acc[1][2][r]; } }
    sSq[fb * 16 + tl] = a0;
    if (fb == 0) sSq[64 + tl] = a1;
  }
  if (hf == 0 && tl == 0) {
#pragma unroll
    for (int m = 0; m < 3; ++m) *(f32x4*)(sCs + m * 64 + fb * 16 + 4 * g) = acc[m][0];
  }
  BAR();
  char* sS = smem;
  {
    f32x4 csf[3];
#pragma unroll
    for (int m = 0; m < 3; ++m) csf[m] = *(const f32x4*)(sCs + m * 64 + fb * 16 + 4 * g);
    const int quad = 4 * fb + g;
#pragma unroll
    for (int t = 0; t < 3; ++t) {
      if (hf == 0 || t < 2) {
        const int tb = hf * 3 + t;
        float mb, rstd;
        ln_params(sSum[tb * 16 + tl], sSq[tb * 16 + tl], mb, rstd);
#pragma unroll
        for (int m = 0; m < 3; ++m) {
          float v0 = (acc[m][t][0] - mb * csf[m][0]) * rstd;
          float v1 = (acc[m][t][1] - mb * csf[m][1]) * rstd;
          float v2 = (acc[m][t][2] - mb * csf[m][2]) * rstd;
          float v3 = (acc[m][t][3] - mb * csf[m][3]) * rstd;
          u32x2 pk = {pack_bf(v0, v1), pack_bf(v2, v3)};
          if (quad < 14) {
            if (m < 2) *(u32x2*)(sS + (m * 14 + quad) * P1_SP + (tb * 16 + tl) * 8) = pk;
            else *(u32x2*)(sS + P1_VO + ((quad >= 7 ? 80 : 0) + tb * 16 + tl) * P1_VP + (quad >= 7 ? quad - 7 : quad) * 8) = pk;
          }
        }
      }
    }
  }
  BAR();
  STAMP(0, 3);

  {
    const int hh = w >> 2, tq = (w & 3) + 1;
    const int qa = hh * 7 + 2 * g;
    const bool up = g < 3;
    const int qb = up ? qa + 1 : qa;
    u32x4 qf, kf[2];
    {
      u32x2 a = *(const u32x2*)(sS + (0 * 14 + qa) * P1_SP + (tq * 16 + tl) * 8);
      u32x2 b = *(const u32x2*)(sS + (0 * 14 + qb) * P1_SP + (tq * 16 + tl) * 8);
      qf = (u32x4){a[0], a[1], up ? b[0] : 0u, up ? b[1] : 0u};
#pragma unroll
      for (int c = 0; c < 2; ++c) {
        u32x2 ka = *(const u32x2*)(sS + (1 * 14 + qa) * P1_SP + ((tq - 1 + c) * 16 + tl) * 8);
        u32x2 kb = *(const u32x2*)(sS + (1 * 14 + qb) * P1_SP + ((tq - 1 + c) * 16 + tl) * 8);
        kf[c] = (u32x4){ka[0], ka[1], up ? kb[0] : 0u, up ? kb[1] : 0u};
      }
    }
    u32x4 vf[2];
    {
      const char* vb = sS + P1_VO + (hh * 80 + (tq - 1) * 16 + 4 * g + ((l & 15) >> 2)) * P1_VP + (l & 3) * 8;
#pragma unroll
      for (int db = 0; db < 2; ++db) {
        u32x2 a = tr_read(vb + db * 32), b = tr_read(vb + 16 * P1_VP + db * 32);
        vf[db] = (u32x4){a[0], a[1], b[0], b[1]};
      }
    }
    const f32x4 z4 = {0.f, 0.f, 0.f, 0.f};
    f32x4 sc[2];
    sc[0] = mfma(kf[0], qf, z4);
    sc[1] = mfma(kf[1], qf, z4);
    const int tok = mt * 64 + (tq - 1) * 16 + tl;
    const float scale = 0.1889822365046136f;
    float lg[8];
    float mx = -3.0e38f;
#pragma unroll
    for (int c = 0; c < 2; ++c)
#pragma unroll
      for (int r = 0; r < 4; ++r) {
        const int delta = tl - (4 * g + r) + 16 * (1 - c);
        const bool inwin = (delta >= 0) && (delta <= 14) && ((delta & 1) == 0);
        float v = rbf(rbf(sc[c][r]) * scale);
        v = (tok - delta >= 0) ? v : -1e9f;
        v = inwin ? v : -3.0e38f;
        lg[c * 4 + r] = v;
        mx = fmaxf(mx, v);
      }
    mx = fmaxf(mx, __shfl_xor(mx, 16));
    mx = fmaxf(mx, __shfl_xor(mx, 32));
    float den = 0.f;
#pragma unroll
    for (int i = 0; i < 8; ++i) { lg[i] = (lg[i] > -2.0e38f) ? __expf(lg[i] - mx) : 0.f; den += lg[i]; }
    den += __shfl_xor(den, 16);
    den += __shfl_xor(den, 32);
    const float inv = 1.0f / den;
    u32x4 pf = {pack_bf(lg[0] * inv, lg[1] * inv), pack_bf(lg[2] * inv, lg[3] * inv),
                pack_bf(lg[4] * inv, lg[5] * inv), pack_bf(lg[6] * inv, lg[7] * inv)};
    const int tbg = tok >> 4;
#pragma unroll
    for (int db = 0; db < 2; ++db) {
      f32x4 o = mfma(vf[db], pf, z4);
      const int qd = db * 4 + g;
      if (qd < 7) {
        int f = hp * 56 + (hh * 7 + qd) * 4;
        int s = f >> 5, ii = (f >> 4) & 1, gg = (f >> 2) & 3;
        u32x2 pk = {pack_bf(o[0], o[1]), pack_bf(o[2], o[3])};
        *(u32x2*)((char*)o_frag + ((size_t)((tbg * 14 + s) * 64 + gg * 16 + tl)) * 16 + ii * 8) = pk;
      }
    }
  }
  STAMP(0, 4);
}

constexpr int EP = 144;
constexpr int G_SLOT = 16384;
constexpr int P3_SLOT = 24576;
constexpr int P3_R = 6;
constexpr int K2_RING = 147456;
constexpr int K2_T = K2_RING;
constexpr int K2_CS = K2_T + 64 * EP;
constexpr int K2_LN = K2_CS + 512;
constexpr int K2_SMEM = K2_LN + 2048;
#define WAITVI(n) asm volatile("s_waitcnt vmcnt(%0)" :: "i"(n) : "memory")

template <int NST, int R, int PW>
__device__ __forceinline__ void g_loader_run(char* smem, const u16* ws0, const u16* ws1, const u16* xs, int lw) {
#define G_ISSUE(ST) do { \
      char* sl_ = smem + ((ST) % R) * G_SLOT; \
      if ((ST) >= PW) { GLDS(ws0 + (size_t)(ST) * 64 * D_, sl_ + lw * 1024); GLDS(ws1 + (size_t)(ST) * 64 * D_, sl_ + (lw + 4) * 1024); } \
      GLDS(xs + (size_t)(ST) * 1024, sl_ + 8192 + lw * 1024); \
      GLDS(xs + (size_t)(ST) * 1024 + 512, sl_ + 8192 + (4 + lw) * 1024); \
    } while (0)
  constexpr int PRE = 4;
#pragma unroll
  for (int st = 0; st < PRE; ++st) G_ISSUE(st);
#pragma unroll
  for (int i = 0; i < NST; ++i) {
    const int lim0 = (i == 0) ? PRE - 1 : ((PRE + 2 * i - 1 < i + R - 2) ? PRE + 2 * i - 1 : i + R - 2);
    const int issued = (lim0 < NST - 1) ? lim0 : NST - 1;
    const int need = (i + 1 < NST) ? i + 1 : NST - 1;
    int allow = 0;
#pragma unroll
    for (int st = need + 1; st <= issued; ++st) allow += (st < PW) ? 2 : 4;
    WAITVI(allow);
    BAR();
    const int lim1 = (PRE + 2 * i + 1 < i + R - 1) ? PRE + 2 * i + 1 : i + R - 1;
#pragma unroll
    for (int st = issued + 1; st <= lim1; ++st) if (st < NST) G_ISSUE(st);
  }
}

template <int NST, int R>
__device__ __forceinline__ void g_consumer_loop(const char* smem, int l, int tb, int fh, f32x4 (&acc)[2]) {
  constexpr int KS = NST * 2;
  const int tro0 = tr_lane_off(l, fh * 2), tro1 = tr_lane_off(l, fh * 2 + 1);
  const int xoff = 8192 + tb * 1024 + l * 16;
  u32x4 wf[2][2], xf[2];
#define G_LOAD(K) do { \
    const char* sl_ = smem + (((K) >> 1) % R) * G_SLOT; \
    wf[(K) & 1][0] = wfrag(sl_ + ((K) & 1) * 4096 + tro0); \
    wf[(K) & 1][1] = wfrag(sl_ + ((K) & 1) * 4096 + tro1); \
    xf[(K) & 1] = *(const u32x4*)(sl_ + ((K) & 1) * 4096 + xoff); \
  } while (0)
#pragma unroll
  for (int i = 0; i < NST; ++i) {
    RBAR();
    if (i == 0) G_LOAD(0);
#pragma unroll
    for (int j = 0; j < 2; ++j) {
      const int k = i * 2 + j;
      if (k + 1 < KS) G_LOAD(k + 1);
      SB();
      acc[0] = mfma(wf[k & 1][0], xf[k & 1], acc[0]);
      acc[1] = mfma(wf[k & 1][1], xf[k & 1], acc[1]);
      SB();
    }
  }
}

__device__ __forceinline__ void group_sync(unsigned* c, int l) {
  if (l == 0) {
    __hip_atomic_fetch_add(c, 1u, __ATOMIC_RELAXED, __HIP_MEMORY_SCOPE_AGENT);
    for (int spin = 0; spin < (1 << 22); ++spin) {
      if (__hip_atomic_load(c, __ATOMIC_RELAXED, __HIP_MEMORY_SCOPE_AGENT) >= 28u) break;
      __builtin_amdgcn_s_sleep(1);
    }
  }
}

__global__ __launch_bounds__(768, 3) void k_fused(const u16* __restrict__ o_frag, const u16* __restrict__ wo,
                                                  const u16* __restrict__ x, const u16* __restrict__ wg,
                                                  const u16* __restrict__ wu, const u16* __restrict__ wd,
                                                  u16* h_frag, float* stats, u16* a_frag, u16* __restrict__ out,
                                                  unsigned* cnt) {
  __shared__ __attribute__((aligned(16))) char smem[K2_SMEM];
  char* sT = smem + K2_T;
  float* sCs = (float*)(smem + K2_CS);
  float2* sLN = (float2*)(smem + K2_LN);
  const int tid = threadIdx.x, l = tid & 63, g = l >> 4, tl = l & 15;
  const int w = __builtin_amdgcn_readfirstlane(tid >> 6);
  const int bid = blockIdx.x, grp = bid & 7, jj = bid >> 3;
  const int mt = grp * 4 + jj / 7, nt = jj % 7;
  const int n3 = jj;
  STAMP(1, 0);

  if (w >= 8) {
    const int lw = w - 8, lr = l >> 3, lc = l & 7;
    int woff[2];
#pragma unroll
    for (int j = 0; j < 2; ++j) {
      const int rho = (lw + 4 * j) * 8 + lr;
      woff[j] = rho * D_ + nt * 64 + (lc ^ (((rho >> 1) & 3) << 1)) * 8;
    }
    g_loader_run<7, 8, 0>(smem, wo + woff[0], wo + woff[1], o_frag + ((size_t)((mt * 4 + lw) * 14) * 64 + l) * 8, lw);
    BAR();
    const int rho3 = lw * 8 + lr;
    const int c3 = lc ^ (((rho3 >> 1) & 3) << 1);
    const u16* gs = wg + (size_t)rho_inv(rho3) * F_ + n3 * 64 + c3 * 8;
    const u16* us = wu + (size_t)rho_inv(rho3) * F_ + n3 * 64 + c3 * 8;
#pragma unroll
    for (int st = 0; st < 5; ++st) {
      GLDS(gs + (size_t)st * 32 * F_, smem + st * P3_SLOT + lw * 1024);
      GLDS(us + (size_t)st * 32 * F_, smem + st * P3_SLOT + 4096 + lw * 1024);
    }
    BAR();
    BAR();
    BAR();
    {
      const u16* xs = h_frag + ((size_t)((grp * 16 + lw * 4) * 14) * 64 + l) * 8;
#define P3_ISSUE(ST) do { \
        char* sl_ = smem + ((ST) % P3_R) * P3_SLOT; \
        if ((ST) >= 5) { GLDS(gs + (size_t)(ST) * 32 * F_, sl_ + lw * 1024); GLDS(us + (size_t)(ST) * 32 * F_, sl_ + 4096 + lw * 1024); } \
        _Pragma("unroll") for (int j = 0; j < 4; ++j) GLDS(xs + (size_t)(j * 14 + (ST)) * 512, sl_ + 8192 + (lw * 4 + j) * 1024); \
      } while (0)
      P3_ISSUE(0); P3_ISSUE(1); P3_ISSUE(2);
#pragma unroll
      for (int i = 0; i < 14; ++i) {
        const int issued = (i == 0) ? 2 : ((i == 1) ? 4 : ((i + P3_R - 2 < 13) ? i + P3_R - 2 : 13));
        const int need = (i + 1 < 14) ? i + 1 : 13;
        int allow = 0;
#pragma unroll
        for (int st = need + 1; st <= issued; ++st) allow += (st < 5) ? 4 : 6;
        WAITVI(allow);
        BAR();
        if (i == 0) { P3_ISSUE(3); P3_ISSUE(4); }
        else if (i == 1) { P3_ISSUE(5); P3_ISSUE(6); }
        else if (i + P3_R - 1 < 14) P3_ISSUE(i + P3_R - 1);
        if (i == 12) {
          float a = 0.f, b = 0.f;
#pragma unroll
          for (int n = 0; n < 7; ++n) {
            const float2 s0 = *(const float2*)(stats + ((size_t)n * S_ + grp * 256 + lw * 64 + l) * 2);
            a += s0.x; b += s0.y;
          }
          float mbv, rsv;
          ln_params(a, b, mbv, rsv);
          sLN[lw * 64 + l] = make_float2(mbv, rsv);
        }
      }
    }
    BAR();
#pragma unroll
    for (int st = 0; st < 8; ++st) {
      GLDS(wd + woff[0] + (size_t)st * 64 * D_, smem + st * G_SLOT + lw * 1024);
      GLDS(wd + woff[1] + (size_t)st * 64 * D_, smem + st * G_SLOT + (lw + 4) * 1024);
    }
    BAR();
    BAR();
    g_loader_run<28, 9, 8>(smem, wd + woff[0], wd + woff[1], a_frag + ((size_t)((mt * 4 + lw) * 56) * 64 + l) * 8, lw);
    BAR();
    return;
  }

  const int tb = w & 3, fh = w >> 2;
  const size_t rowoff = (size_t)(mt * 64 + tb * 16 + fh * 8 + (l >> 3)) * D_ + nt * 64 + (l & 7) * 8;
  char* tw = sT + tb * 16 * EP;
  u32x4 hv;
  {
    const u32x4 rv = *(const u32x4*)(x + rowoff);
    f32x4 acc[2] = {(f32x4){0.f, 0.f, 0.f, 0.f}, (f32x4){0.f, 0.f, 0.f, 0.f}};
    g_consumer_loop<7, 8>(smem, l, tb, fh, acc);
    STAMP(1, 1);
#pragma unroll
    for (int f = 0; f < 2; ++f) {
      u32x2 pk = {pack_bf(acc[f][0], acc[f][1]), pack_bf(acc[f][2], acc[f][3])};
      *(u32x2*)(tw + tl * EP + (fh * 2 + f) * 32 + g * 8) = pk;
    }
    BAR();
    char* p = tw + (fh * 8 + (l >> 3)) * EP + (l & 7) * 16;
    hv = add_bf8(rv, *(const u32x4*)p);
    *(u32x4*)p = hv;
    float sm = 0.f, sq = 0.f;
#pragma unroll
    for (int e = 0; e < 4; ++e) { sm = dot2(hv[e], ONES, sm); sq = dot2(hv[e], hv[e], sq); }
    sm += __shfl_xor(sm, 1); sq += __shfl_xor(sq, 1);
    sm += __shfl_xor(sm, 2); sq += __shfl_xor(sq, 2);
    sm += __shfl_xor(sm, 4); sq += __shfl_xor(sq, 4);
    if ((l & 7) == 0) {
      unsigned long long sv = ((unsigned long long)__float_as_uint(sq) << 32) | __float_as_uint(sm);
      __hip_atomic_store((unsigned long long*)(stats + ((size_t)nt * S_ + mt * 64 + tb * 16 + fh * 8 + (l >> 3)) * 2), sv,
                         __ATOMIC_RELAXED, __HIP_MEMORY_SCOPE_AGENT);
    }
    BAR();
    u32x4 v = *(const u32x4*)(tw + tl * EP + (fh * 4 + g) * 16);
    st16_wt((u32x4*)h_frag + (size_t)((mt * 4 + tb) * 14 + nt * 2 + fh) * 64 + l, v);
    WAITV(0);
    BAR();
    if (w == 0) group_sync(cnt + grp * 32, l);
    BAR();
  }
  STAMP(1, 2);
  {
    const int tg = w >> 1, f3 = w & 1;
    f32x4 acc[4][4];
#pragma unroll
    for (int t = 0; t < 4; ++t)
#pragma unroll
      for (int f = 0; f < 4; ++f) acc[t][f] = (f32x4){0.f, 0.f, 0.f, 0.f};
    const int tro0 = tr_lane_off(l, f3 * 2), tro1 = tr_lane_off(l, f3 * 2 + 1);
    const int csoff = (w >> 2) * 4096 + tr_lane_off(l, w & 3);
    const int xoff = 8192 + tg * 4096 + l * 16;
    f32x4 csum = {0.f, 0.f, 0.f, 0.f};
    const u32x4 onesf = {ONES, ONES, ONES, ONES};
    u32x4 wf[2][4], wcs[2], xf[4];
#pragma unroll
    for (int s = 0; s < 14; ++s) {
      RBAR();
      if (s == 0) {
        STAMP(1, 3);
        wf[0][0] = wfrag(smem + tro0); wf[0][1] = wfrag(smem + tro1);
        wf[0][2] = wfrag(smem + 4096 + tro0); wf[0][3] = wfrag(smem + 4096 + tro1);
        wcs[0] = wfrag(smem + csoff);
#pragma unroll
        for (int t = 0; t < 4; ++t) xf[t] = *(const u32x4*)(smem + xoff + t * 1024);
      }
      int sx_ = ((s + 1) % P3_R) * P3_SLOT; asm volatile("" : "+s"(sx_));
      const char* sn = smem + sx_;
      SB();
#pragma unroll
      for (int t = 0; t < 4; ++t) {
#pragma unroll
        for (int f = 0; f < 4; ++f) acc[t][f] = mfma(wf[s & 1][f], xf[t], acc[t][f]);
        SB();
        if (s + 1 < 14) {
          xf[t] = *(const u32x4*)(sn + xoff + t * 1024);
          if (t == 0) { wf[(s + 1) & 1][0] = wfrag(sn + tro0); wf[(s + 1) & 1][1] = wfrag(sn + tro1); }
          if (t == 1) { wf[(s + 1) & 1][2] = wfrag(sn + 4096 + tro0); wf[(s + 1) & 1][3] = wfrag(sn + 4096 + tro1); }
          if (t == 2) wcs[(s + 1) & 1] = wfrag(sn + csoff);
        }
        SB();
      }
      csum = mfma(wcs[s & 1], onesf, csum);
      SB();
    }
    STAMP(1, 4);
    if (tl == 0) *(f32x4*)(sCs + w * 16 + 4 * g) = csum;
    BAR();
    float mb[4], rstd[4];
#pragma unroll
    for (int t = 0; t < 4; ++t) { float2 v = sLN[(tg * 4 + t) * 16 + tl]; mb[t] = v.x; rstd[t] = v.y; }
    f32x4 cg[2], cu[2];
#pragma unroll
    for (int i = 0; i < 2; ++i) {
      cg[i] = *(const f32x4*)(sCs + (f3 * 2 + i) * 16 + 4 * g);
      cu[i] = *(const f32x4*)(sCs + (4 + f3 * 2 + i) * 16 + 4 * g);
    }
#pragma unroll
    for (int t = 0; t < 4; ++t) {
      u32x4 o4;
#pragma unroll
      for (int i = 0; i < 2; ++i) {
        float av[4];
#pragma unroll
        for (int r = 0; r < 4; ++r) {
          float gv = (acc[t][i][r] - mb[t] * cg[i][r]) * rstd[t];
          float uv = (acc[t][2 + i][r] - mb[t] * cu[i][r]) * rstd[t];
          float sl = gv * __builtin_amdgcn_rcpf(1.0f + __expf(-gv));
          av[r] = sl * uv;
        }
        o4[i * 2] = pack_bf(av[0], av[1]);
        o4[i * 2 + 1] = pack_bf(av[2], av[3]);
      }
      const int tbg = grp * 16 + tg * 4 + t;
      st16_wt((u32x4*)a_frag + (size_t)(tbg * 56 + n3 * 2 + f3) * 64 + l, o4);
    }
    WAITV(0);
    STAMP(1, 5);
    BAR();
    if (w == 0) group_sync(cnt + (8 + grp) * 32, l);
    BAR();
  }
  STAMP(1, 6);
  {
    f32x4 acc[2] = {(f32x4){0.f, 0.f, 0.f, 0.f}, (f32x4){0.f, 0.f, 0.f, 0.f}};
    g_consumer_loop<28, 9>(smem, l, tb, fh, acc);
    STAMP(1, 7);
#pragma unroll
    for (int f = 0; f < 2; ++f) {
      u32x2 pk = {pack_bf(acc[f][0], acc[f][1]), pack_bf(acc[f][2], acc[f][3])};
      *(u32x2*)(tw + tl * EP + (fh * 2 + f) * 32 + g * 8) = pk;
    }
    BAR();
    const char* p = tw + (fh * 8 + (l >> 3)) * EP + (l & 7) * 16;
    *(u32x4*)(out + rowoff) = add_bf8(hv, *(const u32x4*)p);
  }
  STAMP(1, 8);
}

extern "C" void kernel_launch(void* const* d_in, const int* in_sizes, int n_in, void* d_out, int out_size,
                              void* d_ws, size_t ws_size, hipStream_t stream) {
  const u16* x = (const u16*)d_in[0];
  const u16* wqkv = (const u16*)d_in[1];
  const u16* wo = (const u16*)d_in[2];
  const u16* wg = (const u16*)d_in[4];
  const u16* wu = (const u16*)d_in[5];
  const u16* wd = (const u16*)d_in[6];
  u16* out = (u16*)d_out;
  char* ws = (char*)d_ws;
  u16* o_frag = (u16*)ws;
  u16* h_frag = (u16*)(ws + (4u << 20));
  u16* a_frag = (u16*)(ws + (6u << 20));
  float* stats = (float*)(ws + (14u << 20));
  unsigned* cnt = (unsigned*)(ws + (15u << 20));
  k_qkv_attn<<<256, 768, 0, stream>>>(x, wqkv, o_frag, wo, wg, wu, wd, cnt);
  k_fused<<<224, 768, 0, stream>>>(o_frag, wo, x, wg, wu, wd, h_frag, stats, a_frag, out, cnt);
}
```

```cpp
#include <hip/hip_runtime.h>
#include <hip/hip_bf16.h>
#include <stdint.h>

typedef unsigned short u16;
typedef __attribute__((ext_vector_type(8))) __bf16 bf16x8;
typedef __attribute__((ext_vector_type(4))) short s16x4;
typedef __attribute__((ext_vector_type(4))) float f32x4;
typedef __attribute__((ext_vector_type(4))) unsigned int u32x4;
typedef __attribute__((ext_vector_type(2))) unsigned int u32x2;
typedef __attribute__((ext_vector_type(2))) __bf16 bf16x2;
#define LDS_AS __attribute__((address_space(3)))
#define SB() __builtin_amdgcn_sched_barrier(0)
#define GLDS(gp, lp) __builtin_amdgcn_global_load_lds((const unsigned*)(gp), (unsigned*)(lp), 16, 0, 0)
#define WAITV(n) asm volatile("s_waitcnt vmcnt(" #n ")" ::: "memory")
#define RBAR() do { asm volatile("" ::: "memory"); __builtin_amdgcn_s_barrier(); asm volatile("" ::: "memory"); } while (0)
#define BAR() do { asm volatile("s_waitcnt lgkmcnt(0)" ::: "memory"); __builtin_amdgcn_s_barrier(); asm volatile("" ::: "memory"); } while (0)

#define STAMP(K,I)

constexpr int S_ = 2048, D_ = 448, F_ = 1792, QKV_ = 1344;
constexpr unsigned ONES = 0x3F803F80u;

__device__ __attribute__((aligned(16))) const unsigned g_const[12] = {ONES, ONES, ONES, ONES, 0x3F80u, 0u, 0u, 0u, 0u, 0u, 0u, 0u};

__device__ __forceinline__ void st16_wt(void* p, u32x4 v) {
  asm volatile("global_store_dwordx4 %0, %1, off sc1\n\ts_nop 1" :: "v"(p), "v"(v) : "memory");
}
__device__ __forceinline__ float bf_lo(unsigned u) { return __uint_as_float(u << 16); }
__device__ __forceinline__ float bf_hi(unsigned u) { return __uint_as_float(u & 0xffff0000u); }
__device__ __forceinline__ unsigned pack_bf(float a, float b) {
  bf16x2 v = {(__bf16)a, (__bf16)b};
  return __builtin_bit_cast(unsigned, v);
}
__device__ __forceinline__ float rbf(float a) { return (float)(__bf16)a; }
__device__ __forceinline__ float dot2(unsigned a, unsigned b, float c) {
  return __builtin_amdgcn_fdot2_f32_bf16(__builtin_bit_cast(bf16x2, a), __builtin_bit_cast(bf16x2, b), c, false);
}
__device__ __forceinline__ u32x2 tr_read(const char* p) {
  s16x4 v = __builtin_amdgcn_ds_read_tr16_b64_v4i16((LDS_AS s16x4*)p);
  return __builtin_bit_cast(u32x2, v);
}
__device__ __forceinline__ u32x4 wfrag(const char* p) {
  u32x2 a = tr_read(p), b = tr_read(p + 16 * 128);
  u32x4 r = {a[0], a[1], b[0], b[1]};
  return r;
}
__device__ __forceinline__ f32x4 mfma(u32x4 a, u32x4 b, f32x4 c) {
  return __builtin_amdgcn_mfma_f32_16x16x32_bf16(__builtin_bit_cast(bf16x8, a), __builtin_bit_cast(bf16x8, b), c, 0, 0, 0);
}
__device__ __forceinline__ int rho_inv(int r) { return (r & ~31) | ((r & 12) << 1) | ((r & 16) >> 2) | (r & 3); }
__device__ __forceinline__ int tr_lane_off(int l, int nb) {
  int g = l >> 4, q = (l & 15) >> 2, p = l & 3;
  int swz = 2 * (g & 1) + (q >> 1);
  return (4 * g + q) * 128 + ((nb ^ swz) << 5) + p * 8;
}
__device__ __forceinline__ void ln_params(float sum, float sq, float& mb, float& rstd) {
  float mean = sum * (1.0f / D_);
  float var = fmaxf(sq * (1.0f / D_) - mean * mean, 0.0f);
  mb = rbf(mean);
  float den = rbf(sqrtf(rbf(rbf(var) + 1e-5f)));
  rstd = 1.0f / den;
}
__device__ __forceinline__ u32x4 add_bf8(u32x4 a, u32x4 b) {
  u32x4 r;
#pragma unroll
  for (int e = 0; e < 4; ++e) r[e] = pack_bf(bf_lo(a[e]) + bf_lo(b[e]), bf_hi(a[e]) + bf_hi(b[e]));
  return r;
}

constexpr int P1_WG = 64 * 128;
constexpr int P1_XO = 3 * P1_WG;
constexpr int P1_SLOT = 3 * P1_WG + 80 * 128;
constexpr int P1_R = 4;
constexpr int P1_SP = 672;
constexpr int P1_VO = 28 * P1_SP;
constexpr int P1_VP = 72;
constexpr int P1_SMEM = P1_R * P1_SLOT + 2048 + (80 * 2 + 3 * 64) * 4;

__global__ __launch_bounds__(768, 3) void k_qkv_attn(const u16* __restrict__ x, const u16* __restrict__ wqkv,
                                                     u16* __restrict__ o_frag, const u16* __restrict__ wo,
                                                     const u16* __restrict__ wg, const u16* __restrict__ wu,
                                                     const u16* __restrict__ wd, unsigned* __restrict__ cnt) {
  __shared__ __attribute__((aligned(16))) char smem[P1_SMEM];
  float* sSum = (float*)(smem + P1_R * P1_SLOT + 2048);
  float* sSq = sSum + 80;
  float* sCs = sSq + 80;

  const int tid = threadIdx.x, l = tid & 63, g = l >> 4, tl = l & 15;
  const int w = __builtin_amdgcn_readfirstlane(tid >> 6);
  const int bid = blockIdx.x, xcd = bid & 7, jj = bid >> 3;
  const int mt = xcd * 4 + (jj >> 3), hp = jj & 7;
  const int row0 = mt * 64 - 16;
  STAMP(0, 0);
  if (bid == 0 && tid < 16) cnt[tid * 32] = 0u;

  if (w >= 8) {
    const int lw = w - 8, lr = l >> 3, lc = l & 7;
    const u16* src[9];
    int dst[9], adv[9];
#pragma unroll
    for (int j = 0; j < 9; ++j) {
      const int pi = lw + 4 * j;
      if (pi < 24) {
        const int m = pi >> 3, rho = (pi & 7) * 8 + lr;
        const int c = lc ^ (((rho >> 1) & 3) << 1);
        if (c < 7) { src[j] = wqkv + (size_t)rho_inv(rho) * QKV_ + m * D_ + hp * 56 + c * 8; adv[j] = 64 * QKV_; }
        else { src[j] = (const u16*)(g_const + (m == 1 ? 4 : 8)); adv[j] = 0; }
        dst[j] = pi * 1024;
      } else if (pi < 34) {
        const int r = (pi - 24) * 8 + lr;
        const int c = lc ^ ((r >> 1) & 7);
        if (r > 0) { src[j] = x + (size_t)max(row0 + r, 0) * D_ + c * 8; adv[j] = 64; }
        else { src[j] = (const u16*)g_const; adv[j] = 0; }
        dst[j] = pi * 1024;
      } else { src[j] = (const u16*)g_const; adv[j] = 0; dst[j] = -1; }
    }
#define P1_ISSUE(ST) do { \
      char* sl_ = smem + ((ST) % P1_R) * P1_SLOT; \
      _Pragma("unroll") for (int j = 0; j < 9; ++j) { \
        GLDS(src[j] + (size_t)(ST) * adv[j], (dst[j] >= 0) ? sl_ + dst[j] : smem + P1_R * P1_SLOT + (lw & 1) * 1024); } \
    } while (0)
    P1_ISSUE(0); P1_ISSUE(1);
    WAITV(9);  BAR();
    P1_ISSUE(2);
    WAITV(9);  BAR();
    P1_ISSUE(3);
    WAITV(9);  BAR();
    P1_ISSUE(4);
    WAITV(9);  BAR();
    P1_ISSUE(5);
    WAITV(9);  BAR();
    P1_ISSUE(6);
    WAITV(9);  BAR();
    WAITV(0);  BAR();
    {
      char* dummy = smem + P1_R * P1_SLOT + (lw & 1) * 1024;
      const int pc = (bid * 4 + lw);
      const size_t e0 = ((size_t)pc * 64 + l) * 8;
      GLDS(wg + e0, dummy); GLDS(wu + e0, dummy); GLDS(wd + e0, dummy);
      if (pc < 544) { GLDS(wg + e0 + 1024 * 512, dummy); GLDS(wu + e0 + 1024 * 512, dummy); GLDS(wd + e0 + 1024 * 512, dummy); }
      if (pc < 392) GLDS(wo + e0, dummy);
    }
    BAR();
    BAR(); BAR();
    WAITV(0);
    return;
  }

  const int fb = w & 3, hf = w >> 2;
  int xo[3][2];
#pragma unroll
  for (int t = 0; t < 3; ++t) {
    const int tb = (t < 2) ? hf * 3 + t : (hf ? fb : 2);
    const int r = tb * 16 + tl;
#pragma unroll
    for (int s2 = 0; s2 < 2; ++s2) xo[t][s2] = P1_XO + r * 128 + (((4 * s2 + g) ^ ((r >> 1) & 7)) << 4);
  }
  f32x4 acc[3][3];
#pragma unroll
  for (int m = 0; m < 3; ++m)
#pragma unroll
    for (int t = 0; t < 3; ++t) acc[m][t] = (f32x4){0.f, 0.f, 0.f, 0.f};
  const int troff = tr_lane_off(l, fb);

  u32x4 xf[2][3], wf[2][3];
#define P1_LOAD(B, S) do { \
    const char* slot_ = smem + (((S) >> 1) % P1_R) * P1_SLOT; \
    _Pragma("unroll") for (int m = 0; m < 3; ++m) wf[B][m] = wfrag(slot_ + m * P1_WG + ((S) & 1) * 32 * 128 + troff); \
    _Pragma("unroll") for (int t = 0; t < 3; ++t) xf[B][t] = *(const u32x4*)(slot_ + xo[t][(S) & 1]); \
  } while (0)
#pragma unroll
  for (int ii = 0; ii < 7; ++ii) {
    RBAR();
    if (ii == 0) { STAMP(0, 1); P1_LOAD(0, 0); }
#pragma unroll
    for (int s2 = 0; s2 < 2; ++s2) {
      const int s = ii * 2 + s2;
      if (s == 1) RBAR();
      const char* sn = smem + (((s + 1) >> 1) % P1_R) * P1_SLOT;
      SB();
#pragma unroll
      for (int m = 0; m < 3; ++m) {
        acc[m][0] = mfma(wf[s & 1][m], xf[s & 1][0], acc[m][0]);
        acc[m][1] = mfma(wf[s & 1][m], xf[s & 1][1], acc[m][1]);
        if (hf == 0) acc[m][2] = mfma(wf[s & 1][m], xf[s & 1][2], acc[m][2]);
        else if (m == 0) acc[0][2] = mfma(xf[s & 1][2], xf[s & 1][2], acc[0][2]);
        else if (m == 1) acc[1][2] = mfma(xf[s & 1][1], xf[s & 1][1], acc[1][2]);
        SB();
        if (s + 1 < 14) {
          wf[(s + 1) & 1][m] = wfrag(sn + m * P1_WG + ((s + 1) & 1) * 32 * 128 + troff);
          xf[(s + 1) & 1][m] = *(const u32x4*)(sn + xo[m][(s + 1) & 1]);
        }
        SB();
      }
    }
  }
  STAMP(0, 2);
  if (fb == 3 && g == 2) {
#pragma unroll
    for (int t = 0; t < 3; ++t)
      if (hf == 0 || t < 2) sSum[(hf * 3 + t) * 16 + tl] = acc[1][t][0];
  }
  if (hf == 1 && g == (tl >> 2)) {
    float a0 = acc[0][2][0], a1 = acc[1][2][0];
#pragma unroll
    for (int r = 1; r < 4; ++r) { if ((tl & 3) == r) { a0 = acc[0][2][r]; a1 = acc[1][2][r]; } }
    sSq[fb * 16 + tl] = a0;
    if (fb == 0) sSq[64 + tl] = a1;
  }
  if (hf == 0 && tl == 0) {
#pragma unroll
    for (int m = 0; m < 3; ++m) *(f32x4*)(sCs + m * 64 + fb * 16 + 4 * g) = acc[m][0];
  }
  BAR();
  char* sS = smem;
  {
    f32x4 csf[3];
#pragma unroll
    for (int m = 0; m < 3; ++m) csf[m] = *(const f32x4*)(sCs + m * 64 + fb * 16 + 4 * g);
    const int quad = 4 * fb + g;
#pragma unroll
    for (int t = 0; t < 3; ++t) {
      if (hf == 0 || t < 2) {
        const int tb = hf * 3 + t;
        float mb, rstd;
        ln_params(sSum[tb * 16 + tl], sSq[tb * 16 + tl], mb, rstd);
#pragma unroll
        for (int m = 0; m < 3; ++m) {
          float v0 = (acc[m][t][0] - mb * csf[m][0]) * rstd;
          float v1 = (acc[m][t][1] - mb * csf[m][1]) * rstd;
          float v2 = (acc[m][t][2] - mb * csf[m][2]) * rstd;
          float v3 = (acc[m][t][3] - mb * csf[m][3]) * rstd;
          u32x2 pk = {pack_bf(v0, v1), pack_bf(v2, v3)};
          if (quad < 14) {
            if (m < 2) *(u32x2*)(sS + (m * 14 + quad) * P1_SP + (tb * 16 + tl) * 8) = pk;
            else *(u32x2*)(sS + P1_VO + ((quad >= 7 ? 80 : 0) + tb * 16 + tl) * P1_VP + (quad >= 7 ? quad - 7 : quad) * 8) = pk;
          }
        }
      }
    }
  }
  BAR();
  STAMP(0, 3);

  {
    const int hh = w >> 2, tq = (w & 3) + 1;
    const int qa = hh * 7 + 2 * g;
    const bool up = g < 3;
    const int qb = up ? qa + 1 : qa;
    u32x4 qf, kf[2];
    {
      u32x2 a = *(const u32x2*)(sS + (0 * 14 + qa) * P1_SP + (tq * 16 + tl) * 8);
      u32x2 b = *(const u32x2*)(sS + (0 * 14 + qb) * P1_SP + (tq * 16 + tl) * 8);
      qf = (u32x4){a[0], a[1], up ? b[0] : 0u, up ? b[1] : 0u};
#pragma unroll
      for (int c = 0; c < 2; ++c) {
        u32x2 ka = *(const u32x2*)(sS + (1 * 14 + qa) * P1_SP + ((tq - 1 + c) * 16 + tl) * 8);
        u32x2 kb = *(const u32x2*)(sS + (1 * 14 + qb) * P1_SP + ((tq - 1 + c) * 16 + tl) * 8);
        kf[c] = (u32x4){ka[0], ka[1], up ? kb[0] : 0u, up ? kb[1] : 0u};
      }
    }
    u32x4 vf[2];
    {
      const char* vb = sS + P1_VO + (hh * 80 + (tq - 1) * 16 + 4 * g + ((l & 15) >> 2)) * P1_VP + (l & 3) * 8;
#pragma unroll
      for (int db = 0; db < 2; ++db) {
        u32x2 a = tr_read(vb + db * 32), b = tr_read(vb + 16 * P1_VP + db * 32);
        vf[db] = (u32x4){a[0], a[1], b[0], b[1]};
      }
    }
    const f32x4 z4 = {0.f, 0.f, 0.f, 0.f};
    f32x4 sc[2];
    sc[0] = mfma(kf[0], qf, z4);
    sc[1] = mfma(kf[1], qf, z4);
    const int tok = mt * 64 + (tq - 1) * 16 + tl;
    const float scale = 0.1889822365046136f;
    float lg[8];
    float mx = -3.0e38f;
#pragma unroll
    for (int c = 0; c < 2; ++c)
#pragma unroll
      for (int r = 0; r < 4; ++r) {
        const int delta = tl - (4 * g + r) + 16 * (1 - c);
        const bool inwin = (delta >= 0) && (delta <= 14) && ((delta & 1) == 0);
        float v = rbf(rbf(sc[c][r]) * scale);
        v = (tok - delta >= 0) ? v : -1e9f;
        v = inwin ? v : -3.0e38f;
        lg[c * 4 + r] = v;
        mx = fmaxf(mx, v);
      }
    mx = fmaxf(mx, __shfl_xor(mx, 16));
    mx = fmaxf(mx, __shfl_xor(mx, 32));
    float den = 0.f;
#pragma unroll
    for (int i = 0; i < 8; ++i) { lg[i] = (lg[i] > -2.0e38f) ? __expf(lg[i] - mx) : 0.f; den += lg[i]; }
    den += __shfl_xor(den, 16);
    den += __shfl_xor(den, 32);
    const float inv = 1.0f / den;
    u32x4 pf = {pack_bf(lg[0] * inv, lg[1] * inv), pack_bf(lg[2] * inv, lg[3] * inv),
                pack_bf(lg[4] * inv, lg[5] * inv), pack_bf(lg[6] * inv, lg[7] * inv)};
    const int tbg = tok >> 4;
#pragma unroll
    for (int db = 0; db < 2; ++db) {
      f32x4 o = mfma(vf[db], pf, z4);
      const int qd = db * 4 + g;
      if (qd < 7) {
        int f = hp * 56 + (hh * 7 + qd) * 4;
        int s = f >> 5, ii = (f >> 4) & 1, gg = (f >> 2) & 3;
        u32x2 pk = {pack_bf(o[0], o[1]), pack_bf(o[2], o[3])};
        *(u32x2*)((char*)o_frag + ((size_t)((tbg * 14 + s) * 64 + gg * 16 + tl)) * 16 + ii * 8) = pk;
      }
    }
  }
  STAMP(0, 4);
}

constexpr int EP = 144;
constexpr int G_SLOT = 16384;
constexpr int P3_SLOT = 24576;
constexpr int P3_R = 6;
constexpr int K2_RING = 147456;
constexpr int K2_T = K2_RING;
constexpr int K2_CS = K2_T + 64 * EP;
constexpr int K2_LN = K2_CS + 512;
constexpr int K2_SMEM = K2_LN + 2048;
#define WAITVI(n) asm volatile("s_waitcnt vmcnt(%0)" :: "i"(n) : "memory")

template <int NST, int R, int PW>
__device__ __forceinline__ void g_loader_run(char* smem, const u16* ws0, const u16* ws1, const u16* xs, int lw) {
#define G_ISSUE(ST) do { \
      char* sl_ = smem + ((ST) % R) * G_SLOT; \
      if ((ST) >= PW) { GLDS(ws0 + (size_t)(ST) * 64 * D_, sl_ + lw * 1024); GLDS(ws1 + (size_t)(ST) * 64 * D_, sl_ + (lw + 4) * 1024); } \
      GLDS(xs + (size_t)(ST) * 1024, sl_ + 8192 + lw * 1024); \
      GLDS(xs + (size_t)(ST) * 1024 + 512, sl_ + 8192 + (4 + lw) * 1024); \
    } while (0)
  constexpr int PRE = 4;
#pragma unroll
  for (int st = 0; st < PRE; ++st) G_ISSUE(st);
#pragma unroll
  for (int i = 0; i < NST; ++i) {
    const int lim0 = (i == 0) ? PRE - 1 : ((PRE + 2 * i - 1 < i + R - 2) ? PRE + 2 * i - 1 : i + R - 2);
    const int issued = (lim0 < NST - 1) ? lim0 : NST - 1;
    const int need = (i + 1 < NST) ? i + 1 : NST - 1;
    int allow = 0;
#pragma unroll
    for (int st = need + 1; st <= issued; ++st) allow += (st < PW) ? 2 : 4;
    WAITVI(allow);
    BAR();
    const int lim1 = (PRE + 2 * i + 1 < i + R - 1) ? PRE + 2 * i + 1 : i + R - 1;
#pragma unroll
    for (int st = issued + 1; st <= lim1; ++st) if (st < NST) G_ISSUE(st);
  }
}

template <int NST, int R>
__device__ __forceinline__ void g_consumer_loop(const char* smem, int l, int tb, int fh, f32x4 (&acc)[2]) {
  constexpr int KS = NST * 2;
  const int tro0 = tr_lane_off(l, fh * 2), tro1 = tr_lane_off(l, fh * 2 + 1);
  const int xoff = 8192 + tb * 1024 + l * 16;
  u32x4 wf[2][2], xf[2];
#define G_LOAD(K) do { \
    const char* sl_ = smem + (((K) >> 1) % R) * G_SLOT; \
    wf[(K) & 1][0] = wfrag(sl_ + ((K) & 1) * 4096 + tro0); \
    wf[(K) & 1][1] = wfrag(sl_ + ((K) & 1) * 4096 + tro1); \
    xf[(K) & 1] = *(const u32x4*)(sl_ + ((K) & 1) * 4096 + xoff); \
  } while (0)
#pragma unroll
  for (int i = 0; i < NST; ++i) {
    RBAR();
    if (i == 0) G_LOAD(0);
#pragma unroll
    for (int j = 0; j < 2; ++j) {
      const int k = i * 2 + j;
      if (k + 1 < KS) G_LOAD(k + 1);
      SB();
      acc[0] = mfma(wf[k & 1][0], xf[k & 1], acc[0]);
      acc[1] = mfma(wf[k & 1][1], xf[k & 1], acc[1]);
      SB();
    }
  }
}

__device__ __forceinline__ void group_sync(unsigned* ca, unsigned* c, int l) {
  if (l == 0) {
    __hip_atomic_fetch_add(ca, 1u, __ATOMIC_RELAXED, __HIP_MEMORY_SCOPE_AGENT);
    for (int spin = 0; spin < (1 << 22); ++spin) {
      if (__hip_atomic_load(c, __ATOMIC_RELAXED, __HIP_MEMORY_SCOPE_AGENT) >= 28u) break;
      __builtin_amdgcn_s_sleep(1);
    }
  }
}

__global__ __launch_bounds__(768, 3) void k_fused(const u16* __restrict__ o_frag, const u16* __restrict__ wo,
                                                  const u16* __restrict__ x, const u16* __restrict__ wg,
                                                  const u16* __restrict__ wu, const u16* __restrict__ wd,
                                                  u16* h_frag, float* stats, u16* a_frag, u16* __restrict__ out,
                                                  unsigned* cnt) {
  __shared__ __attribute__((aligned(16))) char smem[K2_SMEM];
  char* sT = smem + K2_T;
  float* sCs = (float*)(smem + K2_CS);
  float2* sLN = (float2*)(smem + K2_LN);
  const int tid = threadIdx.x, l = tid & 63, g = l >> 4, tl = l & 15;
  const int w = __builtin_amdgcn_readfirstlane(tid >> 6);
  const int bid = blockIdx.x, grp = bid & 7, jj = bid >> 3;
  const int mt = grp * 4 + jj / 7, nt = jj % 7;
  const int m3 = (grp & 1) * 4 + jj / 7;
  const int n3 = (grp >> 1) * 7 + jj % 7;
  STAMP(1, 0);

  if (w >= 8) {
    const int lw = w - 8, lr = l >> 3, lc = l & 7;
    int woff[2];
#pragma unroll
    for (int j = 0; j < 2; ++j) {
      const int rho = (lw + 4 * j) * 8 + lr;
      woff[j] = rho * D_ + nt * 64 + (lc ^ (((rho >> 1) & 3) << 1)) * 8;
    }
    g_loader_run<7, 8, 0>(smem, wo + woff[0], wo + woff[1], o_frag + ((size_t)((mt * 4 + lw) * 14) * 64 + l) * 8, lw);
    BAR();
    const int rho3 = lw * 8 + lr;
    const int c3 = lc ^ (((rho3 >> 1) & 3) << 1);
    const u16* gs = wg + (size_t)rho_inv(rho3) * F_ + n3 * 64 + c3 * 8;
    const u16* us = wu + (size_t)rho_inv(rho3) * F_ + n3 * 64 + c3 * 8;
#pragma unroll
    for (int st = 0; st < 5; ++st) {
      GLDS(gs + (size_t)st * 32 * F_, smem + st * P3_SLOT + lw * 1024);
      GLDS(us + (size_t)st * 32 * F_, smem + st * P3_SLOT + 4096 + lw * 1024);
    }
    BAR();
    BAR();
    BAR();
    {
      const u16* xs = h_frag + ((size_t)((m3 * 16 + lw * 4) * 14) * 64 + l) * 8;
#define P3_ISSUE(ST) do { \
        char* sl_ = smem + ((ST) % P3_R) * P3_SLOT; \
        if ((ST) >= 5) { GLDS(gs + (size_t)(ST) * 32 * F_, sl_ + lw * 1024); GLDS(us + (size_t)(ST) * 32 * F_, sl_ + 4096 + lw * 1024); } \
        _Pragma("unroll") for (int j = 0; j < 4; ++j) GLDS(xs + (size_t)(j * 14 + (ST)) * 512, sl_ + 8192 + (lw * 4 + j) * 1024); \
      } while (0)
      P3_ISSUE(0); P3_ISSUE(1); P3_ISSUE(2);
#pragma unroll
      for (int i = 0; i < 14; ++i) {
        const int issued = (i == 0) ? 2 : ((i == 1) ? 4 : ((i + P3_R - 2 < 13) ? i + P3_R - 2 : 13));
        const int need = (i + 1 < 14) ? i + 1 : 13;
        int allow = 0;
#pragma unroll
        for (int st = need + 1; st <= issued; ++st) allow += (st < 5) ? 4 : 6;
        WAITVI(allow);
        BAR();
        if (i == 0) { P3_ISSUE(3); P3_ISSUE(4); }
        else if (i == 1) { P3_ISSUE(5); P3_ISSUE(6); }
        else if (i + P3_R - 1 < 14) P3_ISSUE(i + P3_R - 1);
        if (i == 12) {
          float a = 0.f, b = 0.f;
#pragma unroll
          for (int n = 0; n < 7; ++n) {
            const float2 s0 = *(const float2*)(stats + ((size_t)n * S_ + m3 * 256 + lw * 64 + l) * 2);
            a += s0.x; b += s0.y;
          }
          float mbv, rsv;
          ln_params(a, b, mbv, rsv);
          sLN[lw * 64 + l] = make_float2(mbv, rsv);
        }
      }
    }
    BAR();
#pragma unroll
    for (int st = 0; st < 8; ++st) {
      GLDS(wd + woff[0] + (size_t)st * 64 * D_, smem + st * G_SLOT + lw * 1024);
      GLDS(wd + woff[1] + (size_t)st * 64 * D_, smem + st * G_SLOT + (lw + 4) * 1024);
    }
    BAR();
    BAR();
    g_loader_run<28, 9, 8>(smem, wd + woff[0], wd + woff[1], a_frag + ((size_t)((mt * 4 + lw) * 56) * 64 + l) * 8, lw);
    BAR();
    return;
  }

  const int tb = w & 3, fh = w >> 2;
  const size_t rowoff = (size_t)(mt * 64 + tb * 16 + fh * 8 + (l >> 3)) * D_ + nt * 64 + (l & 7) * 8;
  char* tw = sT + tb * 16 * EP;
  u32x4 hv;
  {
    const u32x4 rv = *(const u32x4*)(x + rowoff);
    f32x4 acc[2] = {(f32x4){0.f, 0.f, 0.f, 0.f}, (f32x4){0.f, 0.f, 0.f, 0.f}};
    g_consumer_loop<7, 8>(smem, l, tb, fh, acc);
    STAMP(1, 1);
#pragma unroll
    for (int f = 0; f < 2; ++f) {
      u32x2 pk = {pack_bf(acc[f][0], acc[f][1]), pack_bf(acc[f][2], acc[f][3])};
      *(u32x2*)(tw + tl * EP + (fh * 2 + f) * 32 + g * 8) = pk;
    }
    BAR();
    char* p = tw + (fh * 8 + (l >> 3)) * EP + (l & 7) * 16;
    hv = add_bf8(rv, *(const u32x4*)p);
    *(u32x4*)p = hv;
    float sm = 0.f, sq = 0.f;
#pragma unroll
    for (int e = 0; e < 4; ++e) { sm = dot2(hv[e], ONES, sm); sq = dot2(hv[e], hv[e], sq); }
    sm += __shfl_xor(sm, 1); sq += __shfl_xor(sq, 1);
    sm += __shfl_xor(sm, 2); sq += __shfl_xor(sq, 2);
    sm += __shfl_xor(sm, 4); sq += __shfl_xor(sq, 4);
    if ((l & 7) == 0) {
      unsigned long long sv = ((unsigned long long)__float_as_uint(sq) << 32) | __float_as_uint(sm);
      __hip_atomic_store((unsigned long long*)(stats + ((size_t)nt * S_ + mt * 64 + tb * 16 + fh * 8 + (l >> 3)) * 2), sv,
                         __ATOMIC_RELAXED, __HIP_MEMORY_SCOPE_AGENT);
    }
    BAR();
    u32x4 v = *(const u32x4*)(tw + tl * EP + (fh * 4 + g) * 16);
    st16_wt((u32x4*)h_frag + (size_t)((mt * 4 + tb) * 14 + nt * 2 + fh) * 64 + l, v);
    WAITV(0);
    BAR();
    if (w == 0) group_sync(cnt + grp * 32, cnt + m3 * 32, l);
    BAR();
  }
  STAMP(1, 2);
  {
    const int tg = w >> 1, f3 = w & 1;
    f32x4 acc[4][4];
#pragma unroll
    for (int t = 0; t < 4; ++t)
#pragma unroll
      for (int f = 0; f < 4; ++f) acc[t][f] = (f32x4){0.f, 0.f, 0.f, 0.f};
    const int tro0 = tr_lane_off(l, f3 * 2), tro1 = tr_lane_off(l, f3 * 2 + 1);
    const int csoff = (w >> 2) * 4096 + tr_lane_off(l, w & 3);
    const int xoff = 8192 + tg * 4096 + l * 16;
    f32x4 csum = {0.f, 0.f, 0.f, 0.f};
    const u32x4 onesf = {ONES, ONES, ONES, ONES};
    u32x4 wf[2][4], wcs[2], xf[4];
#pragma unroll
    for (int s = 0; s < 14; ++s) {
      RBAR();
      if (s == 0) {
        STAMP(1, 3);
        wf[0][0] = wfrag(smem + tro0); wf[0][1] = wfrag(smem + tro1);
        wf[0][2] = wfrag(smem + 4096 + tro0); wf[0][3] = wfrag(smem + 4096 + tro1);
        wcs[0] = wfrag(smem + csoff);
#pragma unroll
        for (int t = 0; t < 4; ++t) xf[t] = *(const u32x4*)(smem + xoff + t * 1024);
      }
      int sx_ = ((s + 1) % P3_R) * P3_SLOT; asm volatile("" : "+s"(sx_));
      const char* sn = smem + sx_;
      SB();
#pragma unroll
      for (int t = 0; t < 4; ++t) {
#pragma unroll
        for (int f = 0; f < 4; ++f) acc[t][f] = mfma(wf[s & 1][f], xf[t], acc[t][f]);
        SB();
        if (s + 1 < 14) {
          xf[t] = *(const u32x4*)(sn + xoff + t * 1024);
          if (t == 0) { wf[(s + 1) & 1][0] = wfrag(sn + tro0); wf[(s + 1) & 1][1] = wfrag(sn + tro1); }
          if (t == 1) { wf[(s + 1) & 1][2] = wfrag(sn + 4096 + tro0); wf[(s + 1) & 1][3] = wfrag(sn + 4096 + tro1); }
          if (t == 2) wcs[(s + 1) & 1] = wfrag(sn + csoff);
        }
        SB();
      }
      csum = mfma(wcs[s & 1], onesf, csum);
      SB();
    }
    STAMP(1, 4);
    if (tl == 0) *(f32x4*)(sCs + w * 16 + 4 * g) = csum;
    BAR();
    float mb[4], rstd[4];
#pragma unroll
    for (int t = 0; t < 4; ++t) { float2 v = sLN[(tg * 4 + t) * 16 + tl]; mb[t] = v.x; rstd[t] = v.y; }
    f32x4 cg[2], cu[2];
#pragma unroll
    for (int i = 0; i < 2; ++i) {
      cg[i] = *(const f32x4*)(sCs + (f3 * 2 + i) * 16 + 4 * g);
      cu[i] = *(const f32x4*)(sCs + (4 + f3 * 2 + i) * 16 + 4 * g);
    }
#pragma unroll
    for (int t = 0; t < 4; ++t) {
      u32x4 o4;
#pragma unroll
      for (int i = 0; i < 2; ++i) {
        float av[4];
#pragma unroll
        for (int r = 0; r < 4; ++r) {
          float gv = (acc[t][i][r] - mb[t] * cg[i][r]) * rstd[t];
          float uv = (acc[t][2 + i][r] - mb[t] * cu[i][r]) * rstd[t];
          float sl = gv * __builtin_amdgcn_rcpf(1.0f + __expf(-gv));
          av[r] = sl * uv;
        }
        o4[i * 2] = pack_bf(av[0], av[1]);
        o4[i * 2 + 1] = pack_bf(av[2], av[3]);
      }
      const int tbg = m3 * 16 + tg * 4 + t;
      st16_wt((u32x4*)a_frag + (size_t)(tbg * 56 + n3 * 2 + f3) * 64 + l, o4);
    }
    WAITV(0);
    STAMP(1, 5);
    BAR();
    if (w == 0) group_sync(cnt + (8 + m3) * 32, cnt + (8 + grp) * 32, l);
    BAR();
  }
  STAMP(1, 6);
  {
    f32x4 acc[2] = {(f32x4){0.f, 0.f, 0.f, 0.f}, (f32x4){0.f, 0.f, 0.f, 0.f}};
    g_consumer_loop<28, 9>(smem, l, tb, fh, acc);
    STAMP(1, 7);
#pragma unroll
    for (int f = 0; f < 2; ++f) {
      u32x2 pk = {pack_bf(acc[f][0], acc[f][1]), pack_bf(acc[f][2], acc[f][3])};
      *(u32x2*)(tw + tl * EP + (fh * 2 + f) * 32 + g * 8) = pk;
    }
    BAR();
    const char* p = tw + (fh * 8 + (l >> 3)) * EP + (l & 7) * 16;
    *(u32x4*)(out + rowoff) = add_bf8(hv, *(const u32x4*)p);
  }
  STAMP(1, 8);
}

extern "C" void kernel_launch(void* const* d_in, const int* in_sizes, int n_in, void* d_out, int out_size,
                              void* d_ws, size_t ws_size, hipStream_t stream) {
  const u16* x = (const u16*)d_in[0];
  const u16* wqkv = (const u16*)d_in[1];
  const u16* wo = (const u16*)d_in[2];
  const u16* wg = (const u16*)d_in[4];
  const u16* wu = (const u16*)d_in[5];
  const u16* wd = (const u16*)d_in[6];
  u16* out = (u16*)d_out;
  char* ws = (char*)d_ws;
  u16* o_frag = (u16*)ws;
  u16* h_frag = (u16*)(ws + (4u << 20));
  u16* a_frag = (u16*)(ws + (6u << 20));
  float* stats = (float*)(ws + (14u << 20));
  unsigned* cnt = (unsigned*)(ws + (15u << 20));
  k_qkv_attn<<<256, 768, 0, stream>>>(x, wqkv, o_frag, wo, wg, wu, wd, cnt);
  k_fused<<<224, 768, 0, stream>>>(o_frag, wo, x, wg, wu, wd, h_frag, stats, a_frag, out, cnt);
}
```

```cpp
#include <hip/hip_runtime.h>
#include <hip/hip_bf16.h>
#include <stdint.h>

typedef unsigned short u16;
typedef __attribute__((ext_vector_type(8))) __bf16 bf16x8;
typedef __attribute__((ext_vector_type(4))) short s16x4;
typedef __attribute__((ext_vector_type(4))) float f32x4;
typedef __attribute__((ext_vector_type(4))) unsigned int u32x4;
typedef __attribute__((ext_vector_type(2))) unsigned int u32x2;
typedef __attribute__((ext_vector_type(2))) __bf16 bf16x2;
#define LDS_AS __attribute__((address_space(3)))
#define SB() __builtin_amdgcn_sched_barrier(0)
#define GLDS(gp, lp) __builtin_amdgcn_global_load_lds((const unsigned*)(gp), (unsigned*)(lp), 16, 0, 0)
#define WAITV(n) asm volatile("s_waitcnt vmcnt(" #n ")" ::: "memory")
#define RBAR() do { asm volatile("" ::: "memory"); __builtin_amdgcn_s_barrier(); asm volatile("" ::: "memory"); } while (0)
#define BAR() do { asm volatile("s_waitcnt lgkmcnt(0)" ::: "memory"); __builtin_amdgcn_s_barrier(); asm volatile("" ::: "memory"); } while (0)

#define STAMP(K,I)

constexpr int S_ = 2048, D_ = 448, F_ = 1792, QKV_ = 1344;
constexpr unsigned ONES = 0x3F803F80u;

__device__ __attribute__((aligned(16))) const unsigned g_const[12] = {ONES, ONES, ONES, ONES, 0x3F80u, 0u, 0u, 0u, 0u, 0u, 0u, 0u};

__device__ __forceinline__ void st16_wt(void* p, u32x4 v) {
  asm volatile("global_store_dwordx4 %0, %1, off sc1\n\ts_nop 1" :: "v"(p), "v"(v) : "memory");
}
__device__ __forceinline__ float bf_lo(unsigned u) { return __uint_as_float(u << 16); }
__device__ __forceinline__ float bf_hi(unsigned u) { return __uint_as_float(u & 0xffff0000u); }
__device__ __forceinline__ unsigned pack_bf(float a, float b) {
  bf16x2 v = {(__bf16)a, (__bf16)b};
  return __builtin_bit_cast(unsigned, v);
}
__device__ __forceinline__ float rbf(float a) { return (float)(__bf16)a; }
__device__ __forceinline__ float dot2(unsigned a, unsigned b, float c) {
  return __builtin_amdgcn_fdot2_f32_bf16(__builtin_bit_cast(bf16x2, a), __builtin_bit_cast(bf16x2, b), c, false);
}
__device__ __forceinline__ u32x2 tr_read(const char* p) {
  s16x4 v = __builtin_amdgcn_ds_read_tr16_b64_v4i16((LDS_AS s16x4*)p);
  return __builtin_bit_cast(u32x2, v);
}
__device__ __forceinline__ u32x4 wfrag(const char* p) {
  u32x2 a = tr_read(p), b = tr_read(p + 16 * 128);
  u32x4 r = {a[0], a[1], b[0], b[1]};
  return r;
}
__device__ __forceinline__ f32x4 mfma(u32x4 a, u32x4 b, f32x4 c) {
  return __builtin_amdgcn_mfma_f32_16x16x32_bf16(__builtin_bit_cast(bf16x8, a), __builtin_bit_cast(bf16x8, b), c, 0, 0, 0);
}
__device__ __forceinline__ int rho_inv(int r) { return (r & ~31) | ((r & 12) << 1) | ((r & 16) >> 2) | (r & 3); }
__device__ __forceinline__ int tr_lane_off(int l, int nb) {
  int g = l >> 4, q = (l & 15) >> 2, p = l & 3;
  int swz = 2 * (g & 1) + (q >> 1);
  return (4 * g + q) * 128 + ((nb ^ swz) << 5) + p * 8;
}
__device__ __forceinline__ void ln_params(float sum, float sq, float& mb, float& rstd) {
  float mean = sum * (1.0f / D_);
  float var = fmaxf(sq * (1.0f / D_) - mean * mean, 0.0f);
  mb = rbf(mean);
  float den = rbf(sqrtf(rbf(rbf(var) + 1e-5f)));
  rstd = 1.0f / den;
}
__device__ __forceinline__ u32x4 add_bf8(u32x4 a, u32x4 b) {
  u32x4 r;
#pragma unroll
  for (int e = 0; e < 4; ++e) r[e] = pack_bf(bf_lo(a[e]) + bf_lo(b[e]), bf_hi(a[e]) + bf_hi(b[e]));
  return r;
}

constexpr int P1_WG = 64 * 128;
constexpr int P1_XO = 3 * P1_WG;
constexpr int P1_SLOT = 3 * P1_WG + 80 * 128;
constexpr int P1_R = 4;
constexpr int P1_SP = 672;
constexpr int P1_VO = 28 * P1_SP;
constexpr int P1_VP = 72;
constexpr int P1_SMEM = P1_R * P1_SLOT + 2048 + (80 * 2 + 3 * 64) * 4;

__global__ __launch_bounds__(768, 3) void k_qkv_attn(const u16* __restrict__ x, const u16* __restrict__ wqkv,
                                                     u16* __restrict__ o_frag, const u16* __restrict__ wo,
                                                     const u16* __restrict__ wg, const u16* __restrict__ wu,
                                                     const u16* __restrict__ wd, unsigned* __restrict__ cnt) {
  __shared__ __attribute__((aligned(16))) char smem[P1_SMEM];
  float* sSum = (float*)(smem + P1_R * P1_SLOT + 2048);
  float* sSq = sSum + 80;
  float* sCs = sSq + 80;

  const int tid = threadIdx.x, l = tid & 63, g = l >> 4, tl = l & 15;
  const int w = __builtin_amdgcn_readfirstlane(tid >> 6);
  const int bid = blockIdx.x, xcd = bid & 7, jj = bid >> 3;
  const int mt = xcd * 4 + (jj >> 3), hp = jj & 7;
  const int row0 = mt * 64 - 16;
  STAMP(0, 0);
  if (bid == 0 && tid < 16) cnt[tid * 32] = 0u;

  if (w >= 8) {
    const int lw = w - 8, lr = l >> 3, lc = l & 7;
    const u16* src[9];
    int dst[9], adv[9];
#pragma unroll
    for (int j = 0; j < 9; ++j) {
      const int pi = lw + 4 * j;
      if (pi < 24) {
        const int m = pi >> 3, rho = (pi & 7) * 8 + lr;
        const int c = lc ^ (((rho >> 1) & 3) << 1);
        if (c < 7) { src[j] = wqkv + (size_t)rho_inv(rho) * QKV_ + m * D_ + hp * 56 + c * 8; adv[j] = 64 * QKV_; }
        else { src[j] = (const u16*)(g_const + (m == 1 ? 4 : 8)); adv[j] = 0; }
        dst[j] = pi * 1024;
      } else if (pi < 34) {
        const int r = (pi - 24) * 8 + lr;
        const int c = lc ^ ((r >> 1) & 7);
        if (r > 0) { src[j] = x + (size_t)max(row0 + r, 0) * D_ + c * 8; adv[j] = 64; }
        else { src[j] = (const u16*)g_const; adv[j] = 0; }
        dst[j] = pi * 1024;
      } else { src[j] = (const u16*)g_const; adv[j] = 0; dst[j] = -1; }
    }
#define P1_ISSUE(ST) do { \
      char* sl_ = smem + ((ST) % P1_R) * P1_SLOT; \
      _Pragma("unroll") for (int j = 0; j < 9; ++j) { \
        GLDS(src[j] + (size_t)(ST) * adv[j], (dst[j] >= 0) ? sl_ + dst[j] : smem + P1_R * P1_SLOT + (lw & 1) * 1024); } \
    } while (0)
    P1_ISSUE(0); P1_ISSUE(1); P1_ISSUE(2);
    WAITV(18); BAR();
    P1_ISSUE(3);
    WAITV(18); BAR();
    P1_ISSUE(4);
    WAITV(18); BAR();
    P1_ISSUE(5);
    WAITV(18); BAR();
    P1_ISSUE(6);
    WAITV(18); BAR();
    WAITV(9);  BAR();
    WAITV(0);  BAR();
    {
      char* dummy = smem + P1_R * P1_SLOT + (lw & 1) * 1024;
      const int pc = (bid * 4 + lw);
      const size_t e0 = ((size_t)pc * 64 + l) * 8;
      GLDS(wg + e0, dummy); GLDS(wu + e0, dummy); GLDS(wd + e0, dummy);
      if (pc < 544) { GLDS(wg + e0 + 1024 * 512, dummy); GLDS(wu + e0 + 1024 * 512, dummy); GLDS(wd + e0 + 1024 * 512, dummy); }
      if (pc < 392) GLDS(wo + e0, dummy);
    }
    BAR(); BAR();
    WAITV(0);
    return;
  }

  const int fb = w & 3, hf = w >> 2;
  int xo[3][2];
#pragma unroll
  for (int t = 0; t < 3; ++t) {
    const int tb = (t < 2) ? hf * 3 + t : (hf ? fb : 2);
    const int r = tb * 16 + tl;
#pragma unroll
    for (int s2 = 0; s2 < 2; ++s2) xo[t][s2] = P1_XO + r * 128 + (((4 * s2 + g) ^ ((r >> 1) & 7)) << 4);
  }
  f32x4 acc[3][3];
#pragma unroll
  for (int m = 0; m < 3; ++m)
#pragma unroll
    for (int t = 0; t < 3; ++t) acc[m][t] = (f32x4){0.f, 0.f, 0.f, 0.f};
  const int troff = tr_lane_off(l, fb);

  u32x4 xf[2][3], wf[2][3];
#define P1_LOAD(B, S) do { \
    const char* slot_ = smem + (((S) >> 1) % P1_R) * P1_SLOT; \
    _Pragma("unroll") for (int m = 0; m < 3; ++m) wf[B][m] = wfrag(slot_ + m * P1_WG + ((S) & 1) * 32 * 128 + troff); \
    _Pragma("unroll") for (int t = 0; t < 3; ++t) xf[B][t] = *(const u32x4*)(slot_ + xo[t][(S) & 1]); \
  } while (0)
#pragma unroll
  for (int ii = 0; ii < 7; ++ii) {
    RBAR();
    if (ii == 0) { STAMP(0, 1); }
    P1_LOAD(0, ii * 2);
#pragma unroll
    for (int s2 = 0; s2 < 2; ++s2) {
      const int s = ii * 2 + s2;
      const char* sn = smem + (ii % P1_R) * P1_SLOT;
      SB();
#pragma unroll
      for (int m = 0; m < 3; ++m) {
        if (s2 == 0) {
          wf[1][m] = wfrag(sn + m * P1_WG + 32 * 128 + troff);
          xf[1][m] = *(const u32x4*)(sn + xo[m][1]);
          SB();
        }
        acc[m][0] = mfma(wf[s & 1][m], xf[s & 1][0], acc[m][0]);
        acc[m][1] = mfma(wf[s & 1][m], xf[s & 1][1], acc[m][1]);
        if (hf == 0) acc[m][2] = mfma(wf[s & 1][m], xf[s & 1][2], acc[m][2]);
        else if (m == 0) acc[0][2] = mfma(xf[s & 1][2], xf[s & 1][2], acc[0][2]);
        else if (m == 1) acc[1][2] = mfma(xf[s & 1][1], xf[s & 1][1], acc[1][2]);
        SB();
      }
    }
  }
  STAMP(0, 2);
  if (fb == 3 && g == 2) {
#pragma unroll
    for (int t = 0; t < 3; ++t)
      if (hf == 0 || t < 2) sSum[(hf * 3 + t) * 16 + tl] = acc[1][t][0];
  }
  if (hf == 1 && g == (tl >> 2)) {
    float a0 = acc[0][2][0], a1 = acc[1][2][0];
#pragma unroll
    for (int r = 1; r < 4; ++r) { if ((tl & 3) == r) { a0 = acc[0][2][r]; a1 = acc[1][2][r]; } }
    sSq[fb * 16 + tl] = a0;
    if (fb == 0) sSq[64 + tl] = a1;
  }
  if (hf == 0 && tl == 0) {
#pragma unroll
    for (int m = 0; m < 3; ++m) *(f32x4*)(sCs + m * 64 + fb * 16 + 4 * g) = acc[m][0];
  }
  BAR();
  char* sS = smem;
  {
    f32x4 csf[3];
#pragma unroll
    for (int m = 0; m < 3; ++m) csf[m] = *(const f32x4*)(sCs + m * 64 + fb * 16 + 4 * g);
    const int quad = 4 * fb + g;
#pragma unroll
    for (int t = 0; t < 3; ++t) {
      if (hf == 0 || t < 2) {
        const int tb = hf * 3 + t;
        float mb, rstd;
        ln_params(sSum[tb * 16 + tl], sSq[tb * 16 + tl], mb, rstd);
#pragma unroll
        for (int m = 0; m < 3; ++m) {
          float v0 = (acc[m][t][0] - mb * csf[m][0]) * rstd;
          float v1 = (acc[m][t][1] - mb * csf[m][1]) * rstd;
          float v2 = (acc[m][t][2] - mb * csf[m][2]) * rstd;
          float v3 = (acc[m][t][3] - mb * csf[m][3]) * rstd;
          u32x2 pk = {pack_bf(v0, v1), pack_bf(v2, v3)};
          if (quad < 14) {
            if (m < 2) *(u32x2*)(sS + (m * 14 + quad) * P1_SP + (tb * 16 + tl) * 8) = pk;
            else *(u32x2*)(sS + P1_VO + ((quad >= 7 ? 80 : 0) + tb * 16 + tl) * P1_VP + (quad >= 7 ? quad - 7 : quad) * 8) = pk;
          }
        }
      }
    }
  }
  BAR();
  STAMP(0, 3);

  {
    const int hh = w >> 2, tq = (w & 3) + 1;
    const int qa = hh * 7 + 2 * g;
    const bool up = g < 3;
    const int qb = up ? qa + 1 : qa;
    u32x4 qf, kf[2];
    {
      u32x2 a = *(const u32x2*)(sS + (0 * 14 + qa) * P1_SP + (tq * 16 + tl) * 8);
      u32x2 b = *(const u32x2*)(sS + (0 * 14 + qb) * P1_SP + (tq * 16 + tl) * 8);
      qf = (u32x4){a[0], a[1], up ? b[0] : 0u, up ? b[1] : 0u};
#pragma unroll
      for (int c = 0; c < 2; ++c) {
        u32x2 ka = *(const u32x2*)(sS + (1 * 14 + qa) * P1_SP + ((tq - 1 + c) * 16 + tl) * 8);
        u32x2 kb = *(const u32x2*)(sS + (1 * 14 + qb) * P1_SP + ((tq - 1 + c) * 16 + tl) * 8);
        kf[c] = (u32x4){ka[0], ka[1], up ? kb[0] : 0u, up ? kb[1] : 0u};
      }
    }
    u32x4 vf[2];
    {
      const char* vb = sS + P1_VO + (hh * 80 + (tq - 1) * 16 + 4 * g + ((l & 15) >> 2)) * P1_VP + (l & 3) * 8;
#pragma unroll
      for (int db = 0; db < 2; ++db) {
        u32x2 a = tr_read(vb + db * 32), b = tr_read(vb + 16 * P1_VP + db * 32);
        vf[db] = (u32x4){a[0], a[1], b[0], b[1]};
      }
    }
    const f32x4 z4 = {0.f, 0.f, 0.f, 0.f};
    f32x4 sc[2];
    sc[0] = mfma(kf[0], qf, z4);
    sc[1] = mfma(kf[1], qf, z4);
    const int tok = mt * 64 + (tq - 1) * 16 + tl;
    const float scale = 0.1889822365046136f;
    float lg[8];
    float mx = -3.0e38f;
#pragma unroll
    for (int c = 0; c < 2; ++c)
#pragma unroll
      for (int r = 0; r < 4; ++r) {
        const int delta = tl - (4 * g + r) + 16 * (1 - c);
        const bool inwin = (delta >= 0) && (delta <= 14) && ((delta & 1) == 0);
        float v = rbf(rbf(sc[c][r]) * scale);
        v = (tok - delta >= 0) ? v : -1e9f;
        v = inwin ? v : -3.0e38f;
        lg[c * 4 + r] = v;
        mx = fmaxf(mx, v);
      }
    mx = fmaxf(mx, __shfl_xor(mx, 16));
    mx = fmaxf(mx, __shfl_xor(mx, 32));
    float den = 0.f;
#pragma unroll
    for (int i = 0; i < 8; ++i) { lg[i] = (lg[i] > -2.0e38f) ? __expf(lg[i] - mx) : 0.f; den += lg[i]; }
    den += __shfl_xor(den, 16);
    den += __shfl_xor(den, 32);
    const float inv = 1.0f / den;
    u32x4 pf = {pack_bf(lg[0] * inv, lg[1] * inv), pack_bf(lg[2] * inv, lg[3] * inv),
                pack_bf(lg[4] * inv, lg[5] * inv), pack_bf(lg[6] * inv, lg[7] * inv)};
    const int tbg = tok >> 4;
#pragma unroll
    for (int db = 0; db < 2; ++db) {
      f32x4 o = mfma(vf[db], pf, z4);
      const int qd = db * 4 + g;
      if (qd < 7) {
        int f = hp * 56 + (hh * 7 + qd) * 4;
        int s = f >> 5, ii = (f >> 4) & 1, gg = (f >> 2) & 3;
        u32x2 pk = {pack_bf(o[0], o[1]), pack_bf(o[2], o[3])};
        *(u32x2*)((char*)o_frag + ((size_t)((tbg * 14 + s) * 64 + gg * 16 + tl)) * 16 + ii * 8) = pk;
      }
    }
  }
  STAMP(0, 4);
}

constexpr int EP = 144;
constexpr int G_SLOT = 16384;
constexpr int P3_SLOT = 24576;
constexpr int P3_R = 6;
constexpr int K2_RING = 147456;
constexpr int K2_T = K2_RING;
constexpr int K2_CS = K2_T + 64 * EP;
constexpr int K2_LN = K2_CS + 512;
constexpr int K2_SMEM = K2_LN + 2048;
#define WAITVI(n) asm volatile("s_waitcnt vmcnt(%0)" :: "i"(n) : "memory")

template <int NST, int R, int PW>
__device__ __forceinline__ void g_loader_run(char* smem, const u16* ws0, const u16* ws1, const u16* xs, int lw) {
#define G_ISSUE(ST) do { \
      char* sl_ = smem + ((ST) % R) * G_SLOT; \
      if ((ST) >= PW) { GLDS(ws0 + (size_t)(ST) * 64 * D_, sl_ + lw * 1024); GLDS(ws1 + (size_t)(ST) * 64 * D_, sl_ + (lw + 4) * 1024); } \
      GLDS(xs + (size_t)(ST) * 1024, sl_ + 8192 + lw * 1024); \
      GLDS(xs + (size_t)(ST) * 1024 + 512, sl_ + 8192 + (4 + lw) * 1024); \
    } while (0)
  constexpr int PRE = 4;
#pragma unroll
  for (int st = 0; st < PRE; ++st) G_ISSUE(st);
#pragma unroll
  for (int i = 0; i < NST; ++i) {
    const int lim0 = (i == 0) ? PRE - 1 : ((PRE + 2 * i - 1 < i + R - 2) ? PRE + 2 * i - 1 : i + R - 2);
    const int issued = (lim0 < NST - 1) ? lim0 : NST - 1;
    const int need = (i + 1 < NST) ? i + 1 : NST - 1;
    int allow = 0;
#pragma unroll
    for (int st = need + 1; st <= issued; ++st) allow += (st < PW) ? 2 : 4;
    WAITVI(allow);
    BAR();
    const int lim1 = (PRE + 2 * i + 1 < i + R - 1) ? PRE + 2 * i + 1 : i + R - 1;
#pragma unroll
    for (int st = issued + 1; st <= lim1; ++st) if (st < NST) G_ISSUE(st);
  }
}

template <int NST, int R>
__device__ __forceinline__ void g_consumer_loop(const char* smem, int l, int tb, int fh, f32x4 (&acc)[2]) {
  constexpr int KS = NST * 2;
  const int tro0 = tr_lane_off(l, fh * 2), tro1 = tr_lane_off(l, fh * 2 + 1);
  const int xoff = 8192 + tb * 1024 + l * 16;
  u32x4 wf[2][2], xf[2];
#define G_LOAD(K) do { \
    const char* sl_ = smem + (((K) >> 1) % R) * G_SLOT; \
    wf[(K) & 1][0] = wfrag(sl_ + ((K) & 1) * 4096 + tro0); \
    wf[(K) & 1][1] = wfrag(sl_ + ((K) & 1) * 4096 + tro1); \
    xf[(K) & 1] = *(const u32x4*)(sl_ + ((K) & 1) * 4096 + xoff); \
  } while (0)
#pragma unroll
  for (int i = 0; i < NST; ++i) {
    RBAR();
    if (i == 0) G_LOAD(0);
#pragma unroll
    for (int j = 0; j < 2; ++j) {
      const int k = i * 2 + j;
      if (k + 1 < KS) G_LOAD(k + 1);
      SB();
      acc[0] = mfma(wf[k & 1][0], xf[k & 1], acc[0]);
      acc[1] = mfma(wf[k & 1][1], xf[k & 1], acc[1]);
      SB();
    }
  }
}

__device__ __forceinline__ void group_sync(unsigned* c, int l) {
  if (l == 0) {
    __hip_atomic_fetch_add(c, 1u, __ATOMIC_RELAXED, __HIP_MEMORY_SCOPE_AGENT);
    for (int spin = 0; spin < (1 << 22); ++spin) {
      if (__hip_atomic_load(c, __ATOMIC_RELAXED, __HIP_MEMORY_SCOPE_AGENT) >= 28u) break;
      __builtin_amdgcn_s_sleep(1);
    }
  }
}

__global__ __launch_bounds__(768, 3) void k_fused(const u16* __restrict__ o_frag, const u16* __restrict__ wo,
                                                  const u16* __restrict__ x, const u16* __restrict__ wg,
                                                  const u16* __restrict__ wu, const u16* __restrict__ wd,
                                                  u16* h_frag, float* stats, u16* a_frag, u16* __restrict__ out,
                                                  unsigned* cnt) {
  __shared__ __attribute__((aligned(16))) char smem[K2_SMEM];
  char* sT = smem + K2_T;
  float* sCs = (float*)(smem + K2_CS);
  float2* sLN = (float2*)(smem + K2_LN);
  const int tid = threadIdx.x, l = tid & 63, g = l >> 4, tl = l & 15;
  const int w = __builtin_amdgcn_readfirstlane(tid >> 6);
  const int bid = blockIdx.x, grp = bid & 7, jj = bid >> 3;
  const int mt = grp * 4 + jj / 7, nt = jj % 7;
  const int n3 = jj;
  STAMP(1, 0);

  if (w >= 8) {
    const int lw = w - 8, lr = l >> 3, lc = l & 7;
    int woff[2];
#pragma unroll
    for (int j = 0; j < 2; ++j) {
      const int rho = (lw + 4 * j) * 8 + lr;
      woff[j] = rho * D_ + nt * 64 + (lc ^ (((rho >> 1) & 3) << 1)) * 8;
    }
    g_loader_run<7, 8, 0>(smem, wo + woff[0], wo + woff[1], o_frag + ((size_t)((mt * 4 + lw) * 14) * 64 + l) * 8, lw);
    BAR();
    const int rho3 = lw * 8 + lr;
    const int c3 = lc ^ (((rho3 >> 1) & 3) << 1);
    const u16* gs = wg + (size_t)rho_inv(rho3) * F_ + n3 * 64 + c3 * 8;
    const u16* us = wu + (size_t)rho_inv(rho3) * F_ + n3 * 64 + c3 * 8;
#pragma unroll
    for (int st = 0; st < 5; ++st) {
      GLDS(gs + (size_t)st * 32 * F_, smem + st * P3_SLOT + lw * 1024);
      GLDS(us + (size_t)st * 32 * F_, smem + st * P3_SLOT + 4096 + lw * 1024);
    }
    BAR();
    BAR();
    BAR();
    {
      const u16* xs = h_frag + ((size_t)((grp * 16 + lw * 4) * 14) * 64 + l) * 8;
#define P3_ISSUE(ST) do { \
        char* sl_ = smem + ((ST) % P3_R) * P3_SLOT; \
        if ((ST) >= 5) { GLDS(gs + (size_t)(ST) * 32 * F_, sl_ + lw * 1024); GLDS(us + (size_t)(ST) * 32 * F_, sl_ + 4096 + lw * 1024); } \
        _Pragma("unroll") for (int j = 0; j < 4; ++j) GLDS(xs + (size_t)(j * 14 + (ST)) * 512, sl_ + 8192 + (lw * 4 + j) * 1024); \
      } while (0)
      P3_ISSUE(0); P3_ISSUE(1); P3_ISSUE(2);
#pragma unroll
      for (int i = 0; i < 14; ++i) {
        const int issued = (i == 0) ? 2 : ((i == 1) ? 4 : ((i + P3_R - 2 < 13) ? i + P3_R - 2 : 13));
        const int need = (i + 1 < 14) ? i + 1 : 13;
        int allow = 0;
#pragma unroll
        for (int st = need + 1; st <= issued; ++st) allow += (st < 5) ? 4 : 6;
        WAITVI(allow);
        BAR();
        if (i == 0) { P3_ISSUE(3); P3_ISSUE(4); }
        else if (i == 1) { P3_ISSUE(5); P3_ISSUE(6); }
        else if (i + P3_R - 1 < 14) P3_ISSUE(i + P3_R - 1);
        if (i == 12) {
          float a = 0.f, b = 0.f;
#pragma unroll
          for (int n = 0; n < 7; ++n) {
            const float2 s0 = *(const float2*)(stats + ((size_t)n * S_ + grp * 256 + lw * 64 + l) * 2);
            a += s0.x; b += s0.y;
          }
          float mbv, rsv;
          ln_params(a, b, mbv, rsv);
          sLN[lw * 64 + l] = make_float2(mbv, rsv);
        }
      }
    }
    BAR();
#pragma unroll
    for (int st = 0; st < 8; ++st) {
      GLDS(wd + woff[0] + (size_t)st * 64 * D_, smem + st * G_SLOT + lw * 1024);
      GLDS(wd + woff[1] + (size_t)st * 64 * D_, smem + st * G_SLOT + (lw + 4) * 1024);
    }
    BAR();
    BAR();
    g_loader_run<28, 9, 8>(smem, wd + woff[0], wd + woff[1], a_frag + ((size_t)((mt * 4 + lw) * 56) * 64 + l) * 8, lw);
    BAR();
    return;
  }

  const int tb = w & 3, fh = w >> 2;
  const size_t rowoff = (size_t)(mt * 64 + tb * 16 + fh * 8 + (l >> 3)) * D_ + nt * 64 + (l & 7) * 8;
  char* tw = sT + tb * 16 * EP;
  u32x4 hv;
  {
    const u32x4 rv = *(const u32x4*)(x + rowoff);
    f32x4 acc[2] = {(f32x4){0.f, 0.f, 0.f, 0.f}, (f32x4){0.f, 0.f, 0.f, 0.f}};
    g_consumer_loop<7, 8>(smem, l, tb, fh, acc);
    STAMP(1, 1);
#pragma unroll
    for (int f = 0; f < 2; ++f) {
      u32x2 pk = {pack_bf(acc[f][0], acc[f][1]), pack_bf(acc[f][2], acc[f][3])};
      *(u32x2*)(tw + tl * EP + (fh * 2 + f) * 32 + g * 8) = pk;
    }
    BAR();
    char* p = tw + (fh * 8 + (l >> 3)) * EP + (l & 7) * 16;
    hv = add_bf8(rv, *(const u32x4*)p);
    *(u32x4*)p = hv;
    float sm = 0.f, sq = 0.f;
#pragma unroll
    for (int e = 0; e < 4; ++e) { sm = dot2(hv[e], ONES, sm); sq = dot2(hv[e], hv[e], sq); }
    sm += __shfl_xor(sm, 1); sq += __shfl_xor(sq, 1);
    sm += __shfl_xor(sm, 2); sq += __shfl_xor(sq, 2);
    sm += __shfl_xor(sm, 4); sq += __shfl_xor(sq, 4);
    if ((l & 7) == 0) {
      unsigned long long sv = ((unsigned long long)__float_as_uint(sq) << 32) | __float_as_uint(sm);
      __hip_atomic_store((unsigned long long*)(stats + ((size_t)nt * S_ + mt * 64 + tb * 16 + fh * 8 + (l >> 3)) * 2), sv,
                         __ATOMIC_RELAXED, __HIP_MEMORY_SCOPE_AGENT);
    }
    BAR();
    u32x4 v = *(const u32x4*)(tw + tl * EP + (fh * 4 + g) * 16);
    st16_wt((u32x4*)h_frag + (size_t)((mt * 4 + tb) * 14 + nt * 2 + fh) * 64 + l, v);
    WAITV(0);
    BAR();
    if (w == 0) group_sync(cnt + grp * 32, l);
    BAR();
  }
  STAMP(1, 2);
  {
    const int tg = w >> 1, f3 = w & 1;
    f32x4 acc[4][4];
#pragma unroll
    for (int t = 0; t < 4; ++t)
#pragma unroll
      for (int f = 0; f < 4; ++f) acc[t][f] = (f32x4){0.f, 0.f, 0.f, 0.f};
    const int tro0 = tr_lane_off(l, f3 * 2), tro1 = tr_lane_off(l, f3 * 2 + 1);
    const int csoff = (w >> 2) * 4096 + tr_lane_off(l, w & 3);
    const int xoff = 8192 + tg * 4096 + l * 16;
    f32x4 csum = {0.f, 0.f, 0.f, 0.f};
    const u32x4 onesf = {ONES, ONES, ONES, ONES};
    u32x4 wf[2][4], wcs[2], xf[4];
#pragma unroll
    for (int s = 0; s < 14; ++s) {
      RBAR();
      if (s == 0) {
        STAMP(1, 3);
        wf[0][0] = wfrag(smem + tro0); wf[0][1] = wfrag(smem + tro1);
        wf[0][2] = wfrag(smem + 4096 + tro0); wf[0][3] = wfrag(smem + 4096 + tro1);
        wcs[0] = wfrag(smem + csoff);
#pragma unroll
        for (int t = 0; t < 4; ++t) xf[t] = *(const u32x4*)(smem + xoff + t * 1024);
      }
      int sx_ = ((s + 1) % P3_R) * P3_SLOT; asm volatile("" : "+s"(sx_));
      const char* sn = smem + sx_;
      SB();
#pragma unroll
      for (int t = 0; t < 4; ++t) {
#pragma unroll
        for (int f = 0; f < 4; ++f) acc[t][f] = mfma(wf[s & 1][f], xf[t], acc[t][f]);
        SB();
        if (s + 1 < 14) {
          xf[t] = *(const u32x4*)(sn + xoff + t * 1024);
          if (t == 0) { wf[(s + 1) & 1][0] = wfrag(sn + tro0); wf[(s + 1) & 1][1] = wfrag(sn + tro1); }
          if (t == 1) { wf[(s + 1) & 1][2] = wfrag(sn + 4096 + tro0); wf[(s + 1) & 1][3] = wfrag(sn + 4096 + tro1); }
          if (t == 2) wcs[(s + 1) & 1] = wfrag(sn + csoff);
        }
        SB();
      }
      csum = mfma(wcs[s & 1], onesf, csum);
      SB();
    }
    STAMP(1, 4);
    if (tl == 0) *(f32x4*)(sCs + w * 16 + 4 * g) = csum;
    BAR();
    float mb[4], rstd[4];
#pragma unroll
    for (int t = 0; t < 4; ++t) { float2 v = sLN[(tg * 4 + t) * 16 + tl]; mb[t] = v.x; rstd[t] = v.y; }
    f32x4 cg[2], cu[2];
#pragma unroll
    for (int i = 0; i < 2; ++i) {
      cg[i] = *(const f32x4*)(sCs + (f3 * 2 + i) * 16 + 4 * g);
      cu[i] = *(const f32x4*)(sCs + (4 + f3 * 2 + i) * 16 + 4 * g);
    }
#pragma unroll
    for (int t = 0; t < 4; ++t) {
      u32x4 o4;
#pragma unroll
      for (int i = 0; i < 2; ++i) {
        float av[4];
#pragma unroll
        for (int r = 0; r < 4; ++r) {
          float gv = (acc[t][i][r] - mb[t] * cg[i][r]) * rstd[t];
          float uv = (acc[t][2 + i][r] - mb[t] * cu[i][r]) * rstd[t];
          float sl = gv * __builtin_amdgcn_rcpf(1.0f + __expf(-gv));
          av[r] = sl * uv;
        }
        o4[i * 2] = pack_bf(av[0], av[1]);
        o4[i * 2 + 1] = pack_bf(av[2], av[3]);
      }
      const int tbg = grp * 16 + tg * 4 + t;
      st16_wt((u32x4*)a_frag + (size_t)(tbg * 56 + n3 * 2 + f3) * 64 + l, o4);
    }
    WAITV(0);
    STAMP(1, 5);
    BAR();
    if (w == 0) group_sync(cnt + (8 + grp) * 32, l);
    BAR();
  }
  STAMP(1, 6);
  {
    f32x4 acc[2] = {(f32x4){0.f, 0.f, 0.f, 0.f}, (f32x4){0.f, 0.f, 0.f, 0.f}};
    g_consumer_loop<28, 9>(smem, l, tb, fh, acc);
    STAMP(1, 7);
#pragma unroll
    for (int f = 0; f < 2; ++f) {
      u32x2 pk = {pack_bf(acc[f][0], acc[f][1]), pack_bf(acc[f][2], acc[f][3])};
      *(u32x2*)(tw + tl * EP + (fh * 2 + f) * 32 + g * 8) = pk;
    }
    BAR();
    const char* p = tw + (fh * 8 + (l >> 3)) * EP + (l & 7) * 16;
    *(u32x4*)(out + rowoff) = add_bf8(hv, *(const u32x4*)p);
  }
  STAMP(1, 8);
}

extern "C" void kernel_launch(void* const* d_in, const int* in_sizes, int n_in, void* d_out, int out_size,
                              void* d_ws, size_t ws_size, hipStream_t stream) {
  const u16* x = (const u16*)d_in[0];
  const u16* wqkv = (const u16*)d_in[1];
  const u16* wo = (const u16*)d_in[2];
  const u16* wg = (const u16*)d_in[4];
  const u16* wu = (const u16*)d_in[5];
  const u16* wd = (const u16*)d_in[6];
  u16* out = (u16*)d_out;
  char* ws = (char*)d_ws;
  u16* o_frag = (u16*)ws;
  u16* h_frag = (u16*)(ws + (4u << 20));
  u16* a_frag = (u16*)(ws + (6u << 20));
  float* stats = (float*)(ws + (14u << 20));
  unsigned* cnt = (unsigned*)(ws + (15u << 20));
  k_qkv_attn<<<256, 768, 0, stream>>>(x, wqkv, o_frag, wo, wg, wu, wd, cnt);
  k_fused<<<224, 768, 0, stream>>>(o_frag, wo, x, wg, wu, wd, h_frag, stats, a_frag, out, cnt);
}
```

```cpp
#include <hip/hip_runtime.h>
#include <hip/hip_bf16.h>
#include <stdint.h>

typedef unsigned short u16;
typedef __attribute__((ext_vector_type(8))) __bf16 bf16x8;
typedef __attribute__((ext_vector_type(4))) short s16x4;
typedef __attribute__((ext_vector_type(4))) float f32x4;
typedef __attribute__((ext_vector_type(4))) unsigned int u32x4;
typedef __attribute__((ext_vector_type(2))) unsigned int u32x2;
typedef __attribute__((ext_vector_type(2))) __bf16 bf16x2;
#define LDS_AS __attribute__((address_space(3)))
#define SB() __builtin_amdgcn_sched_barrier(0)
#define GLDS(gp, lp) __builtin_amdgcn_global_load_lds((const unsigned*)(gp), (unsigned*)(lp), 16, 0, 0)
#define WAITV(n) asm volatile("s_waitcnt vmcnt(" #n ")" ::: "memory")
#define RBAR() do { asm volatile("" ::: "memory"); __builtin_amdgcn_s_barrier(); asm volatile("" ::: "memory"); } while (0)
#define BAR() do { asm volatile("s_waitcnt lgkmcnt(0)" ::: "memory"); __builtin_amdgcn_s_barrier(); asm volatile("" ::: "memory"); } while (0)

#define STAMP(K,I)

constexpr int S_ = 2048, D_ = 448, F_ = 1792, QKV_ = 1344;
constexpr unsigned ONES = 0x3F803F80u;

__device__ __attribute__((aligned(16))) const unsigned g_const[12] = {ONES, ONES, ONES, ONES, 0x3F80u, 0u, 0u, 0u, 0u, 0u, 0u, 0u};

__device__ __forceinline__ void st16_wt(void* p, u32x4 v) {
  asm volatile("global_store_dwordx4 %0, %1, off sc1\n\ts_nop 1" :: "v"(p), "v"(v) : "memory");
}
__device__ __forceinline__ float bf_lo(unsigned u) { return __uint_as_float(u << 16); }
__device__ __forceinline__ float bf_hi(unsigned u) { return __uint_as_float(u & 0xffff0000u); }
__device__ __forceinline__ unsigned pack_bf(float a, float b) {
  bf16x2 v = {(__bf16)a, (__bf16)b};
  return __builtin_bit_cast(unsigned, v);
}
__device__ __forceinline__ float rbf(float a) { return (float)(__bf16)a; }
__device__ __forceinline__ float dot2(unsigned a, unsigned b, float c) {
  return __builtin_amdgcn_fdot2_f32_bf16(__builtin_bit_cast(bf16x2, a), __builtin_bit_cast(bf16x2, b), c, false);
}
__device__ __forceinline__ u32x2 tr_read(const char* p) {
  s16x4 v = __builtin_amdgcn_ds_read_tr16_b64_v4i16((LDS_AS s16x4*)p);
  return __builtin_bit_cast(u32x2, v);
}
__device__ __forceinline__ u32x4 wfrag(const char* p) {
  u32x2 a = tr_read(p), b = tr_read(p + 16 * 128);
  u32x4 r = {a[0], a[1], b[0], b[1]};
  return r;
}
__device__ __forceinline__ f32x4 mfma(u32x4 a, u32x4 b, f32x4 c) {
  return __builtin_amdgcn_mfma_f32_16x16x32_bf16(__builtin_bit_cast(bf16x8, a), __builtin_bit_cast(bf16x8, b), c, 0, 0, 0);
}
__device__ __forceinline__ int rho_inv(int r) { return (r & ~31) | ((r & 12) << 1) | ((r & 16) >> 2) | (r & 3); }
__device__ __forceinline__ int tr_lane_off(int l, int nb) {
  int g = l >> 4, q = (l & 15) >> 2, p = l & 3;
  int swz = 2 * (g & 1) + (q >> 1);
  return (4 * g + q) * 128 + ((nb ^ swz) << 5) + p * 8;
}
__device__ __forceinline__ void ln_params(float sum, float sq, float& mb, float& rstd) {
  float mean = sum * (1.0f / D_);
  float var = fmaxf(sq * (1.0f / D_) - mean * mean, 0.0f);
  mb = rbf(mean);
  float den = rbf(sqrtf(rbf(rbf(var) + 1e-5f)));
  rstd = 1.0f / den;
}
__device__ __forceinline__ u32x4 add_bf8(u32x4 a, u32x4 b) {
  u32x4 r;
#pragma unroll
  for (int e = 0; e < 4; ++e) r[e] = pack_bf(bf_lo(a[e]) + bf_lo(b[e]), bf_hi(a[e]) + bf_hi(b[e]));
  return r;
}

constexpr int P1_WG = 64 * 128;
constexpr int P1_XO = 3 * P1_WG;
constexpr int P1_SLOT = 3 * P1_WG + 80 * 128;
constexpr int P1_R = 4;
constexpr int P1_SP = 672;
constexpr int P1_VO = 28 * P1_SP;
constexpr int P1_VP = 72;
constexpr int P1_SMEM = P1_R * P1_SLOT + 2048 + (80 * 2 + 3 * 64) * 4;

__global__ __launch_bounds__(768, 3) void k_qkv_attn(const u16* __restrict__ x, const u16* __restrict__ wqkv,
                                                     u16* __restrict__ o_frag, const u16* __restrict__ wo,
                                                     const u16* __restrict__ wg, const u16* __restrict__ wu,
                                                     const u16* __restrict__ wd, unsigned* __restrict__ cnt) {
  __shared__ __attribute__((aligned(16))) char smem[P1_SMEM];
  float* sSum = (float*)(smem + P1_R * P1_SLOT + 2048);
  float* sSq = sSum + 80;
  float* sCs = sSq + 80;

  const int tid = threadIdx.x, l = tid & 63, g = l >> 4, tl = l & 15;
  const int w = __builtin_amdgcn_readfirstlane(tid >> 6);
  const int bid = blockIdx.x, xcd = bid & 7, jj = bid >> 3;
  const int mt = xcd * 4 + (jj >> 3), hp = jj & 7;
  const int row0 = mt * 64 - 16;
  STAMP(0, 0);
  if (bid == 0 && tid < 16) cnt[tid * 32] = 0u;

  if (w >= 8) {
    const int lw = w - 8, lr = l >> 3, lc = l & 7;
    const u16* src[9];
    int dst[9], adv[9];
#pragma unroll
    for (int j = 0; j < 9; ++j) {
      const int pi = lw + 4 * j;
      if (pi < 24) {
        const int m = pi >> 3, rho = (pi & 7) * 8 + lr;
        const int c = lc ^ (((rho >> 1) & 3) << 1);
        if (c < 7) { src[j] = wqkv + (size_t)rho_inv(rho) * QKV_ + m * D_ + hp * 56 + c * 8; adv[j] = 64 * QKV_; }
        else { src[j] = (const u16*)(g_const + (m == 1 ? 4 : 8)); adv[j] = 0; }
        dst[j] = pi * 1024;
      } else if (pi < 34) {
        const int r = (pi - 24) * 8 + lr;
        const int c = lc ^ ((r >> 1) & 7);
        if (r > 0) { src[j] = x + (size_t)max(row0 + r, 0) * D_ + c * 8; adv[j] = 64; }
        else { src[j] = (const u16*)g_const; adv[j] = 0; }
        dst[j] = pi * 1024;
      } else { src[j] = (const u16*)g_const; adv[j] = 0; dst[j] = -1; }
    }
#define P1_ISSUE(ST) do { \
      char* sl_ = smem + ((ST) % P1_R) * P1_SLOT; \
      _Pragma("unroll") for (int j = 0; j < 9; ++j) { \
        GLDS(src[j] + (size_t)(ST) * adv[j], (dst[j] >= 0) ? sl_ + dst[j] : smem + P1_R * P1_SLOT + (lw & 1) * 1024); } \
    } while (0)
    P1_ISSUE(0);
    {
      char* dummy = smem + P1_R * P1_SLOT + (lw & 1) * 1024;
      const int pc = (bid * 4 + lw);
      GLDS(wqkv + ((size_t)pc * 64 + l) * 8, dummy);
      GLDS(wqkv + ((size_t)(pc < 152 ? pc + 1024 : pc) * 64 + l) * 8, dummy);
      GLDS(x + ((size_t)pc * 64 + l) * 8, dummy);
      GLDS(x + ((size_t)(pc < 768 ? pc + 1024 : pc) * 64 + l) * 8, dummy);
    }
    P1_ISSUE(1); P1_ISSUE(2);
    WAITV(22); BAR();
    P1_ISSUE(3);
    WAITV(18); BAR();
    P1_ISSUE(4);
    WAITV(18); BAR();
    P1_ISSUE(5);
    WAITV(18); BAR();
    P1_ISSUE(6);
    WAITV(18); BAR();
    WAITV(9);  BAR();
    WAITV(0);  BAR();
    {
      char* dummy = smem + P1_R * P1_SLOT + (lw & 1) * 1024;
      const int pc = (bid * 4 + lw);
      const size_t e0 = ((size_t)pc * 64 + l) * 8;
      GLDS(wg + e0, dummy); GLDS(wu + e0, dummy); GLDS(wd + e0, dummy);
      if (pc < 544) { GLDS(wg + e0 + 1024 * 512, dummy); GLDS(wu + e0 + 1024 * 512, dummy); GLDS(wd + e0 + 1024 * 512, dummy); }
      if (pc < 392) GLDS(wo + e0, dummy);
    }
    BAR(); BAR();
    WAITV(0);
    return;
  }

  const int fb = w & 3, hf = w >> 2;
  int xo[3][2];
#pragma unroll
  for (int t = 0; t < 3; ++t) {
    const int tb = (t < 2) ? hf * 3 + t : (hf ? fb : 2);
    const int r = tb * 16 + tl;
#pragma unroll
    for (int s2 = 0; s2 < 2; ++s2) xo[t][s2] = P1_XO + r * 128 + (((4 * s2 + g) ^ ((r >> 1) & 7)) << 4);
  }
  f32x4 acc[3][3];
#pragma unroll
  for (int m = 0; m < 3; ++m)
#pragma unroll
    for (int t = 0; t < 3; ++t) acc[m][t] = (f32x4){0.f, 0.f, 0.f, 0.f};
  const int troff = tr_lane_off(l, fb);

  u32x4 xf[2][3], wf[2][3];
#define P1_LOAD(B, S) do { \
    const char* slot_ = smem + (((S) >> 1) % P1_R) * P1_SLOT; \
    _Pragma("unroll") for (int m = 0; m < 3; ++m) wf[B][m] = wfrag(slot_ + m * P1_WG + ((S) & 1) * 32 * 128 + troff); \
    _Pragma("unroll") for (int t = 0; t < 3; ++t) xf[B][t] = *(const u32x4*)(slot_ + xo[t][(S) & 1]); \
  } while (0)
#pragma unroll
  for (int ii = 0; ii < 7; ++ii) {
    RBAR();
    if (ii == 0) { STAMP(0, 1); }
    P1_LOAD(0, ii * 2);
#pragma unroll
    for (int s2 = 0; s2 < 2; ++s2) {
      const int s = ii * 2 + s2;
      const char* sn = smem + (ii % P1_R) * P1_SLOT;
      SB();
#pragma unroll
      for (int m = 0; m < 3; ++m) {
        if (s2 == 0) {
          wf[1][m] = wfrag(sn + m * P1_WG + 32 * 128 + troff);
          xf[1][m] = *(const u32x4*)(sn + xo[m][1]);
          SB();
        }
        acc[m][0] = mfma(wf[s & 1][m], xf[s & 1][0], acc[m][0]);
        acc[m][1] = mfma(wf[s & 1][m], xf[s & 1][1], acc[m][1]);
        if (hf == 0) acc[m][2] = mfma(wf[s & 1][m], xf[s & 1][2], acc[m][2]);
        else if (m == 0) acc[0][2] = mfma(xf[s & 1][2], xf[s & 1][2], acc[0][2]);
        else if (m == 1) acc[1][2] = mfma(xf[s & 1][1], xf[s & 1][1], acc[1][2]);
        SB();
      }
    }
  }
  STAMP(0, 2);
  if (fb == 3 && g == 2) {
#pragma unroll
    for (int t = 0; t < 3; ++t)
      if (hf == 0 || t < 2) sSum[(hf * 3 + t) * 16 + tl] = acc[1][t][0];
  }
  if (hf == 1 && g == (tl >> 2)) {
    float a0 = acc[0][2][0], a1 = acc[1][2][0];
#pragma unroll
    for (int r = 1; r < 4; ++r) { if ((tl & 3) == r) { a0 = acc[0][2][r]; a1 = acc[1][2][r]; } }
    sSq[fb * 16 + tl] = a0;
    if (fb == 0) sSq[64 + tl] = a1;
  }
  if (hf == 0 && tl == 0) {
#pragma unroll
    for (int m = 0; m < 3; ++m) *(f32x4*)(sCs + m * 64 + fb * 16 + 4 * g) = acc[m][0];
  }
  BAR();
  char* sS = smem;
  {
    f32x4 csf[3];
#pragma unroll
    for (int m = 0; m < 3; ++m) csf[m] = *(const f32x4*)(sCs + m * 64 + fb * 16 + 4 * g);
    const int quad = 4 * fb + g;
#pragma unroll
    for (int t = 0; t < 3; ++t) {
      if (hf == 0 || t < 2) {
        const int tb = hf * 3 + t;
        float mb, rstd;
        ln_params(sSum[tb * 16 + tl], sSq[tb * 16 + tl], mb, rstd);
#pragma unroll
        for (int m = 0; m < 3; ++m) {
          float v0 = (acc[m][t][0] - mb * csf[m][0]) * rstd;
          float v1 = (acc[m][t][1] - mb * csf[m][1]) * rstd;
          float v2 = (acc[m][t][2] - mb * csf[m][2]) * rstd;
          float v3 = (acc[m][t][3] - mb * csf[m][3]) * rstd;
          u32x2 pk = {pack_bf(v0, v1), pack_bf(v2, v3)};
          if (quad < 14) {
            if (m < 2) *(u32x2*)(sS + (m * 14 + quad) * P1_SP + (tb * 16 + tl) * 8) = pk;
            else *(u32x2*)(sS + P1_VO + ((quad >= 7 ? 80 : 0) + tb * 16 + tl) * P1_VP + (quad >= 7 ? quad - 7 : quad) * 8) = pk;
          }
        }
      }
    }
  }
  BAR();
  STAMP(0, 3);

  {
    const int hh = w >> 2, tq = (w & 3) + 1;
    const int qa = hh * 7 + 2 * g;
    const bool up = g < 3;
    const int qb = up ? qa + 1 : qa;
    u32x4 qf, kf[2];
    {
      u32x2 a = *(const u32x2*)(sS + (0 * 14 + qa) * P1_SP + (tq * 16 + tl) * 8);
      u32x2 b = *(const u32x2*)(sS + (0 * 14 + qb) * P1_SP + (tq * 16 + tl) * 8);
      qf = (u32x4){a[0], a[1], up ? b[0] : 0u, up ? b[1] : 0u};
#pragma unroll
      for (int c = 0; c < 2; ++c) {
        u32x2 ka = *(const u32x2*)(sS + (1 * 14 + qa) * P1_SP + ((tq - 1 + c) * 16 + tl) * 8);
        u32x2 kb = *(const u32x2*)(sS + (1 * 14 + qb) * P1_SP + ((tq - 1 + c) * 16 + tl) * 8);
        kf[c] = (u32x4){ka[0], ka[1], up ? kb[0] : 0u, up ? kb[1] : 0u};
      }
    }
    u32x4 vf[2];
    {
      const char* vb = sS + P1_VO + (hh * 80 + (tq - 1) * 16 + 4 * g + ((l & 15) >> 2)) * P1_VP + (l & 3) * 8;
#pragma unroll
      for (int db = 0; db < 2; ++db) {
        u32x2 a = tr_read(vb + db * 32), b = tr_read(vb + 16 * P1_VP + db * 32);
        vf[db] = (u32x4){a[0], a[1], b[0], b[1]};
      }
    }
    const f32x4 z4 = {0.f, 0.f, 0.f, 0.f};
    f32x4 sc[2];
    sc[0] = mfma(kf[0], qf, z4);
    sc[1] = mfma(kf[1], qf, z4);
    const int tok = mt * 64 + (tq - 1) * 16 + tl;
    const float scale = 0.1889822365046136f;
    float lg[8];
    float mx = -3.0e38f;
#pragma unroll
    for (int c = 0; c < 2; ++c)
#pragma unroll
      for (int r = 0; r < 4; ++r) {
        const int delta = tl - (4 * g + r) + 16 * (1 - c);
        const bool inwin = (delta >= 0) && (delta <= 14) && ((delta & 1) == 0);
        float v = rbf(rbf(sc[c][r]) * scale);
        v = (tok - delta >= 0) ? v : -1e9f;
        v = inwin ? v : -3.0e38f;
        lg[c * 4 + r] = v;
        mx = fmaxf(mx, v);
      }
    mx = fmaxf(mx, __shfl_xor(mx, 16));
    mx = fmaxf(mx, __shfl_xor(mx, 32));
    float den = 0.f;
#pragma unroll
    for (int i = 0; i < 8; ++i) { lg[i] = (lg[i] > -2.0e38f) ? __expf(lg[i] - mx) : 0.f; den += lg[i]; }
    den += __shfl_xor(den, 16);
    den += __shfl_xor(den, 32);
    const float inv = 1.0f / den;
    u32x4 pf = {pack_bf(lg[0] * inv, lg[1] * inv), pack_bf(lg[2] * inv, lg[3] * inv),
                pack_bf(lg[4] * inv, lg[5] * inv), pack_bf(lg[6] * inv, lg[7] * inv)};
    const int tbg = tok >> 4;
#pragma unroll
    for (int db = 0; db < 2; ++db) {
      f32x4 o = mfma(vf[db], pf, z4);
      const int qd = db * 4 + g;
      if (qd < 7) {
        int f = hp * 56 + (hh * 7 + qd) * 4;
        int s = f >> 5, ii = (f >> 4) & 1, gg = (f >> 2) & 3;
        u32x2 pk = {pack_bf(o[0], o[1]), pack_bf(o[2], o[3])};
        *(u32x2*)((char*)o_frag + ((size_t)((tbg * 14 + s) * 64 + gg * 16 + tl)) * 16 + ii * 8) = pk;
      }
    }
  }
  STAMP(0, 4);
}

constexpr int EP = 144;
constexpr int G_SLOT = 16384;
constexpr int P3_SLOT = 24576;
constexpr int P3_R = 6;
constexpr int K2_RING = 147456;
constexpr int K2_T = K2_RING;
constexpr int K2_CS = K2_T + 64 * EP;
constexpr int K2_LN = K2_CS + 512;
constexpr int K2_SMEM = K2_LN + 2048;
#define WAITVI(n) asm volatile("s_waitcnt vmcnt(%0)" :: "i"(n) : "memory")

template <int NST, int R, int PW>
__device__ __forceinline__ void g_loader_run(char* smem, const u16* ws0, const u16* ws1, const u16* xs, int lw) {
#define G_ISSUE(ST) do { \
      char* sl_ = smem + ((ST) % R) * G_SLOT; \
      if ((ST) >= PW) { GLDS(ws0 + (size_t)(ST) * 64 * D_, sl_ + lw * 1024); GLDS(ws1 + (size_t)(ST) * 64 * D_, sl_ + (lw + 4) * 1024); } \
      GLDS(xs + (size_t)(ST) * 1024, sl_ + 8192 + lw * 1024); \
      GLDS(xs + (size_t)(ST) * 1024 + 512, sl_ + 8192 + (4 + lw) * 1024); \
    } while (0)
  constexpr int PRE = 4;
#pragma unroll
  for (int st = 0; st < PRE; ++st) G_ISSUE(st);
#pragma unroll
  for (int i = 0; i < NST; ++i) {
    const int lim0 = (i == 0) ? PRE - 1 : ((PRE + 2 * i - 1 < i + R - 2) ? PRE + 2 * i - 1 : i + R - 2);
    const int issued = (lim0 < NST - 1) ? lim0 : NST - 1;
    const int need = (i + 1 < NST) ? i + 1 : NST - 1;
    int allow = 0;
#pragma unroll
    for (int st = need + 1; st <= issued; ++st) allow += (st < PW) ? 2 : 4;
    WAITVI(allow);
    BAR();
    const int lim1 = (PRE + 2 * i + 1 < i + R - 1) ? PRE + 2 * i + 1 : i + R - 1;
#pragma unroll
    for (int st = issued + 1; st <= lim1; ++st) if (st < NST) G_ISSUE(st);
  }
}

template <int NST, int R>
__device__ __forceinline__ void g_consumer_loop(const char* smem, int l, int tb, int fh, f32x4 (&acc)[2]) {
  constexpr int KS = NST * 2;
  const int tro0 = tr_lane_off(l, fh * 2), tro1 = tr_lane_off(l, fh * 2 + 1);
  const int xoff = 8192 + tb * 1024 + l * 16;
  u32x4 wf[2][2], xf[2];
#define G_LOAD(K) do { \
    const char* sl_ = smem + (((K) >> 1) % R) * G_SLOT; \
    wf[(K) & 1][0] = wfrag(sl_ + ((K) & 1) * 4096 + tro0); \
    wf[(K) & 1][1] = wfrag(sl_ + ((K) & 1) * 4096 + tro1); \
    xf[(K) & 1] = *(const u32x4*)(sl_ + ((K) & 1) * 4096 + xoff); \
  } while (0)
#pragma unroll
  for (int i = 0; i < NST; ++i) {
    RBAR();
    if (i == 0) G_LOAD(0);
#pragma unroll
    for (int j = 0; j < 2; ++j) {
      const int k = i * 2 + j;
      if (k + 1 < KS) G_LOAD(k + 1);
      SB();
      acc[0] = mfma(wf[k & 1][0], xf[k & 1], acc[0]);
      acc[1] = mfma(wf[k & 1][1], xf[k & 1], acc[1]);
      SB();
    }
  }
}

__device__ __forceinline__ void group_sync(unsigned* c, int l) {
  if (l == 0) {
    __hip_atomic_fetch_add(c, 1u, __ATOMIC_RELAXED, __HIP_MEMORY_SCOPE_AGENT);
    for (int spin = 0; spin < (1 << 22); ++spin) {
      if (__hip_atomic_load(c, __ATOMIC_RELAXED, __HIP_MEMORY_SCOPE_AGENT) >= 28u) break;
      __builtin_amdgcn_s_sleep(1);
    }
  }
}

__global__ __launch_bounds__(768, 3) void k_fused(const u16* __restrict__ o_frag, const u16* __restrict__ wo,
                                                  const u16* __restrict__ x, const u16* __restrict__ wg,
                                                  const u16* __restrict__ wu, const u16* __restrict__ wd,
                                                  u16* h_frag, float* stats, u16* a_frag, u16* __restrict__ out,
                                                  unsigned* cnt) {
  __shared__ __attribute__((aligned(16))) char smem[K2_SMEM];
  char* sT = smem + K2_T;
  float* sCs = (float*)(smem + K2_CS);
  float2* sLN = (float2*)(smem + K2_LN);
  const int tid = threadIdx.x, l = tid & 63, g = l >> 4, tl = l & 15;
  const int w = __builtin_amdgcn_readfirstlane(tid >> 6);
  const int bid = blockIdx.x, grp = bid & 7, jj = bid >> 3;
  const int mt = grp * 4 + jj / 7, nt = jj % 7;
  const int n3 = jj;
  STAMP(1, 0);

  if (w >= 8) {
    const int lw = w - 8, lr = l >> 3, lc = l & 7;
    int woff[2];
#pragma unroll
    for (int j = 0; j < 2; ++j) {
      const int rho = (lw + 4 * j) * 8 + lr;
      woff[j] = rho * D_ + nt * 64 + (lc ^ (((rho >> 1) & 3) << 1)) * 8;
    }
    g_loader_run<7, 8, 0>(smem, wo + woff[0], wo + woff[1], o_frag + ((size_t)((mt * 4 + lw) * 14) * 64 + l) * 8, lw);
    BAR();
    const int rho3 = lw * 8 + lr;
    const int c3 = lc ^ (((rho3 >> 1) & 3) << 1);
    const u16* gs = wg + (size_t)rho_inv(rho3) * F_ + n3 * 64 + c3 * 8;
    const u16* us = wu + (size_t)rho_inv(rho3) * F_ + n3 * 64 + c3 * 8;
#pragma unroll
    for (int st = 0; st < 5; ++st) {
      GLDS(gs + (size_t)st * 32 * F_, smem + st * P3_SLOT + lw * 1024);
      GLDS(us + (size_t)st * 32 * F_, smem + st * P3_SLOT + 4096 + lw * 1024);
    }
    BAR();
    BAR();
    BAR();
    {
      const u16* xs = h_frag + ((size_t)((grp * 16 + lw * 4) * 14) * 64 + l) * 8;
#define P3_ISSUE(ST) do { \
        char* sl_ = smem + ((ST) % P3_R) * P3_SLOT; \
        if ((ST) >= 5) { GLDS(gs + (size_t)(ST) * 32 * F_, sl_ + lw * 1024); GLDS(us + (size_t)(ST) * 32 * F_, sl_ + 4096 + lw * 1024); } \
        _Pragma("unroll") for (int j = 0; j < 4; ++j) GLDS(xs + (size_t)(j * 14 + (ST)) * 512, sl_ + 8192 + (lw * 4 + j) * 1024); \
      } while (0)
      P3_ISSUE(0); P3_ISSUE(1); P3_ISSUE(2);
#pragma unroll
      for (int i = 0; i < 14; ++i) {
        const int issued = (i == 0) ? 2 : ((i == 1) ? 4 : ((i + P3_R - 2 < 13) ? i + P3_R - 2 : 13));
        const int need = (i + 1 < 14) ? i + 1 : 13;
        int allow = 0;
#pragma unroll
        for (int st = need + 1; st <= issued; ++st) allow += (st < 5) ? 4 : 6;
        WAITVI(allow);
        BAR();
        if (i == 0) { P3_ISSUE(3); P3_ISSUE(4); }
        else if (i == 1) { P3_ISSUE(5); P3_ISSUE(6); }
        else if (i + P3_R - 1 < 14) P3_ISSUE(i + P3_R - 1);
        if (i == 12) {
          float a = 0.f, b = 0.f;
#pragma unroll
          for (int n = 0; n < 7; ++n) {
            const float2 s0 = *(const float2*)(stats + ((size_t)n * S_ + grp * 256 + lw * 64 + l) * 2);
            a += s0.x; b += s0.y;
          }
          float mbv, rsv;
          ln_params(a, b, mbv, rsv);
          sLN[lw * 64 + l] = make_float2(mbv, rsv);
        }
      }
    }
    BAR();
#pragma unroll
    for (int st = 0; st < 8; ++st) {
      GLDS(wd + woff[0] + (size_t)st * 64 * D_, smem + st * G_SLOT + lw * 1024);
      GLDS(wd + woff[1] + (size_t)st * 64 * D_, smem + st * G_SLOT + (lw + 4) * 1024);
    }
    BAR();
    BAR();
    g_loader_run<28, 9, 8>(smem, wd + woff[0], wd + woff[1], a_frag + ((size_t)((mt * 4 + lw) * 56) * 64 + l) * 8, lw);
    BAR();
    return;
  }

  const int tb = w & 3, fh = w >> 2;
  const size_t rowoff = (size_t)(mt * 64 + tb * 16 + fh * 8 + (l >> 3)) * D_ + nt * 64 + (l & 7) * 8;
  char* tw = sT + tb * 16 * EP;
  u32x4 hv;
  {
    const u32x4 rv = *(const u32x4*)(x + rowoff);
    f32x4 acc[2] = {(f32x4){0.f, 0.f, 0.f, 0.f}, (f32x4){0.f, 0.f, 0.f, 0.f}};
    g_consumer_loop<7, 8>(smem, l, tb, fh, acc);
    STAMP(1, 1);
#pragma unroll
    for (int f = 0; f < 2; ++f) {
      u32x2 pk = {pack_bf(acc[f][0], acc[f][1]), pack_bf(acc[f][2], acc[f][3])};
      *(u32x2*)(tw + tl * EP + (fh * 2 + f) * 32 + g * 8) = pk;
    }
    BAR();
    char* p = tw + (fh * 8 + (l >> 3)) * EP + (l & 7) * 16;
    hv = add_bf8(rv, *(const u32x4*)p);
    *(u32x4*)p = hv;
    float sm = 0.f, sq = 0.f;
#pragma unroll
    for (int e = 0; e < 4; ++e) { sm = dot2(hv[e], ONES, sm); sq = dot2(hv[e], hv[e], sq); }
    sm += __shfl_xor(sm, 1); sq += __shfl_xor(sq, 1);
    sm += __shfl_xor(sm, 2); sq += __shfl_xor(sq, 2);
    sm += __shfl_xor(sm, 4); sq += __shfl_xor(sq, 4);
    if ((l & 7) == 0) {
      unsigned long long sv = ((unsigned long long)__float_as_uint(sq) << 32) | __float_as_uint(sm);
      __hip_atomic_store((unsigned long long*)(stats + ((size_t)nt * S_ + mt * 64 + tb * 16 + fh * 8 + (l >> 3)) * 2), sv,
                         __ATOMIC_RELAXED, __HIP_MEMORY_SCOPE_AGENT);
    }
    BAR();
    u32x4 v = *(const u32x4*)(tw + tl * EP + (fh * 4 + g) * 16);
    st16_wt((u32x4*)h_frag + (size_t)((mt * 4 + tb) * 14 + nt * 2 + fh) * 64 + l, v);
    WAITV(0);
    BAR();
    if (w == 0) group_sync(cnt + grp * 32, l);
    BAR();
  }
  STAMP(1, 2);
  {
    const int tg = w >> 1, f3 = w & 1;
    f32x4 acc[4][4];
#pragma unroll
    for (int t = 0; t < 4; ++t)
#pragma unroll
      for (int f = 0; f < 4; ++f) acc[t][f] = (f32x4){0.f, 0.f, 0.f, 0.f};
    const int tro0 = tr_lane_off(l, f3 * 2), tro1 = tr_lane_off(l, f3 * 2 + 1);
    const int csoff = (w >> 2) * 4096 + tr_lane_off(l, w & 3);
    const int xoff = 8192 + tg * 4096 + l * 16;
    f32x4 csum = {0.f, 0.f, 0.f, 0.f};
    const u32x4 onesf = {ONES, ONES, ONES, ONES};
    u32x4 wf[2][4], wcs[2], xf[4];
#pragma unroll
    for (int s = 0; s < 14; ++s) {
      RBAR();
      if (s == 0) {
        STAMP(1, 3);
        wf[0][0] = wfrag(smem + tro0); wf[0][1] = wfrag(smem + tro1);
        wf[0][2] = wfrag(smem + 4096 + tro0); wf[0][3] = wfrag(smem + 4096 + tro1);
        wcs[0] = wfrag(smem + csoff);
#pragma unroll
        for (int t = 0; t < 4; ++t) xf[t] = *(const u32x4*)(smem + xoff + t * 1024);
      }
      int sx_ = ((s + 1) % P3_R) * P3_SLOT; asm volatile("" : "+s"(sx_));
      const char* sn = smem + sx_;
      SB();
#pragma unroll
      for (int t = 0; t < 4; ++t) {
#pragma unroll
        for (int f = 0; f < 4; ++f) acc[t][f] = mfma(wf[s & 1][f], xf[t], acc[t][f]);
        SB();
        if (s + 1 < 14) {
          xf[t] = *(const u32x4*)(sn + xoff + t * 1024);
          if (t == 0) { wf[(s + 1) & 1][0] = wfrag(sn + tro0); wf[(s + 1) & 1][1] = wfrag(sn + tro1); }
          if (t == 1) { wf[(s + 1) & 1][2] = wfrag(sn + 4096 + tro0); wf[(s + 1) & 1][3] = wfrag(sn + 4096 + tro1); }
          if (t == 2) wcs[(s + 1) & 1] = wfrag(sn + csoff);
        }
        SB();
      }
      csum = mfma(wcs[s & 1], onesf, csum);
      SB();
    }
    STAMP(1, 4);
    if (tl == 0) *(f32x4*)(sCs + w * 16 + 4 * g) = csum;
    BAR();
    float mb[4], rstd[4];
#pragma unroll
    for (int t = 0; t < 4; ++t) { float2 v = sLN[(tg * 4 + t) * 16 + tl]; mb[t] = v.x; rstd[t] = v.y; }
    f32x4 cg[2], cu[2];
#pragma unroll
    for (int i = 0; i < 2; ++i) {
      cg[i] = *(const f32x4*)(sCs + (f3 * 2 + i) * 16 + 4 * g);
      cu[i] = *(const f32x4*)(sCs + (4 + f3 * 2 + i) * 16 + 4 * g);
    }
#pragma unroll
    for (int t = 0; t < 4; ++t) {
      u32x4 o4;
#pragma unroll
      for (int i = 0; i < 2; ++i) {
        float av[4];
#pragma unroll
        for (int r = 0; r < 4; ++r) {
          float gv = (acc[t][i][r] - mb[t] * cg[i][r]) * rstd[t];
          float uv = (acc[t][2 + i][r] - mb[t] * cu[i][r]) * rstd[t];
          float sl = gv * __builtin_amdgcn_rcpf(1.0f + __expf(-gv));
          av[r] = sl * uv;
        }
        o4[i * 2] = pack_bf(av[0], av[1]);
        o4[i * 2 + 1] = pack_bf(av[2], av[3]);
      }
      const int tbg = grp * 16 + tg * 4 + t;
      st16_wt((u32x4*)a_frag + (size_t)(tbg * 56 + n3 * 2 + f3) * 64 + l, o4);
    }
    WAITV(0);
    STAMP(1, 5);
    BAR();
    if (w == 0) group_sync(cnt + (8 + grp) * 32, l);
    BAR();
  }
  STAMP(1, 6);
  {
    f32x4 acc[2] = {(f32x4){0.f, 0.f, 0.f, 0.f}, (f32x4){0.f, 0.f, 0.f, 0.f}};
    g_consumer_loop<28, 9>(smem, l, tb, fh, acc);
    STAMP(1, 7);
#pragma unroll
    for (int f = 0; f < 2; ++f) {
      u32x2 pk = {pack_bf(acc[f][0], acc[f][1]), pack_bf(acc[f][2], acc[f][3])};
      *(u32x2*)(tw + tl * EP + (fh * 2 + f) * 32 + g * 8) = pk;
    }
    BAR();
    const char* p = tw + (fh * 8 + (l >> 3)) * EP + (l & 7) * 16;
    *(u32x4*)(out + rowoff) = add_bf8(hv, *(const u32x4*)p);
  }
  STAMP(1, 8);
}

extern "C" void kernel_launch(void* const* d_in, const int* in_sizes, int n_in, void* d_out, int out_size,
                              void* d_ws, size_t ws_size, hipStream_t stream) {
  const u16* x = (const u16*)d_in[0];
  const u16* wqkv = (const u16*)d_in[1];
  const u16* wo = (const u16*)d_in[2];
  const u16* wg = (const u16*)d_in[4];
  const u16* wu = (const u16*)d_in[5];
  const u16* wd = (const u16*)d_in[6];
  u16* out = (u16*)d_out;
  char* ws = (char*)d_ws;
  u16* o_frag = (u16*)ws;
  u16* h_frag = (u16*)(ws + (4u << 20));
  u16* a_frag = (u16*)(ws + (6u << 20));
  float* stats = (float*)(ws + (14u << 20));
  unsigned* cnt = (unsigned*)(ws + (15u << 20));
  k_qkv_attn<<<256, 768, 0, stream>>>(x, wqkv, o_frag, wo, wg, wu, wd, cnt);
  k_fused<<<224, 768, 0, stream>>>(o_frag, wo, x, wg, wu, wd, h_frag, stats, a_frag, out, cnt);
}
```

```cpp
#include <hip/hip_runtime.h>
#include <hip/hip_bf16.h>
#include <stdint.h>

typedef unsigned short u16;
typedef __attribute__((ext_vector_type(8))) __bf16 bf16x8;
typedef __attribute__((ext_vector_type(4))) short s16x4;
typedef __attribute__((ext_vector_type(4))) float f32x4;
typedef __attribute__((ext_vector_type(4))) unsigned int u32x4;
typedef __attribute__((ext_vector_type(2))) unsigned int u32x2;
typedef __attribute__((ext_vector_type(2))) __bf16 bf16x2;
#define LDS_AS __attribute__((address_space(3)))
#define SB() __builtin_amdgcn_sched_barrier(0)
#define GLDS(gp, lp) __builtin_amdgcn_global_load_lds((const unsigned*)(gp), (unsigned*)(lp), 16, 0, 0)
#define WAITV(n) asm volatile("s_waitcnt vmcnt(" #n ")" ::: "memory")
#define RBAR() do { asm volatile("" ::: "memory"); __builtin_amdgcn_s_barrier(); asm volatile("" ::: "memory"); } while (0)
#define BAR() do { asm volatile("s_waitcnt lgkmcnt(0)" ::: "memory"); __builtin_amdgcn_s_barrier(); asm volatile("" ::: "memory"); } while (0)

constexpr int S_ = 2048, D_ = 448, F_ = 1792, QKV_ = 1344;
constexpr unsigned ONES = 0x3F803F80u;

__device__ __attribute__((aligned(16))) const unsigned g_const[12] = {ONES, ONES, ONES, ONES, 0x3F80u, 0u, 0u, 0u, 0u, 0u, 0u, 0u};

__device__ __forceinline__ void st16_wt(void* p, u32x4 v) {
  asm volatile("global_store_dwordx4 %0, %1, off sc1\n\ts_nop 1" :: "v"(p), "v"(v) : "memory");
}
__device__ __forceinline__ float bf_lo(unsigned u) { return __uint_as_float(u << 16); }
__device__ __forceinline__ float bf_hi(unsigned u) { return __uint_as_float(u & 0xffff0000u); }
__device__ __forceinline__ unsigned pack_bf(float a, float b) {
  bf16x2 v = {(__bf16)a, (__bf16)b};
  return __builtin_bit_cast(unsigned, v);
}
__device__ __forceinline__ float rbf(float a) { return (float)(__bf16)a; }
__device__ __forceinline__ float dot2(unsigned a, unsigned b, float c) {
  return __builtin_amdgcn_fdot2_f32_bf16(__builtin_bit_cast(bf16x2, a), __builtin_bit_cast(bf16x2, b), c, false);
}
__device__ __forceinline__ u32x2 tr_read(const char* p) {
  s16x4 v = __builtin_amdgcn_ds_read_tr16_b64_v4i16((LDS_AS s16x4*)p);
  return __builtin_bit_cast(u32x2, v);
}
__device__ __forceinline__ u32x4 wfrag(const char* p) {
  u32x2 a = tr_read(p), b = tr_read(p + 16 * 128);
  u32x4 r = {a[0], a[1], b[0], b[1]};
  return r;
}
__device__ __forceinline__ f32x4 mfma(u32x4 a, u32x4 b, f32x4 c) {
  return __builtin_amdgcn_mfma_f32_16x16x32_bf16(__builtin_bit_cast(bf16x8, a), __builtin_bit_cast(bf16x8, b), c, 0, 0, 0);
}
__device__ __forceinline__ int rho_inv(int r) { return (r & ~31) | ((r & 12) << 1) | ((r & 16) >> 2) | (r & 3); }
__device__ __forceinline__ int tr_lane_off(int l, int nb) {
  int g = l >> 4, q = (l & 15) >> 2, p = l & 3;
  int swz = 2 * (g & 1) + (q >> 1);
  return (4 * g + q) * 128 + ((nb ^ swz) << 5) + p * 8;
}
__device__ __forceinline__ void ln_params(float sum, float sq, float& mb, float& rstd) {
  float mean = sum * (1.0f / D_);
  float var = fmaxf(sq * (1.0f / D_) - mean * mean, 0.0f);
  mb = rbf(mean);
  float den = rbf(sqrtf(rbf(rbf(var) + 1e-5f)));
  rstd = 1.0f / den;
}
__device__ __forceinline__ u32x4 add_bf8(u32x4 a, u32x4 b) {
  u32x4 r;
#pragma unroll
  for (int e = 0; e < 4; ++e) r[e] = pack_bf(bf_lo(a[e]) + bf_lo(b[e]), bf_hi(a[e]) + bf_hi(b[e]));
  return r;
}

constexpr int P1_WG = 64 * 128;
constexpr int P1_XO = 3 * P1_WG;
constexpr int P1_SLOT = 3 * P1_WG + 80 * 128;
constexpr int P1_R = 4;
constexpr int P1_SP = 672;
constexpr int P1_VO = 28 * P1_SP;
constexpr int P1_VP = 72;
constexpr int P1_SMEM = P1_R * P1_SLOT + 2048 + (80 * 2 + 3 * 64) * 4;

__global__ __launch_bounds__(768, 3) void k_qkv_attn(const u16* __restrict__ x, const u16* __restrict__ wqkv,
                                                     u16* __restrict__ o_frag, const u16* __restrict__ wo,
                                                     const u16* __restrict__ wg, const u16* __restrict__ wu,
                                                     const u16* __restrict__ wd, unsigned* __restrict__ cnt) {
  __shared__ __attribute__((aligned(16))) char smem[P1_SMEM];
  float* sSum = (float*)(smem + P1_R * P1_SLOT + 2048);
  float* sSq = sSum + 80;
  float* sCs = sSq + 80;

  const int tid = threadIdx.x, l = tid & 63, g = l >> 4, tl = l & 15;
  const int w = __builtin_amdgcn_readfirstlane(tid >> 6);
  const int bid = blockIdx.x, xcd = bid & 7, jj = bid >> 3;
  const int mt = xcd * 4 + (jj >> 3), hp = jj & 7;
  const int row0 = mt * 64 - 16;
  if (bid == 0 && tid < 16) cnt[tid * 32] = 0u;

  if (w >= 8) {
    const int lw = w - 8, lr = l >> 3, lc = l & 7;
    const u16* src[9];
    int dst[9], adv[9];
#pragma unroll
    for (int j = 0; j < 9; ++j) {
      const int pi = lw + 4 * j;
      if (pi < 24) {
        const int m = pi >> 3, rho = (pi & 7) * 8 + lr;
        const int c = lc ^ (((rho >> 1) & 3) << 1);
        if (c < 7) { src[j] = wqkv + (size_t)rho_inv(rho) * QKV_ + m * D_ + hp * 56 + c * 8; adv[j] = 64 * QKV_; }
        else { src[j] = (const u16*)(g_const + (m == 1 ? 4 : 8)); adv[j] = 0; }
        dst[j] = pi * 1024;
      } else if (pi < 34) {
        const int r = (pi - 24) * 8 + lr;
        const int c = lc ^ ((r >> 1) & 7);
        if (r > 0) { src[j] = x + (size_t)max(row0 + r, 0) * D_ + c * 8; adv[j] = 64; }
        else { src[j] = (const u16*)g_const; adv[j] = 0; }
        dst[j] = pi * 1024;
      } else { src[j] = (const u16*)g_const; adv[j] = 0; dst[j] = -1; }
    }
#define P1_ISSUE(ST) do { \
      char* sl_ = smem + ((ST) % P1_R) * P1_SLOT; \
      _Pragma("unroll") for (int j = 0; j < 9; ++j) { \
        GLDS(src[j] + (size_t)(ST) * adv[j], (dst[j] >= 0) ? sl_ + dst[j] : smem + P1_R * P1_SLOT + (lw & 1) * 1024); } \
    } while (0)
    P1_ISSUE(0);
    {
      char* dummy = smem + P1_R * P1_SLOT + (lw & 1) * 1024;
      const int pc = (bid * 4 + lw);
      GLDS(wqkv + ((size_t)pc * 64 + l) * 8, dummy);
      GLDS(wqkv + ((size_t)(pc < 152 ? pc + 1024 : pc) * 64 + l) * 8, dummy);
      GLDS(x + ((size_t)pc * 64 + l) * 8, dummy);
      GLDS(x + ((size_t)(pc < 768 ? pc + 1024 : pc) * 64 + l) * 8, dummy);
    }
    P1_ISSUE(1); P1_ISSUE(2);
    WAITV(22); BAR();
    P1_ISSUE(3);
    WAITV(18); BAR();
    P1_ISSUE(4);
    WAITV(18); BAR();
    P1_ISSUE(5);
    WAITV(18); BAR();
    P1_ISSUE(6);
    WAITV(18); BAR();
    WAITV(9);  BAR();
    WAITV(0);  BAR();
    {
      char* dummy = smem + P1_R * P1_SLOT + (lw & 1) * 1024;
      const int pc = (bid * 4 + lw);
      const size_t e0 = ((size_t)pc * 64 + l) * 8;
      if (pc < 392) GLDS(wo + e0, dummy);
      GLDS(wg + e0, dummy); GLDS(wu + e0, dummy);
      if (pc < 544) { GLDS(wg + e0 + 1024 * 512, dummy); GLDS(wu + e0 + 1024 * 512, dummy); }
      GLDS(wd + e0, dummy);
      if (pc < 544) GLDS(wd + e0 + 1024 * 512, dummy);
    }
    BAR(); BAR();
    WAITV(0);
    return;
  }

  const int fb = w & 3, hf = w >> 2;
  int xo[3][2];
#pragma unroll
  for (int t = 0; t < 3; ++t) {
    const int tb = (t < 2) ? hf * 3 + t : (hf ? fb : 2);
    const int r = tb * 16 + tl;
#pragma unroll
    for (int s2 = 0; s2 < 2; ++s2) xo[t][s2] = P1_XO + r * 128 + (((4 * s2 + g) ^ ((r >> 1) & 7)) << 4);
  }
  f32x4 acc[3][3];
#pragma unroll
  for (int m = 0; m < 3; ++m)
#pragma unroll
    for (int t = 0; t < 3; ++t) acc[m][t] = (f32x4){0.f, 0.f, 0.f, 0.f};
  const int troff = tr_lane_off(l, fb);

  u32x4 xf[2][3], wf[2][3];
#define P1_LOAD(B, S) do { \
    const char* slot_ = smem + (((S) >> 1) % P1_R) * P1_SLOT; \
    _Pragma("unroll") for (int m = 0; m < 3; ++m) wf[B][m] = wfrag(slot_ + m * P1_WG + ((S) & 1) * 32 * 128 + troff); \
    _Pragma("unroll") for (int t = 0; t < 3; ++t) xf[B][t] = *(const u32x4*)(slot_ + xo[t][(S) & 1]); \
  } while (0)
#pragma unroll
  for (int ii = 0; ii < 7; ++ii) {
    RBAR();
        P1_LOAD(0, ii * 2);
#pragma unroll
    for (int s2 = 0; s2 < 2; ++s2) {
      const int s = ii * 2 + s2;
      const char* sn = smem + (ii % P1_R) * P1_SLOT;
      SB();
#pragma unroll
      for (int m = 0; m < 3; ++m) {
        if (s2 == 0) {
          wf[1][m] = wfrag(sn + m * P1_WG + 32 * 128 + troff);
          xf[1][m] = *(const u32x4*)(sn + xo[m][1]);
          SB();
        }
        acc[m][0] = mfma(wf[s & 1][m], xf[s & 1][0], acc[m][0]);
        acc[m][1] = mfma(wf[s & 1][m], xf[s & 1][1], acc[m][1]);
        if (hf == 0) acc[m][2] = mfma(wf[s & 1][m], xf[s & 1][2], acc[m][2]);
        else if (m == 0) acc[0][2] = mfma(xf[s & 1][2], xf[s & 1][2], acc[0][2]);
        else if (m == 1) acc[1][2] = mfma(xf[s & 1][1], xf[s & 1][1], acc[1][2]);
        SB();
      }
    }
  }
  if (fb == 3 && g == 2) {
#pragma unroll
    for (int t = 0; t < 3; ++t)
      if (hf == 0 || t < 2) sSum[(hf * 3 + t) * 16 + tl] = acc[1][t][0];
  }
  if (hf == 1 && g == (tl >> 2)) {
    float a0 = acc[0][2][0], a1 = acc[1][2][0];
#pragma unroll
    for (int r = 1; r < 4; ++r) { if ((tl & 3) == r) { a0 = acc[0][2][r]; a1 = acc[1][2][r]; } }
    sSq[fb * 16 + tl] = a0;
    if (fb == 0) sSq[64 + tl] = a1;
  }
  if (hf == 0 && tl == 0) {
#pragma unroll
    for (int m = 0; m < 3; ++m) *(f32x4*)(sCs + m * 64 + fb * 16 + 4 * g) = acc[m][0];
  }
  BAR();
  char* sS = smem;
  {
    f32x4 csf[3];
#pragma unroll
    for (int m = 0; m < 3; ++m) csf[m] = *(const f32x4*)(sCs + m * 64 + fb * 16 + 4 * g);
    const int quad = 4 * fb + g;
#pragma unroll
    for (int t = 0; t < 3; ++t) {
      if (hf == 0 || t < 2) {
        const int tb = hf * 3 + t;
        float mb, rstd;
        ln_params(sSum[tb * 16 + tl], sSq[tb * 16 + tl], mb, rstd);
#pragma unroll
        for (int m = 0; m < 3; ++m) {
          float v0 = (acc[m][t][0] - mb * csf[m][0]) * rstd;
          float v1 = (acc[m][t][1] - mb * csf[m][1]) * rstd;
          float v2 = (acc[m][t][2] - mb * csf[m][2]) * rstd;
          float v3 = (acc[m][t][3] - mb * csf[m][3]) * rstd;
          u32x2 pk = {pack_bf(v0, v1), pack_bf(v2, v3)};
          if (quad < 14) {
            if (m < 2) *(u32x2*)(sS + (m * 14 + quad) * P1_SP + (tb * 16 + tl) * 8) = pk;
            else *(u32x2*)(sS + P1_VO + ((quad >= 7 ? 80 : 0) + tb * 16 + tl) * P1_VP + (quad >= 7 ? quad - 7 : quad) * 8) = pk;
          }
        }
      }
    }
  }
  BAR();

  {
    const int hh = w >> 2, tq = (w & 3) + 1;
    const int qa = hh * 7 + 2 * g;
    const bool up = g < 3;
    const int qb = up ? qa + 1 : qa;
    u32x4 qf, kf[2];
    {
      u32x2 a = *(const u32x2*)(sS + (0 * 14 + qa) * P1_SP + (tq * 16 + tl) * 8);
      u32x2 b = *(const u32x2*)(sS + (0 * 14 + qb) * P1_SP + (tq * 16 + tl) * 8);
      qf = (u32x4){a[0], a[1], up ? b[0] : 0u, up ? b[1] : 0u};
#pragma unroll
      for (int c = 0; c < 2; ++c) {
        u32x2 ka = *(const u32x2*)(sS + (1 * 14 + qa) * P1_SP + ((tq - 1 + c) * 16 + tl) * 8);
        u32x2 kb = *(const u32x2*)(sS + (1 * 14 + qb) * P1_SP + ((tq - 1 + c) * 16 + tl) * 8);
        kf[c] = (u32x4){ka[0], ka[1], up ? kb[0] : 0u, up ? kb[1] : 0u};
      }
    }
    u32x4 vf[2];
    {
      const char* vb = sS + P1_VO + (hh * 80 + (tq - 1) * 16 + 4 * g + ((l & 15) >> 2)) * P1_VP + (l & 3) * 8;
#pragma unroll
      for (int db = 0; db < 2; ++db) {
        u32x2 a = tr_read(vb + db * 32), b = tr_read(vb + 16 * P1_VP + db * 32);
        vf[db] = (u32x4){a[0], a[1], b[0], b[1]};
      }
    }
    const f32x4 z4 = {0.f, 0.f, 0.f, 0.f};
    f32x4 sc[2];
    sc[0] = mfma(kf[0], qf, z4);
    sc[1] = mfma(kf[1], qf, z4);
    const int tok = mt * 64 + (tq - 1) * 16 + tl;
    const float scale = 0.1889822365046136f;
    float lg[8];
    float mx = -3.0e38f;
#pragma unroll
    for (int c = 0; c < 2; ++c)
#pragma unroll
      for (int r = 0; r < 4; ++r) {
        const int delta = tl - (4 * g + r) + 16 * (1 - c);
        const bool inwin = (delta >= 0) && (delta <= 14) && ((delta & 1) == 0);
        float v = rbf(rbf(sc[c][r]) * scale);
        v = (tok - delta >= 0) ? v : -1e9f;
        v = inwin ? v : -3.0e38f;
        lg[c * 4 + r] = v;
        mx = fmaxf(mx, v);
      }
    mx = fmaxf(mx, __shfl_xor(mx, 16));
    mx = fmaxf(mx, __shfl_xor(mx, 32));
    float den = 0.f;
#pragma unroll
    for (int i = 0; i < 8; ++i) { lg[i] = (lg[i] > -2.0e38f) ? __expf(lg[i] - mx) : 0.f; den += lg[i]; }
    den += __shfl_xor(den, 16);
    den += __shfl_xor(den, 32);
    const float inv = 1.0f / den;
    u32x4 pf = {pack_bf(lg[0] * inv, lg[1] * inv), pack_bf(lg[2] * inv, lg[3] * inv),
                pack_bf(lg[4] * inv, lg[5] * inv), pack_bf(lg[6] * inv, lg[7] * inv)};
    const int tbg = tok >> 4;
#pragma unroll
    for (int db = 0; db < 2; ++db) {
      f32x4 o = mfma(vf[db], pf, z4);
      const int qd = db * 4 + g;
      if (qd < 7) {
        int f = hp * 56 + (hh * 7 + qd) * 4;
        int s = f >> 5, ii = (f >> 4) & 1, gg = (f >> 2) & 3;
        u32x2 pk = {pack_bf(o[0], o[1]), pack_bf(o[2], o[3])};
        *(u32x2*)((char*)o_frag + ((size_t)((tbg * 14 + s) * 64 + gg * 16 + tl)) * 16 + ii * 8) = pk;
      }
    }
  }
}

constexpr int EP = 144;
constexpr int G_SLOT = 16384;
constexpr int P3_SLOT = 24576;
constexpr int P3_R = 6;
constexpr int K2_RING = 147456;
constexpr int K2_T = K2_RING;
constexpr int K2_CS = K2_T + 64 * EP;
constexpr int K2_LN = K2_CS + 512;
constexpr int K2_SMEM = K2_LN + 2048;
#define WAITVI(n) asm volatile("s_waitcnt vmcnt(%0)" :: "i"(n) : "memory")

template <int NST, int R, int PW>
__device__ __forceinline__ void g_loader_run(char* smem, const u16* ws0, const u16* ws1, const u16* xs, int lw) {
#define G_ISSUE(ST) do { \
      char* sl_ = smem + ((ST) % R) * G_SLOT; \
      if ((ST) >= PW) { GLDS(ws0 + (size_t)(ST) * 64 * D_, sl_ + lw * 1024); GLDS(ws1 + (size_t)(ST) * 64 * D_, sl_ + (lw + 4) * 1024); } \
      GLDS(xs + (size_t)(ST) * 1024, sl_ + 8192 + lw * 1024); \
      GLDS(xs + (size_t)(ST) * 1024 + 512, sl_ + 8192 + (4 + lw) * 1024); \
    } while (0)
  constexpr int PRE = 4;
#pragma unroll
  for (int st = 0; st < PRE; ++st) G_ISSUE(st);
#pragma unroll
  for (int i = 0; i < NST; ++i) {
    const int lim0 = (i == 0) ? PRE - 1 : ((PRE + 2 * i - 1 < i + R - 2) ? PRE + 2 * i - 1 : i + R - 2);
    const int issued = (lim0 < NST - 1) ? lim0 : NST - 1;
    const int need = (i + 1 < NST) ? i + 1 : NST - 1;
    int allow = 0;
#pragma unroll
    for (int st = need + 1; st <= issued; ++st) allow += (st < PW) ? 2 : 4;
    WAITVI(allow);
    BAR();
    const int lim1 = (PRE + 2 * i + 1 < i + R - 1) ? PRE + 2 * i + 1 : i + R - 1;
#pragma unroll
    for (int st = issued + 1; st <= lim1; ++st) if (st < NST) G_ISSUE(st);
  }
}

template <int NST, int R>
__device__ __forceinline__ void g_consumer_loop(const char* smem, int l, int tb, int fh, f32x4 (&acc)[2]) {
  constexpr int KS = NST * 2;
  const int tro0 = tr_lane_off(l, fh * 2), tro1 = tr_lane_off(l, fh * 2 + 1);
  const int xoff = 8192 + tb * 1024 + l * 16;
  u32x4 wf[2][2], xf[2];
#define G_LOAD(K) do { \
    const char* sl_ = smem + (((K) >> 1) % R) * G_SLOT; \
    wf[(K) & 1][0] = wfrag(sl_ + ((K) & 1) * 4096 + tro0); \
    wf[(K) & 1][1] = wfrag(sl_ + ((K) & 1) * 4096 + tro1); \
    xf[(K) & 1] = *(const u32x4*)(sl_ + ((K) & 1) * 4096 + xoff); \
  } while (0)
#pragma unroll
  for (int i = 0; i < NST; ++i) {
    RBAR();
    if (i == 0) G_LOAD(0);
#pragma unroll
    for (int j = 0; j < 2; ++j) {
      const int k = i * 2 + j;
      if (k + 1 < KS) G_LOAD(k + 1);
      SB();
      acc[0] = mfma(wf[k & 1][0], xf[k & 1], acc[0]);
      acc[1] = mfma(wf[k & 1][1], xf[k & 1], acc[1]);
      SB();
    }
  }
}

__device__ __forceinline__ void group_sync(unsigned* c, int l) {
  if (l == 0) {
    __hip_atomic_fetch_add(c, 1u, __ATOMIC_RELAXED, __HIP_MEMORY_SCOPE_AGENT);
    for (int spin = 0; spin < (1 << 22); ++spin) {
      if (__hip_atomic_load(c, __ATOMIC_RELAXED, __HIP_MEMORY_SCOPE_AGENT) >= 28u) break;
      __builtin_amdgcn_s_sleep(1);
    }
  }
}

__global__ __launch_bounds__(768, 3) void k_fused(const u16* __restrict__ o_frag, const u16* __restrict__ wo,
                                                  const u16* __restrict__ x, const u16* __restrict__ wg,
                                                  const u16* __restrict__ wu, const u16* __restrict__ wd,
                                                  u16* h_frag, float* stats, u16* a_frag, u16* __restrict__ out,
                                                  unsigned* cnt) {
  __shared__ __attribute__((aligned(16))) char smem[K2_SMEM];
  char* sT = smem + K2_T;
  float* sCs = (float*)(smem + K2_CS);
  float2* sLN = (float2*)(smem + K2_LN);
  const int tid = threadIdx.x, l = tid & 63, g = l >> 4, tl = l & 15;
  const int w = __builtin_amdgcn_readfirstlane(tid >> 6);
  const int bid = blockIdx.x, grp = bid & 7, jj = bid >> 3;
  const int mt = grp * 4 + jj / 7, nt = jj % 7;
  const int n3 = jj;

  if (w >= 8) {
    const int lw = w - 8, lr = l >> 3, lc = l & 7;
    int woff[2];
#pragma unroll
    for (int j = 0; j < 2; ++j) {
      const int rho = (lw + 4 * j) * 8 + lr;
      woff[j] = rho * D_ + nt * 64 + (lc ^ (((rho >> 1) & 3) << 1)) * 8;
    }
    g_loader_run<7, 8, 0>(smem, wo + woff[0], wo + woff[1], o_frag + ((size_t)((mt * 4 + lw) * 14) * 64 + l) * 8, lw);
    BAR();
    const int rho3 = lw * 8 + lr;
    const int c3 = lc ^ (((rho3 >> 1) & 3) << 1);
    const u16* gs = wg + (size_t)rho_inv(rho3) * F_ + n3 * 64 + c3 * 8;
    const u16* us = wu + (size_t)rho_inv(rho3) * F_ + n3 * 64 + c3 * 8;
#pragma unroll
    for (int st = 0; st < 5; ++st) {
      GLDS(gs + (size_t)st * 32 * F_, smem + st * P3_SLOT + lw * 1024);
      GLDS(us + (size_t)st * 32 * F_, smem + st * P3_SLOT + 4096 + lw * 1024);
    }
    BAR();
    BAR();
    BAR();
    {
      const u16* xs = h_frag + ((size_t)((grp * 16 + lw * 4) * 14) * 64 + l) * 8;
#define P3_ISSUE(ST) do { \
        char* sl_ = smem + ((ST) % P3_R) * P3_SLOT; \
        if ((ST) >= 5) { GLDS(gs + (size_t)(ST) * 32 * F_, sl_ + lw * 1024); GLDS(us + (size_t)(ST) * 32 * F_, sl_ + 4096 + lw * 1024); } \
        _Pragma("unroll") for (int j = 0; j < 4; ++j) GLDS(xs + (size_t)(j * 14 + (ST)) * 512, sl_ + 8192 + (lw * 4 + j) * 1024); \
      } while (0)
      P3_ISSUE(0); P3_ISSUE(1); P3_ISSUE(2);
#pragma unroll
      for (int i = 0; i < 14; ++i) {
        const int issued = (i == 0) ? 2 : ((i == 1) ? 4 : ((i + P3_R - 2 < 13) ? i + P3_R - 2 : 13));
        const int need = (i + 1 < 14) ? i + 1 : 13;
        int allow = 0;
#pragma unroll
        for (int st = need + 1; st <= issued; ++st) allow += (st < 5) ? 4 : 6;
        WAITVI(allow);
        BAR();
        if (i == 0) { P3_ISSUE(3); P3_ISSUE(4); }
        else if (i == 1) { P3_ISSUE(5); P3_ISSUE(6); }
        else if (i + P3_R - 1 < 14) P3_ISSUE(i + P3_R - 1);
        if (i == 12) {
          float a = 0.f, b = 0.f;
#pragma unroll
          for (int n = 0; n < 7; ++n) {
            const float2 s0 = *(const float2*)(stats + ((size_t)n * S_ + grp * 256 + lw * 64 + l) * 2);
            a += s0.x; b += s0.y;
          }
          float mbv, rsv;
          ln_params(a, b, mbv, rsv);
          sLN[lw * 64 + l] = make_float2(mbv, rsv);
        }
      }
    }
    BAR();
#pragma unroll
    for (int st = 0; st < 8; ++st) {
      GLDS(wd + woff[0] + (size_t)st * 64 * D_, smem + st * G_SLOT + lw * 1024);
      GLDS(wd + woff[1] + (size_t)st * 64 * D_, smem + st * G_SLOT + (lw + 4) * 1024);
    }
    BAR();
    BAR();
    g_loader_run<28, 9, 8>(smem, wd + woff[0], wd + woff[1], a_frag + ((size_t)((mt * 4 + lw) * 56) * 64 + l) * 8, lw);
    BAR();
    return;
  }

  const int tb = w & 3, fh = w >> 2;
  const size_t rowoff = (size_t)(mt * 64 + tb * 16 + fh * 8 + (l >> 3)) * D_ + nt * 64 + (l & 7) * 8;
  char* tw = sT + tb * 16 * EP;
  u32x4 hv;
  {
    const u32x4 rv = *(const u32x4*)(x + rowoff);
    f32x4 acc[2] = {(f32x4){0.f, 0.f, 0.f, 0.f}, (f32x4){0.f, 0.f, 0.f, 0.f}};
    g_consumer_loop<7, 8>(smem, l, tb, fh, acc);
#pragma unroll
    for (int f = 0; f < 2; ++f) {
      u32x2 pk = {pack_bf(acc[f][0], acc[f][1]), pack_bf(acc[f][2], acc[f][3])};
      *(u32x2*)(tw + tl * EP + (fh * 2 + f) * 32 + g * 8) = pk;
    }
    BAR();
    char* p = tw + (fh * 8 + (l >> 3)) * EP + (l & 7) * 16;
    hv = add_bf8(rv, *(const u32x4*)p);
    *(u32x4*)p = hv;
    float sm = 0.f, sq = 0.f;
#pragma unroll
    for (int e = 0; e < 4; ++e) { sm = dot2(hv[e], ONES, sm); sq = dot2(hv[e], hv[e], sq); }
    sm += __shfl_xor(sm, 1); sq += __shfl_xor(sq, 1);
    sm += __shfl_xor(sm, 2); sq += __shfl_xor(sq, 2);
    sm += __shfl_xor(sm, 4); sq += __shfl_xor(sq, 4);
    if ((l & 7) == 0) {
      unsigned long long sv = ((unsigned long long)__float_as_uint(sq) << 32) | __float_as_uint(sm);
      __hip_atomic_store((unsigned long long*)(stats + ((size_t)nt * S_ + mt * 64 + tb * 16 + fh * 8 + (l >> 3)) * 2), sv,
                         __ATOMIC_RELAXED, __HIP_MEMORY_SCOPE_AGENT);
    }
    BAR();
    u32x4 v = *(const u32x4*)(tw + tl * EP + (fh * 4 + g) * 16);
    st16_wt((u32x4*)h_frag + (size_t)((mt * 4 + tb) * 14 + nt * 2 + fh) * 64 + l, v);
    WAITV(0);
    BAR();
    if (w == 0) group_sync(cnt + grp * 32, l);
    BAR();
  }
  {
    const int tg = w >> 1, f3 = w & 1;
    f32x4 acc[4][4];
#pragma unroll
    for (int t = 0; t < 4; ++t)
#pragma unroll
      for (int f = 0; f < 4; ++f) acc[t][f] = (f32x4){0.f, 0.f, 0.f, 0.f};
    const int tro0 = tr_lane_off(l, f3 * 2), tro1 = tr_lane_off(l, f3 * 2 + 1);
    const int csoff = (w >> 2) * 4096 + tr_lane_off(l, w & 3);
    const int xoff = 8192 + tg * 4096 + l * 16;
    f32x4 csum = {0.f, 0.f, 0.f, 0.f};
    const u32x4 onesf = {ONES, ONES, ONES, ONES};
    u32x4 wf[2][4], wcs[2], xf[4];
#pragma unroll
    for (int s = 0; s < 14; ++s) {
      RBAR();
      if (s == 0) {
        wf[0][0] = wfrag(smem + tro0); wf[0][1] = wfrag(smem + tro1);
        wf[0][2] = wfrag(smem + 4096 + tro0); wf[0][3] = wfrag(smem + 4096 + tro1);
        wcs[0] = wfrag(smem + csoff);
#pragma unroll
        for (int t = 0; t < 4; ++t) xf[t] = *(const u32x4*)(smem + xoff + t * 1024);
      }
      int sx_ = ((s + 1) % P3_R) * P3_SLOT; asm volatile("" : "+s"(sx_));
      const char* sn = smem + sx_;
      SB();
#pragma unroll
      for (int t = 0; t < 4; ++t) {
#pragma unroll
        for (int f = 0; f < 4; ++f) acc[t][f] = mfma(wf[s & 1][f], xf[t], acc[t][f]);
        SB();
        if (s + 1 < 14) {
          xf[t] = *(const u32x4*)(sn + xoff + t * 1024);
          if (t == 0) { wf[(s + 1) & 1][0] = wfrag(sn + tro0); wf[(s + 1) & 1][1] = wfrag(sn + tro1); }
          if (t == 1) { wf[(s + 1) & 1][2] = wfrag(sn + 4096 + tro0); wf[(s + 1) & 1][3] = wfrag(sn + 4096 + tro1); }
          if (t == 2) wcs[(s + 1) & 1] = wfrag(sn + csoff);
        }
        SB();
      }
      csum = mfma(wcs[s & 1], onesf, csum);
      SB();
    }
    if (tl == 0) *(f32x4*)(sCs + w * 16 + 4 * g) = csum;
    BAR();
    float mb[4], rstd[4];
#pragma unroll
    for (int t = 0; t < 4; ++t) { float2 v = sLN[(tg * 4 + t) * 16 + tl]; mb[t] = v.x; rstd[t] = v.y; }
    f32x4 cg[2], cu[2];
#pragma unroll
    for (int i = 0; i < 2; ++i) {
      cg[i] = *(const f32x4*)(sCs + (f3 * 2 + i) * 16 + 4 * g);
      cu[i] = *(const f32x4*)(sCs + (4 + f3 * 2 + i) * 16 + 4 * g);
    }
#pragma unroll
    for (int t = 0; t < 4; ++t) {
      u32x4 o4;
#pragma unroll
      for (int i = 0; i < 2; ++i) {
        float av[4];
#pragma unroll
        for (int r = 0; r < 4; ++r) {
          float gv = (acc[t][i][r] - mb[t] * cg[i][r]) * rstd[t];
          float uv = (acc[t][2 + i][r] - mb[t] * cu[i][r]) * rstd[t];
          float sl = gv * __builtin_amdgcn_rcpf(1.0f + __expf(-gv));
          av[r] = sl * uv;
        }
        o4[i * 2] = pack_bf(av[0], av[1]);
        o4[i * 2 + 1] = pack_bf(av[2], av[3]);
      }
      const int tbg = grp * 16 + tg * 4 + t;
      st16_wt((u32x4*)a_frag + (size_t)(tbg * 56 + n3 * 2 + f3) * 64 + l, o4);
    }
    WAITV(0);
    BAR();
    if (w == 0) group_sync(cnt + (8 + grp) * 32, l);
    BAR();
  }
  {
    f32x4 acc[2] = {(f32x4){0.f, 0.f, 0.f, 0.f}, (f32x4){0.f, 0.f, 0.f, 0.f}};
    g_consumer_loop<28, 9>(smem, l, tb, fh, acc);
#pragma unroll
    for (int f = 0; f < 2; ++f) {
      u32x2 pk = {pack_bf(acc[f][0], acc[f][1]), pack_bf(acc[f][2], acc[f][3])};
      *(u32x2*)(tw + tl * EP + (fh * 2 + f) * 32 + g * 8) = pk;
    }
    BAR();
    const char* p = tw + (fh * 8 + (l >> 3)) * EP + (l & 7) * 16;
    *(u32x4*)(out + rowoff) = add_bf8(hv, *(const u32x4*)p);
  }
}

extern "C" void kernel_launch(void* const* d_in, const int* in_sizes, int n_in, void* d_out, int out_size,
                              void* d_ws, size_t ws_size, hipStream_t stream) {
  const u16* x = (const u16*)d_in[0];
  const u16* wqkv = (const u16*)d_in[1];
  const u16* wo = (const u16*)d_in[2];
  const u16* wg = (const u16*)d_in[4];
  const u16* wu = (const u16*)d_in[5];
  const u16* wd = (const u16*)d_in[6];
  u16* out = (u16*)d_out;
  char* ws = (char*)d_ws;
  u16* o_frag = (u16*)ws;
  u16* h_frag = (u16*)(ws + (4u << 20));
  u16* a_frag = (u16*)(ws + (6u << 20));
  float* stats = (float*)(ws + (14u << 20));
  unsigned* cnt = (unsigned*)(ws + (15u << 20));
  k_qkv_attn<<<256, 768, 0, stream>>>(x, wqkv, o_frag, wo, wg, wu, wd, cnt);
  k_fused<<<224, 768, 0, stream>>>(o_frag, wo, x, wg, wu, wd, h_frag, stats, a_frag, out, cnt);
}
```

```cpp
#include <hip/hip_runtime.h>
#include <hip/hip_bf16.h>
#include <stdint.h>

typedef unsigned short u16;
typedef __attribute__((ext_vector_type(8))) __bf16 bf16x8;
typedef __attribute__((ext_vector_type(4))) short s16x4;
typedef __attribute__((ext_vector_type(4))) float f32x4;
typedef __attribute__((ext_vector_type(4))) unsigned int u32x4;
typedef __attribute__((ext_vector_type(2))) unsigned int u32x2;
typedef __attribute__((ext_vector_type(2))) __bf16 bf16x2;
#define LDS_AS __attribute__((address_space(3)))
#define SB() __builtin_amdgcn_sched_barrier(0)
#define GLDS(gp, lp) __builtin_amdgcn_global_load_lds((const unsigned*)(gp), (unsigned*)(lp), 16, 0, 0)
#define WAITV(n) asm volatile("s_waitcnt vmcnt(" #n ")" ::: "memory")
#define RBAR() do { asm volatile("" ::: "memory"); __builtin_amdgcn_s_barrier(); asm volatile("" ::: "memory"); } while (0)
#define BAR() do { asm volatile("s_waitcnt lgkmcnt(0)" ::: "memory"); __builtin_amdgcn_s_barrier(); asm volatile("" ::: "memory"); } while (0)

constexpr int S_ = 2048, D_ = 448, F_ = 1792, QKV_ = 1344;
constexpr unsigned ONES = 0x3F803F80u;

__device__ __attribute__((aligned(16))) const unsigned g_const[12] = {ONES, ONES, ONES, ONES, 0x3F80u, 0u, 0u, 0u, 0u, 0u, 0u, 0u};

__device__ __forceinline__ void st16_wt(void* p, u32x4 v) {
  asm volatile("global_store_dwordx4 %0, %1, off sc1\n\ts_nop 1" :: "v"(p), "v"(v) : "memory");
}
__device__ __forceinline__ float bf_lo(unsigned u) { return __uint_as_float(u << 16); }
__device__ __forceinline__ float bf_hi(unsigned u) { return __uint_as_float(u & 0xffff0000u); }
__device__ __forceinline__ unsigned pack_bf(float a, float b) {
  bf16x2 v = {(__bf16)a, (__bf16)b};
  return __builtin_bit_cast(unsigned, v);
}
__device__ __forceinline__ float rbf(float a) { return (float)(__bf16)a; }
__device__ __forceinline__ float dot2(unsigned a, unsigned b, float c) {
  return __builtin_amdgcn_fdot2_f32_bf16(__builtin_bit_cast(bf16x2, a), __builtin_bit_cast(bf16x2, b), c, false);
}
__device__ __forceinline__ u32x2 tr_read(const char* p) {
  s16x4 v = __builtin_amdgcn_ds_read_tr16_b64_v4i16((LDS_AS s16x4*)p);
  return __builtin_bit_cast(u32x2, v);
}
__device__ __forceinline__ u32x4 wfrag(const char* p) {
  u32x2 a = tr_read(p), b = tr_read(p + 16 * 128);
  u32x4 r = {a[0], a[1], b[0], b[1]};
  return r;
}
__device__ __forceinline__ f32x4 mfma(u32x4 a, u32x4 b, f32x4 c) {
  return __builtin_amdgcn_mfma_f32_16x16x32_bf16(__builtin_bit_cast(bf16x8, a), __builtin_bit_cast(bf16x8, b), c, 0, 0, 0);
}
__device__ __forceinline__ int rho_inv(int r) { return (r & ~31) | ((r & 12) << 1) | ((r & 16) >> 2) | (r & 3); }
__device__ __forceinline__ int tr_lane_off(int l, int nb) {
  int g = l >> 4, q = (l & 15) >> 2, p = l & 3;
  int swz = 2 * (g & 1) + (q >> 1);
  return (4 * g + q) * 128 + ((nb ^ swz) << 5) + p * 8;
}
__device__ __forceinline__ void ln_params(float sum, float sq, float& mb, float& rstd) {
  float mean = sum * (1.0f / D_);
  float var = fmaxf(sq * (1.0f / D_) - mean * mean, 0.0f);
  mb = rbf(mean);
  float den = rbf(sqrtf(rbf(rbf(var) + 1e-5f)));
  rstd = 1.0f / den;
}
__device__ __forceinline__ u32x4 add_bf8(u32x4 a, u32x4 b) {
  u32x4 r;
#pragma unroll
  for (int e = 0; e < 4; ++e) r[e] = pack_bf(bf_lo(a[e]) + bf_lo(b[e]), bf_hi(a[e]) + bf_hi(b[e]));
  return r;
}

constexpr int P1_WG = 64 * 128;
constexpr int P1_XO = 3 * P1_WG;
constexpr int P1_SLOT = 3 * P1_WG + 80 * 128;
constexpr int P1_R = 4;
constexpr int P1_SP = 672;
constexpr int P1_VO = 28 * P1_SP;
constexpr int P1_VP = 72;
constexpr int P1_SMEM = P1_R * P1_SLOT + 2048 + (80 * 2 + 3 * 64) * 4;

__global__ __launch_bounds__(768, 3) void k_qkv_attn(const u16* __restrict__ x, const u16* __restrict__ wqkv,
                                                     u16* __restrict__ o_frag, const u16* __restrict__ wo,
                                                     const u16* __restrict__ wg, const u16* __restrict__ wu,
                                                     const u16* __restrict__ wd, unsigned* __restrict__ cnt) {
  __shared__ __attribute__((aligned(16))) char smem[P1_SMEM];
  float* sSum = (float*)(smem + P1_R * P1_SLOT + 2048);
  float* sSq = sSum + 80;
  float* sCs = sSq + 80;

  const int tid = threadIdx.x, l = tid & 63, g = l >> 4, tl = l & 15;
  const int w = __builtin_amdgcn_readfirstlane(tid >> 6);
  const int bid = blockIdx.x, xcd = bid & 7, jj = bid >> 3;
  const int mt = xcd * 4 + (jj >> 3), hp = jj & 7;
  const int row0 = mt * 64 - 16;
  if (bid == 0 && tid < 16) cnt[tid * 32] = 0u;

  if (w >= 8) {
    const int lw = w - 8, lr = l >> 3, lc = l & 7;
    const u16* src[9];
    int dst[9], adv[9];
#pragma unroll
    for (int j = 0; j < 9; ++j) {
      const int pi = lw + 4 * j;
      if (pi < 24) {
        const int m = pi >> 3, rho = (pi & 7) * 8 + lr;
        const int c = lc ^ (((rho >> 1) & 3) << 1);
        if (c < 7) { src[j] = wqkv + (size_t)rho_inv(rho) * QKV_ + m * D_ + hp * 56 + c * 8; adv[j] = 64 * QKV_; }
        else { src[j] = (const u16*)(g_const + (m == 1 ? 4 : 8)); adv[j] = 0; }
        dst[j] = pi * 1024;
      } else if (pi < 34) {
        const int r = (pi - 24) * 8 + lr;
        const int c = lc ^ ((r >> 1) & 7);
        if (r > 0) { src[j] = x + (size_t)max(row0 + r, 0) * D_ + c * 8; adv[j] = 64; }
        else { src[j] = (const u16*)g_const; adv[j] = 0; }
        dst[j] = pi * 1024;
      } else { src[j] = (const u16*)g_const; adv[j] = 0; dst[j] = -1; }
    }
#define P1_ISSUE(ST) do { \
      char* sl_ = smem + ((ST) % P1_R) * P1_SLOT; \
      _Pragma("unroll") for (int j = 0; j < 9; ++j) { \
        GLDS(src[j] + (size_t)(ST) * adv[j], (dst[j] >= 0) ? sl_ + dst[j] : smem + P1_R * P1_SLOT + (lw & 1) * 1024); } \
    } while (0)
    P1_ISSUE(0);
    {
      char* dummy = smem + P1_R * P1_SLOT + (lw & 1) * 1024;
      const int pc = (bid * 4 + lw);
      GLDS(wqkv + ((size_t)pc * 64 + l) * 8, dummy);
      GLDS(wqkv + ((size_t)(pc < 152 ? pc + 1024 : pc) * 64 + l) * 8, dummy);
      GLDS(x + ((size_t)pc * 64 + l) * 8, dummy);
      GLDS(x + ((size_t)(pc < 768 ? pc + 1024 : pc) * 64 + l) * 8, dummy);
    }
    P1_ISSUE(1); P1_ISSUE(2);
    WAITV(22); BAR();
    P1_ISSUE(3);
    WAITV(18); BAR();
    P1_ISSUE(4);
    WAITV(18); BAR();
    P1_ISSUE(5);
    WAITV(18); BAR();
    P1_ISSUE(6);
    WAITV(18); BAR();
    WAITV(9);  BAR();
    WAITV(0);  BAR();
    {
      char* dummy = smem + P1_R * P1_SLOT + (lw & 1) * 1024;
      const int pc = (bid * 4 + lw);
      const size_t e0 = ((size_t)pc * 64 + l) * 8;
      GLDS(wg + e0, dummy); GLDS(wu + e0, dummy); GLDS(wd + e0, dummy);
      if (pc < 544) { GLDS(wg + e0 + 1024 * 512, dummy); GLDS(wu + e0 + 1024 * 512, dummy); GLDS(wd + e0 + 1024 * 512, dummy); }
      if (pc < 392) GLDS(wo + e0, dummy);
    }
    BAR(); BAR();
    WAITV(0);
    return;
  }

  const int fb = w & 3, hf = w >> 2;
  int xo[3][2];
#pragma unroll
  for (int t = 0; t < 3; ++t) {
    const int tb = (t < 2) ? hf * 3 + t : (hf ? fb : 2);
    const int r = tb * 16 + tl;
#pragma unroll
    for (int s2 = 0; s2 < 2; ++s2) xo[t][s2] = P1_XO + r * 128 + (((4 * s2 + g) ^ ((r >> 1) & 7)) << 4);
  }
  f32x4 acc[3][3];
#pragma unroll
  for (int m = 0; m < 3; ++m)
#pragma unroll
    for (int t = 0; t < 3; ++t) acc[m][t] = (f32x4){0.f, 0.f, 0.f, 0.f};
  const int troff = tr_lane_off(l, fb);

  u32x4 xf[2][3], wf[2][3];
#define P1_LOAD(B, S) do { \
    const char* slot_ = smem + (((S) >> 1) % P1_R) * P1_SLOT; \
    _Pragma("unroll") for (int m = 0; m < 3; ++m) wf[B][m] = wfrag(slot_ + m * P1_WG + ((S) & 1) * 32 * 128 + troff); \
    _Pragma("unroll") for (int t = 0; t < 3; ++t) xf[B][t] = *(const u32x4*)(slot_ + xo[t][(S) & 1]); \
  } while (0)
#pragma unroll
  for (int ii = 0; ii < 7; ++ii) {
    RBAR();
        P1_LOAD(0, ii * 2);
#pragma unroll
    for (int s2 = 0; s2 < 2; ++s2) {
      const int s = ii * 2 + s2;
      const char* sn = smem + (ii % P1_R) * P1_SLOT;
      SB();
#pragma unroll
      for (int m = 0; m < 3; ++m) {
        if (s2 == 0) {
          wf[1][m] = wfrag(sn + m * P1_WG + 32 * 128 + troff);
          xf[1][m] = *(const u32x4*)(sn + xo[m][1]);
          SB();
        }
        acc[m][0] = mfma(wf[s & 1][m], xf[s & 1][0], acc[m][0]);
        acc[m][1] = mfma(wf[s & 1][m], xf[s & 1][1], acc[m][1]);
        if (hf == 0) acc[m][2] = mfma(wf[s & 1][m], xf[s & 1][2], acc[m][2]);
        else if (m == 0) acc[0][2] = mfma(xf[s & 1][2], xf[s & 1][2], acc[0][2]);
        else if (m == 1) acc[1][2] = mfma(xf[s & 1][1], xf[s & 1][1], acc[1][2]);
        SB();
      }
    }
  }
  if (fb == 3 && g == 2) {
#pragma unroll
    for (int t = 0; t < 3; ++t)
      if (hf == 0 || t < 2) sSum[(hf * 3 + t) * 16 + tl] = acc[1][t][0];
  }
  if (hf == 1 && g == (tl >> 2)) {
    float a0 = acc[0][2][0], a1 = acc[1][2][0];
#pragma unroll
    for (int r = 1; r < 4; ++r) { if ((tl & 3) == r) { a0 = acc[0][2][r]; a1 = acc[1][2][r]; } }
    sSq[fb * 16 + tl] = a0;
    if (fb == 0) sSq[64 + tl] = a1;
  }
  if (hf == 0 && tl == 0) {
#pragma unroll
    for (int m = 0; m < 3; ++m) *(f32x4*)(sCs + m * 64 + fb * 16 + 4 * g) = acc[m][0];
  }
  BAR();
  char* sS = smem;
  {
    f32x4 csf[3];
#pragma unroll
    for (int m = 0; m < 3; ++m) csf[m] = *(const f32x4*)(sCs + m * 64 + fb * 16 + 4 * g);
    const int quad = 4 * fb + g;
#pragma unroll
    for (int t = 0; t < 3; ++t) {
      if (hf == 0 || t < 2) {
        const int tb = hf * 3 + t;
        float mb, rstd;
        ln_params(sSum[tb * 16 + tl], sSq[tb * 16 + tl], mb, rstd);
#pragma unroll
        for (int m = 0; m < 3; ++m) {
          float v0 = (acc[m][t][0] - mb * csf[m][0]) * rstd;
          float v1 = (acc[m][t][1] - mb * csf[m][1]) * rstd;
          float v2 = (acc[m][t][2] - mb * csf[m][2]) * rstd;
          float v3 = (acc[m][t][3] - mb * csf[m][3]) * rstd;
          u32x2 pk = {pack_bf(v0, v1), pack_bf(v2, v3)};
          if (quad < 14) {
            if (m < 2) *(u32x2*)(sS + (m * 14 + quad) * P1_SP + (tb * 16 + tl) * 8) = pk;
            else *(u32x2*)(sS + P1_VO + ((quad >= 7 ? 80 : 0) + tb * 16 + tl) * P1_VP + (quad >= 7 ? quad - 7 : quad) * 8) = pk;
          }
        }
      }
    }
  }
  BAR();

  {
    const int hh = w >> 2, tq = (w & 3) + 1;
    const int qa = hh * 7 + 2 * g;
    const bool up = g < 3;
    const int qb = up ? qa + 1 : qa;
    u32x4 qf, kf[2];
    {
      u32x2 a = *(const u32x2*)(sS + (0 * 14 + qa) * P1_SP + (tq * 16 + tl) * 8);
      u32x2 b = *(const u32x2*)(sS + (0 * 14 + qb) * P1_SP + (tq * 16 + tl) * 8);
      qf = (u32x4){a[0], a[1], up ? b[0] : 0u, up ? b[1] : 0u};
#pragma unroll
      for (int c = 0; c < 2; ++c) {
        u32x2 ka = *(const u32x2*)(sS + (1 * 14 + qa) * P1_SP + ((tq - 1 + c) * 16 + tl) * 8);
        u32x2 kb = *(const u32x2*)(sS + (1 * 14 + qb) * P1_SP + ((tq - 1 + c) * 16 + tl) * 8);
        kf[c] = (u32x4){ka[0], ka[1], up ? kb[0] : 0u, up ? kb[1] : 0u};
      }
    }
    u32x4 vf[2];
    {
      const char* vb = sS + P1_VO + (hh * 80 + (tq - 1) * 16 + 4 * g + ((l & 15) >> 2)) * P1_VP + (l & 3) * 8;
#pragma unroll
      for (int db = 0; db < 2; ++db) {
        u32x2 a = tr_read(vb + db * 32), b = tr_read(vb + 16 * P1_VP + db * 32);
        vf[db] = (u32x4){a[0], a[1], b[0], b[1]};
      }
    }
    const f32x4 z4 = {0.f, 0.f, 0.f, 0.f};
    f32x4 sc[2];
    sc[0] = mfma(kf[0], qf, z4);
    sc[1] = mfma(kf[1], qf, z4);
    const int tok = mt * 64 + (tq - 1) * 16 + tl;
    const float scale = 0.1889822365046136f;
    float lg[8];
    float mx = -3.0e38f;
#pragma unroll
    for (int c = 0; c < 2; ++c)
#pragma unroll
      for (int r = 0; r < 4; ++r) {
        const int delta = tl - (4 * g + r) + 16 * (1 - c);
        const bool inwin = (delta >= 0) && (delta <= 14) && ((delta & 1) == 0);
        float v = rbf(rbf(sc[c][r]) * scale);
        v = (tok - delta >= 0) ? v : -1e9f;
        v = inwin ? v : -3.0e38f;
        lg[c * 4 + r] = v;
        mx = fmaxf(mx, v);
      }
    mx = fmaxf(mx, __shfl_xor(mx, 16));
    mx = fmaxf(mx, __shfl_xor(mx, 32));
    float den = 0.f;
#pragma unroll
    for (int i = 0; i < 8; ++i) { lg[i] = (lg[i] > -2.0e38f) ? __expf(lg[i] - mx) : 0.f; den += lg[i]; }
    den += __shfl_xor(den, 16);
    den += __shfl_xor(den, 32);
    const float inv = 1.0f / den;
    u32x4 pf = {pack_bf(lg[0] * inv, lg[1] * inv), pack_bf(lg[2] * inv, lg[3] * inv),
                pack_bf(lg[4] * inv, lg[5] * inv), pack_bf(lg[6] * inv, lg[7] * inv)};
    const int tbg = tok >> 4;
#pragma unroll
    for (int db = 0; db < 2; ++db) {
      f32x4 o = mfma(vf[db], pf, z4);
      const int qd = db * 4 + g;
      if (qd < 7) {
        int f = hp * 56 + (hh * 7 + qd) * 4;
        int s = f >> 5, ii = (f >> 4) & 1, gg = (f >> 2) & 3;
        u32x2 pk = {pack_bf(o[0], o[1]), pack_bf(o[2], o[3])};
        *(u32x2*)((char*)o_frag + ((size_t)((tbg * 14 + s) * 64 + gg * 16 + tl)) * 16 + ii * 8) = pk;
      }
    }
  }
}

constexpr int EP = 144;
constexpr int G_SLOT = 16384;
constexpr int P3_SLOT = 24576;
constexpr int P3_R = 6;
constexpr int K2_RING = 147456;
constexpr int K2_T = K2_RING;
constexpr int K2_CS = K2_T + 64 * EP;
constexpr int K2_LN = K2_CS + 512;
constexpr int K2_SMEM = K2_LN + 2048;
#define WAITVI(n) asm volatile("s_waitcnt vmcnt(%0)" :: "i"(n) : "memory")

template <int NST, int R, int PW>
__device__ __forceinline__ void g_loader_run(char* smem, const u16* ws0, const u16* ws1, const u16* xs, int lw) {
#define G_ISSUE(ST) do { \
      char* sl_ = smem + ((ST) % R) * G_SLOT; \
      if ((ST) >= PW) { GLDS(ws0 + (size_t)(ST) * 64 * D_, sl_ + lw * 1024); GLDS(ws1 + (size_t)(ST) * 64 * D_, sl_ + (lw + 4) * 1024); } \
      GLDS(xs + (size_t)(ST) * 1024, sl_ + 8192 + lw * 1024); \
      GLDS(xs + (size_t)(ST) * 1024 + 512, sl_ + 8192 + (4 + lw) * 1024); \
    } while (0)
  constexpr int PRE = 4;
#pragma unroll
  for (int st = 0; st < PRE; ++st) G_ISSUE(st);
#pragma unroll
  for (int i = 0; i < NST; ++i) {
    const int lim0 = (i == 0) ? PRE - 1 : ((PRE + 2 * i - 1 < i + R - 2) ? PRE + 2 * i - 1 : i + R - 2);
    const int issued = (lim0 < NST - 1) ? lim0 : NST - 1;
    const int need = (i + 1 < NST) ? i + 1 : NST - 1;
    int allow = 0;
#pragma unroll
    for (int st = need + 1; st <= issued; ++st) allow += (st < PW) ? 2 : 4;
    WAITVI(allow);
    BAR();
    const int lim1 = (PRE + 2 * i + 1 < i + R - 1) ? PRE + 2 * i + 1 : i + R - 1;
#pragma unroll
    for (int st = issued + 1; st <= lim1; ++st) if (st < NST) G_ISSUE(st);
  }
}

template <int NST, int R>
__device__ __forceinline__ void g_consumer_loop(const char* smem, int l, int tb, int fh, f32x4 (&acc)[2]) {
  constexpr int KS = NST * 2;
  const int tro0 = tr_lane_off(l, fh * 2), tro1 = tr_lane_off(l, fh * 2 + 1);
  const int xoff = 8192 + tb * 1024 + l * 16;
  u32x4 wf[2][2], xf[2];
#define G_LOAD(K) do { \
    const char* sl_ = smem + (((K) >> 1) % R) * G_SLOT; \
    wf[(K) & 1][0] = wfrag(sl_ + ((K) & 1) * 4096 + tro0); \
    wf[(K) & 1][1] = wfrag(sl_ + ((K) & 1) * 4096 + tro1); \
    xf[(K) & 1] = *(const u32x4*)(sl_ + ((K) & 1) * 4096 + xoff); \
  } while (0)
#pragma unroll
  for (int i = 0; i < NST; ++i) {
    RBAR();
    if (i == 0) G_LOAD(0);
#pragma unroll
    for (int j = 0; j < 2; ++j) {
      const int k = i * 2 + j;
      if (k + 1 < KS) G_LOAD(k + 1);
      SB();
      acc[0] = mfma(wf[k & 1][0], xf[k & 1], acc[0]);
      acc[1] = mfma(wf[k & 1][1], xf[k & 1], acc[1]);
      SB();
    }
  }
}

__device__ __forceinline__ void group_sync(unsigned* c, int l) {
  if (l == 0) {
    __hip_atomic_fetch_add(c, 1u, __ATOMIC_RELAXED, __HIP_MEMORY_SCOPE_AGENT);
    for (int spin = 0; spin < (1 << 22); ++spin) {
      if (__hip_atomic_load(c, __ATOMIC_RELAXED, __HIP_MEMORY_SCOPE_AGENT) >= 28u) break;
      __builtin_amdgcn_s_sleep(1);
    }
  }
}

__global__ __launch_bounds__(768, 3) void k_fused(const u16* __restrict__ o_frag, const u16* __restrict__ wo,
                                                  const u16* __restrict__ x, const u16* __restrict__ wg,
                                                  const u16* __restrict__ wu, const u16* __restrict__ wd,
                                                  u16* h_frag, float* stats, u16* a_frag, u16* __restrict__ out,
                                                  unsigned* cnt) {
  __shared__ __attribute__((aligned(16))) char smem[K2_SMEM];
  char* sT = smem + K2_T;
  float* sCs = (float*)(smem + K2_CS);
  float2* sLN = (float2*)(smem + K2_LN);
  const int tid = threadIdx.x, l = tid & 63, g = l >> 4, tl = l & 15;
  const int w = __builtin_amdgcn_readfirstlane(tid >> 6);
  const int bid = blockIdx.x, grp = bid & 7, jj = bid >> 3;
  const int mt = grp * 4 + jj / 7, nt = jj % 7;
  const int n3 = jj;

  if (w >= 8) {
    const int lw = w - 8, lr = l >> 3, lc = l & 7;
    int woff[2];
#pragma unroll
    for (int j = 0; j < 2; ++j) {
      const int rho = (lw + 4 * j) * 8 + lr;
      woff[j] = rho * D_ + nt * 64 + (lc ^ (((rho >> 1) & 3) << 1)) * 8;
    }
    g_loader_run<7, 8, 0>(smem, wo + woff[0], wo + woff[1], o_frag + ((size_t)((mt * 4 + lw) * 14) * 64 + l) * 8, lw);
    BAR();
    const int rho3 = lw * 8 + lr;
    const int c3 = lc ^ (((rho3 >> 1) & 3) << 1);
    const u16* gs = wg + (size_t)rho_inv(rho3) * F_ + n3 * 64 + c3 * 8;
    const u16* us = wu + (size_t)rho_inv(rho3) * F_ + n3 * 64 + c3 * 8;
#pragma unroll
    for (int st = 0; st < 5; ++st) {
      GLDS(gs + (size_t)st * 32 * F_, smem + st * P3_SLOT + lw * 1024);
      GLDS(us + (size_t)st * 32 * F_, smem + st * P3_SLOT + 4096 + lw * 1024);
    }
    BAR();
    BAR();
    BAR();
    {
      const u16* xs = h_frag + ((size_t)((grp * 16 + lw * 4) * 14) * 64 + l) * 8;
#define P3_ISSUE(ST) do { \
        char* sl_ = smem + ((ST) % P3_R) * P3_SLOT; \
        if ((ST) >= 5) { GLDS(gs + (size_t)(ST) * 32 * F_, sl_ + lw * 1024); GLDS(us + (size_t)(ST) * 32 * F_, sl_ + 4096 + lw * 1024); } \
        _Pragma("unroll") for (int j = 0; j < 4; ++j) GLDS(xs + (size_t)(j * 14 + (ST)) * 512, sl_ + 8192 + (lw * 4 + j) * 1024); \
      } while (0)
      P3_ISSUE(0); P3_ISSUE(1); P3_ISSUE(2);
#pragma unroll
      for (int i = 0; i < 14; ++i) {
        const int issued = (i == 0) ? 2 : ((i == 1) ? 4 : ((i + P3_R - 2 < 13) ? i + P3_R - 2 : 13));
        const int need = (i + 1 < 14) ? i + 1 : 13;
        int allow = 0;
#pragma unroll
        for (int st = need + 1; st <= issued; ++st) allow += (st < 5) ? 4 : 6;
        WAITVI(allow);
        BAR();
        if (i == 0) { P3_ISSUE(3); P3_ISSUE(4); }
        else if (i == 1) { P3_ISSUE(5); P3_ISSUE(6); }
        else if (i + P3_R - 1 < 14) P3_ISSUE(i + P3_R - 1);
        if (i == 12) {
          float a = 0.f, b = 0.f;
#pragma unroll
          for (int n = 0; n < 7; ++n) {
            const float2 s0 = *(const float2*)(stats + ((size_t)n * S_ + grp * 256 + lw * 64 + l) * 2);
            a += s0.x; b += s0.y;
          }
          float mbv, rsv;
          ln_params(a, b, mbv, rsv);
          sLN[lw * 64 + l] = make_float2(mbv, rsv);
        }
      }
    }
    BAR();
#pragma unroll
    for (int st = 0; st < 8; ++st) {
      GLDS(wd + woff[0] + (size_t)st * 64 * D_, smem + st * G_SLOT + lw * 1024);
      GLDS(wd + woff[1] + (size_t)st * 64 * D_, smem + st * G_SLOT + (lw + 4) * 1024);
    }
    BAR();
    BAR();
    g_loader_run<28, 9, 8>(smem, wd + woff[0], wd + woff[1], a_frag + ((size_t)((mt * 4 + lw) * 56) * 64 + l) * 8, lw);
    BAR();
    return;
  }

  const int tb = w & 3, fh = w >> 2;
  const size_t rowoff = (size_t)(mt * 64 + tb * 16 + fh * 8 + (l >> 3)) * D_ + nt * 64 + (l & 7) * 8;
  char* tw = sT + tb * 16 * EP;
  u32x4 hv;
  {
    const u32x4 rv = *(const u32x4*)(x + rowoff);
    f32x4 acc[2] = {(f32x4){0.f, 0.f, 0.f, 0.f}, (f32x4){0.f, 0.f, 0.f, 0.f}};
    g_consumer_loop<7, 8>(smem, l, tb, fh, acc);
#pragma unroll
    for (int f = 0; f < 2; ++f) {
      u32x2 pk = {pack_bf(acc[f][0], acc[f][1]), pack_bf(acc[f][2], acc[f][3])};
      *(u32x2*)(tw + tl * EP + (fh * 2 + f) * 32 + g * 8) = pk;
    }
    BAR();
    char* p = tw + (fh * 8 + (l >> 3)) * EP + (l & 7) * 16;
    hv = add_bf8(rv, *(const u32x4*)p);
    *(u32x4*)p = hv;
    float sm = 0.f, sq = 0.f;
#pragma unroll
    for (int e = 0; e < 4; ++e) { sm = dot2(hv[e], ONES, sm); sq = dot2(hv[e], hv[e], sq); }
    sm += __shfl_xor(sm, 1); sq += __shfl_xor(sq, 1);
    sm += __shfl_xor(sm, 2); sq += __shfl_xor(sq, 2);
    sm += __shfl_xor(sm, 4); sq += __shfl_xor(sq, 4);
    if ((l & 7) == 0) {
      unsigned long long sv = ((unsigned long long)__float_as_uint(sq) << 32) | __float_as_uint(sm);
      __hip_atomic_store((unsigned long long*)(stats + ((size_t)nt * S_ + mt * 64 + tb * 16 + fh * 8 + (l >> 3)) * 2), sv,
                         __ATOMIC_RELAXED, __HIP_MEMORY_SCOPE_AGENT);
    }
    BAR();
    u32x4 v = *(const u32x4*)(tw + tl * EP + (fh * 4 + g) * 16);
    st16_wt((u32x4*)h_frag + (size_t)((mt * 4 + tb) * 14 + nt * 2 + fh) * 64 + l, v);
    WAITV(0);
    BAR();
    if (w == 0) group_sync(cnt + grp * 32, l);
    BAR();
  }
  {
    const int tg = w >> 1, f3 = w & 1;
    f32x4 acc[4][4];
#pragma unroll
    for (int t = 0; t < 4; ++t)
#pragma unroll
      for (int f = 0; f < 4; ++f) acc[t][f] = (f32x4){0.f, 0.f, 0.f, 0.f};
    const int tro0 = tr_lane_off(l, f3 * 2), tro1 = tr_lane_off(l, f3 * 2 + 1);
    const int csoff = (w >> 2) * 4096 + tr_lane_off(l, w & 3);
    const int xoff = 8192 + tg * 4096 + l * 16;
    f32x4 csum = {0.f, 0.f, 0.f, 0.f};
    const u32x4 onesf = {ONES, ONES, ONES, ONES};
    u32x4 wf[2][4], wcs[2], xf[4];
#pragma unroll
    for (int s = 0; s < 14; ++s) {
      RBAR();
      if (s == 0) {
        wf[0][0] = wfrag(smem + tro0); wf[0][1] = wfrag(smem + tro1);
        wf[0][2] = wfrag(smem + 4096 + tro0); wf[0][3] = wfrag(smem + 4096 + tro1);
        wcs[0] = wfrag(smem + csoff);
#pragma unroll
        for (int t = 0; t < 4; ++t) xf[t] = *(const u32x4*)(smem + xoff + t * 1024);
      }
      int sx_ = ((s + 1) % P3_R) * P3_SLOT; asm volatile("" : "+s"(sx_));
      const char* sn = smem + sx_;
      SB();
#pragma unroll
      for (int t = 0; t < 4; ++t) {
#pragma unroll
        for (int f = 0; f < 4; ++f) acc[t][f] = mfma(wf[s & 1][f], xf[t], acc[t][f]);
        SB();
        if (s + 1 < 14) {
          xf[t] = *(const u32x4*)(sn + xoff + t * 1024);
          if (t == 0) { wf[(s + 1) & 1][0] = wfrag(sn + tro0); wf[(s + 1) & 1][1] = wfrag(sn + tro1); }
          if (t == 1) { wf[(s + 1) & 1][2] = wfrag(sn + 4096 + tro0); wf[(s + 1) & 1][3] = wfrag(sn + 4096 + tro1); }
          if (t == 2) wcs[(s + 1) & 1] = wfrag(sn + csoff);
        }
        SB();
      }
      csum = mfma(wcs[s & 1], onesf, csum);
      SB();
    }
    if (tl == 0) *(f32x4*)(sCs + w * 16 + 4 * g) = csum;
    BAR();
    float mb[4], rstd[4];
#pragma unroll
    for (int t = 0; t < 4; ++t) { float2 v = sLN[(tg * 4 + t) * 16 + tl]; mb[t] = v.x; rstd[t] = v.y; }
    f32x4 cg[2], cu[2];
#pragma unroll
    for (int i = 0; i < 2; ++i) {
      cg[i] = *(const f32x4*)(sCs + (f3 * 2 + i) * 16 + 4 * g);
      cu[i] = *(const f32x4*)(sCs + (4 + f3 * 2 + i) * 16 + 4 * g);
    }
#pragma unroll
    for (int t = 0; t < 4; ++t) {
      u32x4 o4;
#pragma unroll
      for (int i = 0; i < 2; ++i) {
        float av[4];
#pragma unroll
        for (int r = 0; r < 4; ++r) {
          float gv = (acc[t][i][r] - mb[t] * cg[i][r]) * rstd[t];
          float uv = (acc[t][2 + i][r] - mb[t] * cu[i][r]) * rstd[t];
          float sl = gv * __builtin_amdgcn_rcpf(1.0f + __expf(-gv));
          av[r] = sl * uv;
        }
        o4[i * 2] = pack_bf(av[0], av[1]);
        o4[i * 2 + 1] = pack_bf(av[2], av[3]);
      }
      const int tbg = grp * 16 + tg * 4 + t;
      st16_wt((u32x4*)a_frag + (size_t)(tbg * 56 + n3 * 2 + f3) * 64 + l, o4);
    }
    WAITV(0);
    BAR();
    if (w == 0) group_sync(cnt + (8 + grp) * 32, l);
    BAR();
  }
  {
    f32x4 acc[2] = {(f32x4){0.f, 0.f, 0.f, 0.f}, (f32x4){0.f, 0.f, 0.f, 0.f}};
    g_consumer_loop<28, 9>(smem, l, tb, fh, acc);
#pragma unroll
    for (int f = 0; f < 2; ++f) {
      u32x2 pk = {pack_bf(acc[f][0], acc[f][1]), pack_bf(acc[f][2], acc[f][3])};
      *(u32x2*)(tw + tl * EP + (fh * 2 + f) * 32 + g * 8) = pk;
    }
    BAR();
    const char* p = tw + (fh * 8 + (l >> 3)) * EP + (l & 7) * 16;
    *(u32x4*)(out + rowoff) = add_bf8(hv, *(const u32x4*)p);
  }
}

extern "C" void kernel_launch(void* const* d_in, const int* in_sizes, int n_in, void* d_out, int out_size,
                              void* d_ws, size_t ws_size, hipStream_t stream) {
  const u16* x = (const u16*)d_in[0];
  const u16* wqkv = (const u16*)d_in[1];
  const u16* wo = (const u16*)d_in[2];
  const u16* wg = (const u16*)d_in[4];
  const u16* wu = (const u16*)d_in[5];
  const u16* wd = (const u16*)d_in[6];
  u16* out = (u16*)d_out;
  char* ws = (char*)d_ws;
  u16* o_frag = (u16*)ws;
  u16* h_frag = (u16*)(ws + (4u << 20));
  u16* a_frag = (u16*)(ws + (6u << 20));
  float* stats = (float*)(ws + (14u << 20));
  unsigned* cnt = (unsigned*)(ws + (15u << 20));
  k_qkv_attn<<<256, 768, 0, stream>>>(x, wqkv, o_frag, wo, wg, wu, wd, cnt);
  k_fused<<<224, 768, 0, stream>>>(o_frag, wo, x, wg, wu, wd, h_frag, stats, a_frag, out, cnt);
}
```
